```python
import torch

def _softmax(x, axis=-1):
    return torch.softmax(x, dim=axis)
def _gelu(x):
    return torch.nn.functional.gelu(x, approximate="tanh")
def _silu(x):
    return torch.nn.functional.silu(x)
def _sigmoid(x):
    return torch.sigmoid(x)
def _layernorm(x, axis=-1, eps=1e-5):
    m = x.mean(dim=axis, keepdim=True)
    v = x.var(dim=axis, keepdim=True, unbiased=False)
    return (x - m) / torch.sqrt(v + eps)
def _scatter_add(tgt, idx, src, axis=0):
    return tgt.clone().index_add_(axis, idx, src)
def _segment_sum(src, seg, n):
    out = torch.zeros((n,) + src.shape[1:], dtype=src.dtype)
    return out.index_add_(0, seg, src)
def _pad(x, pw):
    flat = tuple(v for pair in reversed(pw) for v in pair)
    return torch.nn.functional.pad(x, flat)

kernel_name = 'gated_mix__ssm_conv_s5__glu_gated_reglu__prompt'

def setup_inputs():

    _w = (lambda a: (lambda t: t.to(torch.bfloat16) if t.is_floating_point() else t)(torch.as_tensor(a)))
    def _A_setup():
        B, S, D = 1, 2048, 1024
        P = D
        Lambda_re = -0.5 * (lambda _x: torch.exp(torch.as_tensor(_x)))((lambda *s: torch.randn(*s, dtype=torch.bfloat16) * 0.2)(P))
        Lambda_im = (lambda *s: torch.randn(*s, dtype=torch.bfloat16) * 0.4)(P) * 1.0
        B_re = (lambda *s: torch.randn(*s, dtype=torch.bfloat16) * 0.2)(P, D) * (D ** -0.5)
        B_im = (lambda *s: torch.randn(*s, dtype=torch.bfloat16) * 0.2)(P, D) * (D ** -0.5)
        C_re = (lambda *s: torch.randn(*s, dtype=torch.bfloat16) * 0.2)(D, P) * (P ** -0.5)
        C_im = (lambda *s: torch.randn(*s, dtype=torch.bfloat16) * 0.2)(D, P) * (P ** -0.5)
        log_dt = (lambda *s: torch.randn(*s, dtype=torch.bfloat16) * 0.2)(P) * 0.3 - 2.0
        Dskip = (lambda *s: torch.randn(*s, dtype=torch.bfloat16) * 0.05)(D)
        return {
            "x": _w((lambda *s: torch.randn(*s, dtype=torch.bfloat16) * 0.05)(B, S, D)),
            "Lambda_re": _w(Lambda_re),
            "Lambda_im": _w(Lambda_im),
            "B_re": _w(B_re),
            "B_im": _w(B_im),
            "C_re": _w(C_re),
            "C_im": _w(C_im),
            "log_dt": _w(log_dt),
            "Dskip": _w(Dskip),
        }
    _A_in = _A_setup()

    def _B_setup():
        B, S, D, F = 1, 2048, 1024, 3584
        return {
            "x": _w((lambda *s: torch.randn(*s, dtype=torch.bfloat16) * 0.05)(B, S, D)),
            "W_gate": _w((lambda *s: torch.randn(*s, dtype=torch.bfloat16) * 0.8)(D, F) * (2.0 / D) ** 0.5),
            "W_up": _w((lambda *s: torch.randn(*s, dtype=torch.bfloat16) * 0.8)(D, F) * (2.0 / D) ** 0.5),
            "W_down": _w((lambda *s: torch.randn(*s, dtype=torch.bfloat16) * 0.8)(F, D) * (1.0 / F) ** 0.5),
        }
    _B_in = _B_setup()

    return {
        "A_x": _A_in["x"],
        "A_Lambda_re": _A_in["Lambda_re"],
        "A_Lambda_im": _A_in["Lambda_im"],
        "A_B_re": _A_in["B_re"],
        "A_B_im": _A_in["B_im"],
        "A_C_re": _A_in["C_re"],
        "A_C_im": _A_in["C_im"],
        "A_log_dt": _A_in["log_dt"],
        "A_Dskip": _A_in["Dskip"],
        "B_x": _B_in["x"],
        "B_W_gate": _B_in["W_gate"],
        "B_W_up": _B_in["W_up"],
        "B_W_down": _B_in["W_down"],
        "gate_w": _w((lambda *s: torch.randn(*s, dtype=torch.bfloat16) * 0.05)(1024) * 16.0),
    }

def reference(A_x, A_Lambda_re, A_Lambda_im, A_B_re, A_B_im, A_C_re, A_C_im, A_log_dt, A_Dskip, B_x, B_W_gate, B_W_up, B_W_down, gate_w):
    def _A_ref(x, Lambda_re, Lambda_im, B_re, B_im, C_re, C_im, log_dt, Dskip):
        Bsz, L, H = x.shape
        Lam = Lambda_re + 1j * Lambda_im
        Bmat = B_re + 1j * B_im
        Cmat = C_re + 1j * C_im
        dt = torch.exp(log_dt)
        Lam_bar = torch.exp(dt * Lam)
        B_bar = ((Lam_bar - 1.0) / Lam)[:, None] * Bmat
        Bu = torch.einsum("bld,pd->blp", x + 0j, B_bar)
        h = torch.zeros_like(Bu[:, 0, :])
        hs = []
        for t in range(L):
            h = Lam_bar[None, :] * h + Bu[:, t, :]
            hs.append(h)
        hseq = torch.stack(hs, 1)
        y = 2.0 * torch.einsum("blp,dp->bld", hseq, Cmat).real + x * Dskip[None, None, :]
        return y

    def _B_ref(x, W_gate, W_up, W_down):
        gate = torch.einsum("bsd,df->bsf", x, W_gate)
        up = torch.einsum("bsd,df->bsf", x, W_up)
        gate = gate * (lambda x, dt: x.to(dt))(gate > 0, gate.dtype)
        h = gate * up
        return torch.einsum("bsf,fd->bsd", h, W_down)

    x = A_x
    g = _sigmoid((x * gate_w).sum(-1, keepdims=True))
    a = _A_ref(x, A_Lambda_re, A_Lambda_im, A_B_re, A_B_im, A_C_re, A_C_im, A_log_dt, A_Dskip)
    b = _B_ref(x, B_W_gate, B_W_up, B_W_down)
    return g * a + (1 - g) * b
```

```cpp
#include <hip/hip_runtime.h>
#include <hip/hip_bf16.h>
typedef unsigned short u16;
__device__ __forceinline__ float bf2f(u16 v) { return __uint_as_float(((unsigned)v) << 16); }
__device__ __forceinline__ u16 f2bf(float f) { __hip_bfloat16 b = __float2bfloat16(f); return *(u16*)&b; }
__device__ __forceinline__ float rbf(float f) { return bf2f(f2bf(f)); }
constexpr int L = 2048, D = 1024, P = 1024, F = 3584;

template <bool AF32, bool BKN>
__global__ void gemm_naive(const void* A, const u16* B, float* C, int M, int N, int K, float alpha, int accum) {
  __shared__ float sa[32][33], sb[32][33];
  int tx = threadIdx.x, ty = threadIdx.y;
  int m = blockIdx.y * 32 + ty, n = blockIdx.x * 32 + tx;
  float acc = 0.f;
  for (int k0 = 0; k0 < K; k0 += 32) {
    sa[ty][tx] = AF32 ? ((const float*)A)[(size_t)m * K + k0 + tx] : bf2f(((const u16*)A)[(size_t)m * K + k0 + tx]);
    if (BKN) sb[tx][ty] = bf2f(B[(size_t)(k0 + ty) * N + blockIdx.x * 32 + tx]);
    else sb[ty][tx] = bf2f(B[(size_t)(blockIdx.x * 32 + ty) * K + k0 + tx]);
    __syncthreads();
    for (int k = 0; k < 32; ++k) acc += sa[ty][k] * sb[tx][k];
    __syncthreads();
  }
  if (accum) C[(size_t)m * N + n] += alpha * acc; else C[(size_t)m * N + n] = alpha * acc;
}
__global__ void scan_naive(float* ur, float* ui, const u16* lre, const u16* lim, const u16* logdt) {
  int p = blockIdx.x * blockDim.x + threadIdx.x;
  float lr = bf2f(lre[p]), li = bf2f(lim[p]);
  float dt = rbf(expf(bf2f(logdt[p])));
  float e = expf(dt * lr), ar = e * cosf(dt * li), ai = e * sinf(dt * li);
  float nr = ar - 1.f, ni = ai, den = lr * lr + li * li;
  float cr = (nr * lr + ni * li) / den, ci = (ni * lr - nr * li) / den;
  float hr = 0.f, hi = 0.f;
  for (int t = 0; t < L; ++t) {
    float xr = ur[(size_t)t * P + p], xi = ui[(size_t)t * P + p];
    float br = cr * xr - ci * xi, bi = cr * xi + ci * xr;
    float nhr = ar * hr - ai * hi + br, nhi = ar * hi + ai * hr + bi;
    hr = nhr; hi = nhi;
    ur[(size_t)t * P + p] = hr; ui[(size_t)t * P + p] = hi;
  }
}
__global__ void glu_naive(float* g, const float* u, int n) {
  int i = blockIdx.x * blockDim.x + threadIdx.x;
  if (i < n) { float a = rbf(g[i]), b = rbf(u[i]); g[i] = rbf(a > 0.f ? a * b : 0.f); }
}
__global__ void final_naive(float* out, const float* y, const float* b, const u16* x, const u16* dskip, const u16* gw) {
  int t = blockIdx.x; __shared__ float red[256];
  float s = 0.f;
  for (int d = threadIdx.x; d < D; d += 256) s += rbf(bf2f(x[t * D + d]) * bf2f(gw[d]));
  red[threadIdx.x] = s; __syncthreads();
  for (int o = 128; o > 0; o >>= 1) { if (threadIdx.x < o) red[threadIdx.x] += red[threadIdx.x + o]; __syncthreads(); }
  float sv = rbf(red[0]); float g = rbf(1.f / (1.f + expf(-sv))); float omg = rbf(1.f - g);
  for (int d = threadIdx.x; d < D; d += 256) {
    float a = y[t * D + d] + rbf(bf2f(x[t * D + d]) * bf2f(dskip[d]));
    out[t * D + d] = g * a + rbf(omg * rbf(b[t * D + d]));
  }
}
extern "C" void kernel_launch(void* const* d_in, const int* in_sizes, int n_in, void* d_out, int out_size, void* d_ws, size_t ws_size, hipStream_t stream) {
  const u16* x = (const u16*)d_in[0]; const u16* lre = (const u16*)d_in[1]; const u16* lim = (const u16*)d_in[2];
  const u16* Bre = (const u16*)d_in[3]; const u16* Bim = (const u16*)d_in[4]; const u16* Cre = (const u16*)d_in[5]; const u16* Cim = (const u16*)d_in[6];
  const u16* logdt = (const u16*)d_in[7]; const u16* dskip = (const u16*)d_in[8];
  const u16* Wg = (const u16*)d_in[10]; const u16* Wu = (const u16*)d_in[11]; const u16* Wd = (const u16*)d_in[12]; const u16* gw = (const u16*)d_in[13];
  float* ws = (float*)d_ws;
  float* ur = ws; float* ui = ur + L * P; float* y = ui + L * P; float* b = y + L * D; float* G = b + L * D; float* U = G + (size_t)L * F;
  dim3 blk(32, 32);
  gemm_naive<false, false><<<dim3(P / 32, L / 32), blk, 0, stream>>>(x, Bre, ur, L, P, D, 1.f, 0);
  gemm_naive<false, false><<<dim3(P / 32, L / 32), blk, 0, stream>>>(x, Bim, ui, L, P, D, 1.f, 0);
  scan_naive<<<P / 64, 64, 0, stream>>>(ur, ui, lre, lim, logdt);
  gemm_naive<true, false><<<dim3(D / 32, L / 32), blk, 0, stream>>>(ur, Cre, y, L, D, P, 2.f, 0);
  gemm_naive<true, false><<<dim3(D / 32, L / 32), blk, 0, stream>>>(ui, Cim, y, L, D, P, -2.f, 1);
  gemm_naive<false, true><<<dim3(F / 32, L / 32), blk, 0, stream>>>(x, Wg, G, L, F, D, 1.f, 0);
  gemm_naive<false, true><<<dim3(F / 32, L / 32), blk, 0, stream>>>(x, Wu, U, L, F, D, 1.f, 0);
  glu_naive<<<(L * F + 255) / 256, 256, 0, stream>>>(G, U, L * F);
  gemm_naive<true, true><<<dim3(D / 32, L / 32), blk, 0, stream>>>(G, Wd, b, L, D, F, 1.f, 0);
  final_naive<<<L, 256, 0, stream>>>((float*)d_out, y, b, x, dskip, gw);
}
```

```cpp
#include <hip/hip_runtime.h>
#include <hip/hip_bf16.h>

typedef unsigned short u16;
typedef __bf16 bf16x8 __attribute__((ext_vector_type(8)));
typedef short s16x4 __attribute__((ext_vector_type(4)));
typedef short s16x8 __attribute__((ext_vector_type(8)));
typedef float f32x4 __attribute__((ext_vector_type(4)));
typedef float f32x2 __attribute__((ext_vector_type(2)));
typedef unsigned u32x4 __attribute__((ext_vector_type(4)));
typedef unsigned u32x2 __attribute__((ext_vector_type(2)));
#define LAS __attribute__((address_space(3)))
#define GAS __attribute__((address_space(1)))
typedef GAS unsigned gu32;
typedef GAS unsigned long long gu64;
#define RLX_AGENT __ATOMIC_RELAXED, __HIP_MEMORY_SCOPE_AGENT

constexpr int SEQ = 2048, DM = 1024, NP = 1024, FF = 3584;
constexpr int KSTEPS2 = (2 * NP + FF) / 64;

constexpr size_t WS_CARRY = 0;
constexpr size_t WS_CARRY_BYTES = 8 * 1024 * 8;
constexpr size_t WS_FLAG2 = 65536;
constexpr size_t WS_G = 131072;
constexpr size_t WS_A2 = 1 << 20;
constexpr size_t WS_A2_BYTES = (size_t)KSTEPS2 * 16 * 16384;
constexpr size_t WS_PART = 32 << 20;

__device__ __forceinline__ float bf2f(u16 v) { return __uint_as_float(((unsigned)v) << 16); }
__device__ __forceinline__ float rbf(float f) { return (float)(__bf16)f; }
__device__ __forceinline__ unsigned pk2(float a, float b) {
    typedef __bf16 bf16x2 __attribute__((ext_vector_type(2)));
    bf16x2 v; v.x = (__bf16)a; v.y = (__bf16)b; return __builtin_bit_cast(unsigned, v);
}

#define WAIT_V(n) asm volatile("s_waitcnt vmcnt(" #n ")" ::: "memory")
#define WAIT_L0() asm volatile("s_waitcnt lgkmcnt(0)" ::: "memory")
#define BAR() __builtin_amdgcn_s_barrier()
#define SCHED() __builtin_amdgcn_sched_barrier(0)

__device__ __forceinline__ void dma16(unsigned m0v, unsigned voff, __amdgpu_buffer_rsrc_t srd, unsigned soff) {
    asm volatile("s_mov_b32 m0, %0\n\ts_nop 0\n\tbuffer_load_dwordx4 %1, %2, %3 offen lds" ::"s"(m0v), "v"(voff), "s"(srd), "s"(soff) : "m0", "memory");
}
__device__ __forceinline__ bf16x8 lds_b128(LAS char* p) { return __builtin_bit_cast(bf16x8, *(LAS s16x8*)p); }
__device__ __forceinline__ bf16x8 lds_tr(LAS char* p0, LAS char* p1) {
    s16x4 a = __builtin_amdgcn_ds_read_tr16_b64_v4i16((LAS s16x4*)p0);
    s16x4 b = __builtin_amdgcn_ds_read_tr16_b64_v4i16((LAS s16x4*)p1);
    return __builtin_bit_cast(bf16x8, __builtin_shufflevector(a, b, 0, 1, 2, 3, 4, 5, 6, 7));
}
#define MFMA(a, b, c) __builtin_amdgcn_mfma_f32_16x16x32_bf16(a, b, c, 0, 0, 0)
__device__ __forceinline__ f32x2 cmul(f32x2 a, f32x2 b) { return (f32x2){a.x * b.x - a.y * b.y, a.x * b.y + a.y * b.x}; }
__device__ __forceinline__ f32x2 cfma(f32x2 a, f32x2 b, f32x2 c) { return (f32x2){a.x * b.x - a.y * b.y + c.x, a.x * b.y + a.y * b.x + c.y}; }

constexpr int K1_A = 0;
constexpr int K1_S = 65536;
constexpr int K1_W = 81920;
constexpr int K1_WBUF = 28704, K1_UOFF = 14352;
constexpr int K1_MISC = K1_W + 2 * K1_WBUF;
constexpr int K1_LDS = K1_MISC + 2048;

__global__ __launch_bounds__(512, 2) void k1_gemm_scan(const u16* __restrict__ x, const u16* __restrict__ Wg, const u16* __restrict__ Wu,
                                                       const u16* __restrict__ Bre, const u16* __restrict__ Bim, const u16* __restrict__ lre,
                                                       const u16* __restrict__ lim, const u16* __restrict__ logdt, const u16* __restrict__ gw,
                                                       char* __restrict__ ws) {
    extern __shared__ __attribute__((aligned(16))) char smem[];
    LAS char* lds = (LAS char*)smem;
    const unsigned ldsb = (unsigned)(size_t)lds;
    const int tid = threadIdx.x, lane = tid & 63;
    const int wid = __builtin_amdgcn_readfirstlane(tid >> 6);
    const int wr = wid & 3, wc = wid >> 2;
    const int bid = blockIdx.x, idx = bid >> 3, j = (bid & 7) * 4 + (idx & 3), pm = idx >> 2;
    const int fr = lane & 15, fq = lane >> 4;

    const __amdgpu_buffer_rsrc_t sX = __builtin_amdgcn_make_buffer_rsrc((void*)x, (short)0, SEQ * DM * 2, 0x00020000);
    const __amdgpu_buffer_rsrc_t sG = __builtin_amdgcn_make_buffer_rsrc((void*)Wg, (short)0, DM * FF * 2, 0x00020000);
    const __amdgpu_buffer_rsrc_t sU = __builtin_amdgcn_make_buffer_rsrc((void*)Wu, (short)0, DM * FF * 2, 0x00020000);
    const __amdgpu_buffer_rsrc_t sB = __builtin_amdgcn_make_buffer_rsrc((void*)(((wid >> 1) & 1) ? Bim : Bre), (short)0, NP * DM * 2, 0x00020000);

    const int l3 = lane >> 3;
    const unsigned voffX = (unsigned)(((l3 >> 2) * 16 + (l3 & 3)) * 2048 + (((lane & 7) ^ (l3 & 6)) * 16));
    const int ww = wid < 6 ? wid : 6;
    unsigned voffW;
    { const int gam = ww * 64 + lane, rho = gam / 14, c = gam - rho * 14, k = (rho & 0x13) | ((rho & 4) << 1) | ((rho & 8) >> 1);
      voffW = (unsigned)(k * (FF * 2) + c * 16); }
    const unsigned soX = (unsigned)((pm * 256 + (wid >> 1) * 64 + (wid & 1) * 8) * 2048);
    const unsigned soS = (unsigned)((32 * j + 8 * (wid >> 2) + 4 * (wid & 1)) * 2048);
    const unsigned soW = (unsigned)(j * 224);
    const unsigned mX = ldsb + K1_A + wid * 4096, mS = ldsb + K1_S + wid * 1024, mW = ldsb + K1_W + ww * 1024;

#define ST_X(t, b) do { _Pragma("unroll") for (int i = 0; i < 4; ++i) dma16(mX + (b) * 32768 + i * 1024, voffX, sX, soX + (i & 1) * 65536 + (i >> 1) * 8192 + (t) * 128); } while (0)
#define ST_S(t, b) dma16(mS + (b) * 8192, voffX, sB, soS + (t) * 128)
#define ST_G(t, b) do { dma16(mW + (b) * K1_WBUF, voffW, sG, soW + (t) * (64 * FF * 2)); dma16(mW + (b) * K1_WBUF + 7168, voffW, sG, soW + (t) * (64 * FF * 2) + 32 * FF * 2); } while (0)
#define ST_U(t, b) do { dma16(mW + (b) * K1_WBUF + K1_UOFF, voffW, sU, soW + (t) * (64 * FF * 2)); dma16(mW + (b) * K1_WBUF + K1_UOFF + 7168, voffW, sU, soW + (t) * (64 * FF * 2) + 32 * FF * 2); } while (0)

    asm volatile("s_nop 4");
    ST_X(0, 0); ST_S(0, 0); ST_G(0, 0); ST_U(0, 0);
    ST_X(1, 1); ST_S(1, 1); ST_G(1, 1);

    const int ch0 = (fq ^ (fr & 6)) * 16;
    LAS char* rA0 = lds + K1_A + wr * 8192 + fr * 128 + ch0;
    LAS char* rA1 = lds + K1_A + wr * 8192 + fr * 128 + (ch0 ^ 64);
    LAS char* rS0 = lds + K1_S + wc * 4096 + fr * 128 + ch0;
    LAS char* rS1 = lds + K1_S + wc * 4096 + fr * 128 + (ch0 ^ 64);
    const int trow = (16 * (fq >> 1) + 4 * (fq & 1) + ((lane & 15) >> 2)) * 224;
    LAS char* rW = lds + K1_W + trow + wc * 96 + (lane & 3) * 8;
    LAS char* rM = lds + K1_W + trow + 192 + wc * 16 + (lane & 1) * 8 + ((lane >> 1) & 1) * K1_UOFF;

    f32x4 acc[4][9];
#pragma unroll
    for (int m = 0; m < 4; ++m)
#pragma unroll
        for (int n = 0; n < 9; ++n) acc[m][n] = (f32x4){0.f, 0.f, 0.f, 0.f};
    bf16x8 Af[4][2], Bf[3][2];

#define RD_A(b) do { _Pragma("unroll") for (int m = 0; m < 4; ++m) { Af[m][0] = lds_b128(rA0 + (b) * 32768 + m * 2048); Af[m][1] = lds_b128(rA1 + (b) * 32768 + m * 2048); } } while (0)
#define RD_S(slot, ri, b) do { Bf[slot][0] = lds_b128(rS0 + (b) * 8192 + (ri) * 2048); Bf[slot][1] = lds_b128(rS1 + (b) * 8192 + (ri) * 2048); } while (0)
#define RD_W(slot, base, off) do { Bf[slot][0] = lds_tr((base) + (off), (base) + (off) + 1792); Bf[slot][1] = lds_tr((base) + (off) + 7168, (base) + (off) + 7168 + 1792); } while (0)
#define MMA3(n0, n1, n2) do { __builtin_amdgcn_s_setprio(1); \
        _Pragma("unroll") for (int kk = 0; kk < 2; ++kk) _Pragma("unroll") for (int m = 0; m < 4; ++m) { \
            acc[m][n0] = MFMA(Af[m][kk], Bf[0][kk], acc[m][n0]); acc[m][n1] = MFMA(Af[m][kk], Bf[1][kk], acc[m][n1]); acc[m][n2] = MFMA(Af[m][kk], Bf[2][kk], acc[m][n2]); } \
        __builtin_amdgcn_s_setprio(0); } while (0)
#define KSTEP(t, b, MODE) do { \
        RD_S(0, 0, b); RD_S(1, 1, b); RD_W(2, rW, (b) * K1_WBUF); RD_A(b); \
        if (MODE < 2) ST_U((t) + 1, (b) ^ 1); \
        if (MODE < 2) WAIT_V(9); else WAIT_V(0); \
        WAIT_L0(); BAR(); SCHED(); MMA3(7, 8, 0); SCHED(); BAR(); \
        RD_W(0, rW, (b) * K1_WBUF + 32); RD_W(1, rW, (b) * K1_WBUF + 64); RD_W(2, rM, (b) * K1_WBUF); \
        if (MODE == 0) ST_X((t) + 2, b); \
        WAIT_L0(); BAR(); SCHED(); MMA3(1, 2, 6); SCHED(); BAR(); \
        RD_W(0, rW, (b) * K1_WBUF + K1_UOFF); RD_W(1, rW, (b) * K1_WBUF + K1_UOFF + 32); RD_W(2, rW, (b) * K1_WBUF + K1_UOFF + 64); \
        if (MODE == 0) { ST_S((t) + 2, b); ST_G((t) + 2, b); WAIT_V(9); } \
        if (MODE == 1) WAIT_V(2); \
        WAIT_L0(); BAR(); SCHED(); MMA3(3, 4, 5); SCHED(); BAR(); \
    } while (0)

    WAIT_V(9); BAR();
    if (wc == 1) BAR();
    for (int t = 0; t < 14; t += 2) {
        KSTEP(t, 0, 0);
        KSTEP(t + 1, 1, 0);
    }
    KSTEP(14, 0, 1);
    KSTEP(15, 1, 2);
    if (wc == 0) BAR();

    const int ch = 32 * j + 8 * wc + 4 * (fr >> 3) + 16 * ((fr >> 2) & 1) + (fr & 3);
    const float lr = bf2f(lre[ch]), li = bf2f(lim[ch]), ldt = bf2f(logdt[ch]);
    const int grow = pm * 256 + j * 8 + wid;
    const u32x4 xa = *(const u32x4*)(x + (size_t)grow * DM + lane * 8), xb = *(const u32x4*)(x + (size_t)grow * DM + 512 + lane * 8);
    const u32x4 ga = *(const u32x4*)(gw + lane * 8), gb = *(const u32x4*)(gw + 512 + lane * 8);
    if (bid < 128 && tid == 0) *(unsigned*)(ws + WS_FLAG2 + bid * 128) = 0u;

    const float dt = rbf(expf(ldt));
    f32x2 a1, cf;
    { const float e = expf(dt * lr); float s, c; sincosf(dt * li, &s, &c); a1 = (f32x2){e * c, e * s};
      const float nr = a1.x - 1.f, ni = a1.y, den = lr * lr + li * li; cf = (f32x2){(nr * lr + ni * li) / den, (ni * lr - nr * li) / den}; }
    f32x2 h = {0.f, 0.f};
#pragma unroll
    for (int m = 0; m < 4; ++m)
#pragma unroll
        for (int q = 0; q < 4; ++q) {
            const f32x2 b = cmul(cf, (f32x2){acc[m][7][q], acc[m][8][q]});
            acc[m][7][q] = b.x; acc[m][8][q] = b.y; h = cfma(a1, h, b);
        }
    f32x2 a16 = cmul(a1, a1); a16 = cmul(a16, a16); a16 = cmul(a16, a16); a16 = cmul(a16, a16);
    const f32x2 a32 = cmul(a16, a16), a48 = cmul(a32, a16), a64 = cmul(a32, a32), a128 = cmul(a64, a64), a192 = cmul(a128, a64), a256 = cmul(a128, a128);
    f32x2 E[4];
#pragma unroll
    for (int f = 0; f < 4; ++f) E[f] = (f32x2){__shfl(h.x, fr + 16 * f), __shfl(h.y, fr + 16 * f)};
    const f32x2 P2 = cfma(a16, E[0], E[1]), P3 = cfma(a16, P2, E[2]), wag = cfma(a16, P3, E[3]);
    const f32x2 Pq = fq == 0 ? (f32x2){0.f, 0.f} : fq == 1 ? E[0] : fq == 2 ? P2 : P3;
    const f32x2 aq = fq == 0 ? (f32x2){1.f, 0.f} : fq == 1 ? a16 : fq == 2 ? a32 : a48;
    LAS f32x2* wtab = (LAS f32x2*)(lds + K1_MISC);
    if (fq == 0) wtab[wr * 32 + wc * 16 + fr] = wag;
    __syncthreads();
    const f32x2 W0 = wtab[wc * 16 + fr], W1 = wtab[32 + wc * 16 + fr], W2 = wtab[64 + wc * 16 + fr], W3 = wtab[96 + wc * 16 + fr];
    const f32x2 Q2 = cfma(a64, W0, W1), Q3 = cfma(a64, Q2, W2), gag = cfma(a64, Q3, W3);
    const f32x2 Qw = wr == 0 ? (f32x2){0.f, 0.f} : wr == 1 ? W0 : wr == 2 ? Q2 : Q3;
    const f32x2 aw = wr == 0 ? (f32x2){1.f, 0.f} : wr == 1 ? a64 : wr == 2 ? a128 : a192;
    gu64* carry = (gu64*)(ws + WS_CARRY);
    if (wr == 0 && fq == 0 && pm < 7) {
        unsigned ux = __float_as_uint(gag.x), uy = __float_as_uint(gag.y);
        if (ux == 0xFFFFFFFFu) ux = 0x7FC00000u;
        __hip_atomic_store(carry + pm * 1024 + ch, ((unsigned long long)uy << 32) | ux, RLX_AGENT);
    }

    char* a2 = ws + WS_A2;
    const int mblk = pm * 2 + (wr >> 1);
    {
        const unsigned lo = (unsigned)(((fr >> 2) & 1) * 2048 + (fq >> 1) * 256 + ((fr >> 3) * 4 + (fr & 3)) * 32 + (fq & 1) * 16);
#pragma unroll
        for (int n = 0; n < 3; ++n) {
            const int K16 = 128 + 7 * j + 3 * wc + n;
            char* dst = a2 + (size_t)(((K16 >> 2) * 16 + mblk) * 16384 + (K16 & 3) * 4096 + (wr & 1) * 1024) + lo;
#pragma unroll
            for (int hh = 0; hh < 2; ++hh) {
                float v[8];
#pragma unroll
                for (int e = 0; e < 8; ++e) v[e] = fmaxf(acc[2 * hh + (e >> 2)][n][e & 3], 0.f) * acc[2 * hh + (e >> 2)][3 + n][e & 3];
                *(u32x4*)(dst + hh * 512) = (u32x4){pk2(v[0], v[1]), pk2(v[2], v[3]), pk2(v[4], v[5]), pk2(v[6], v[7])};
            }
        }
        const int K16 = 128 + 7 * j + 6;
        const unsigned lm = (unsigned)(((fr >> 2) & 1) * 2048 + (fr >> 3) * 512 + (fq >> 1) * 256 + (wc * 4 + (fr & 3)) * 32 + (fq & 1) * 16);
        char* dst = a2 + (size_t)(((K16 >> 2) * 16 + mblk) * 16384 + (K16 & 3) * 4096 + (wr & 1) * 1024) + lm;
        float v[8];
        const bool lowh = fr < 8;
#pragma unroll
        for (int e = 0; e < 8; ++e) {
            const float v0 = acc[e >> 2][6][e & 3], v1 = acc[2 + (e >> 2)][6][e & 3];
            const float snd = lowh ? v1 : v0, own = lowh ? v0 : v1;
            const float rcv = __int_as_float(__builtin_amdgcn_mov_dpp(__float_as_int(snd), 0x128, 0xf, 0xf, true));
            v[e] = lowh ? fmaxf(own, 0.f) * rcv : fmaxf(rcv, 0.f) * own;
        }
        *(u32x4*)dst = (u32x4){pk2(v[0], v[1]), pk2(v[2], v[3]), pk2(v[4], v[5]), pk2(v[6], v[7])};
    }
    {
        float s = 0.f;
#pragma unroll
        for (int e = 0; e < 4; ++e) {
            s += rbf(__uint_as_float(xa[e] << 16) * __uint_as_float(ga[e] << 16)) + rbf(__uint_as_float(xa[e] & 0xffff0000u) * __uint_as_float(ga[e] & 0xffff0000u));
            s += rbf(__uint_as_float(xb[e] << 16) * __uint_as_float(gb[e] << 16)) + rbf(__uint_as_float(xb[e] & 0xffff0000u) * __uint_as_float(gb[e] & 0xffff0000u));
        }
#pragma unroll
        for (int o = 32; o > 0; o >>= 1) s += __shfl_xor(s, o);
        const float g = rbf(1.f / (1.f + expf(-rbf(s))));
        if (lane == 0) *(f32x2*)(ws + WS_G + grow * 8) = (f32x2){g, rbf(1.f - g)};
    }
    f32x2 cg = {0.f, 0.f};
    if (pm > 0) {
        unsigned long long gq[7];
        for (unsigned spins = 0;; ++spins) {
            bool ok = true;
#pragma unroll
            for (int q = 0; q < 7; ++q) {
                gq[q] = __hip_atomic_load(carry + (q < pm ? q : 0) * 1024 + ch, RLX_AGENT);
                ok &= (unsigned)gq[q] != 0xFFFFFFFFu;
            }
            if (__all(ok) || spins > (1u << 18)) break;
            __builtin_amdgcn_s_sleep(1);
        }
#pragma unroll
        for (int q = 0; q < 7; ++q)
            if (q < pm) cg = cfma(a256, cg, (f32x2){__uint_as_float((unsigned)gq[q]), __uint_as_float((unsigned)(gq[q] >> 32))});
    }
    h = cfma(aq, cfma(aw, cg, Qw), Pq);
    {
        const unsigned lo = (unsigned)(((fr >> 2) & 1) * 4096 + (fr >> 3) * 2048 + (fq >> 1) * 256 + (4 * wc + (fr & 3)) * 32 + (fq & 1) * 16);
        char* dst = a2 + (size_t)(((j >> 1) * 32 + mblk) * 16384 + (j & 1) * 8192 + (wr & 1) * 1024) + lo;
#pragma unroll
        for (int hh = 0; hh < 2; ++hh) {
            float vr[8], vi[8];
#pragma unroll
            for (int e = 0; e < 8; ++e) {
                h = cfma(a1, h, (f32x2){acc[2 * hh + (e >> 2)][7][e & 3], acc[2 * hh + (e >> 2)][8][e & 3]});
                vr[e] = h.x; vi[e] = -h.y;
            }
            *(u32x4*)(dst + hh * 512) = (u32x4){pk2(vr[0], vr[1]), pk2(vr[2], vr[3]), pk2(vr[4], vr[5]), pk2(vr[6], vr[7])};
            *(u32x4*)(dst + 16 * 16384 + hh * 512) = (u32x4){pk2(vi[0], vi[1]), pk2(vi[2], vi[3]), pk2(vi[4], vi[5]), pk2(vi[6], vi[7])};
        }
    }
}

#define K2_SPLIT 40
#define K2_NS 4
constexpr int K2_LDS = K2_NS * 32768;

__global__ __launch_bounds__(512, 2) void k2_gemm_out(const u16* __restrict__ x, const u16* __restrict__ Cre, const u16* __restrict__ Cim,
                                                      const u16* __restrict__ Wd, const u16* __restrict__ dskip, char* __restrict__ ws,
                                                      float* __restrict__ out) {
    extern __shared__ __attribute__((aligned(16))) char smem[];
    LAS char* lds = (LAS char*)smem;
    const unsigned ldsb = (unsigned)(size_t)lds;
    const int tid = threadIdx.x, lane = tid & 63;
    const int wid = __builtin_amdgcn_readfirstlane(tid >> 6);
    const int kk = wid >> 2, wm = (wid >> 1) & 1, wn = wid & 1;
    const int bid = blockIdx.x, xc = bid & 7, idx = bid >> 3;
    const int late = xc & 1, mblk = (xc >> 2) * 8 + (idx >> 2), nblk = ((xc >> 1) & 1) * 4 + (idx & 3);
    const int tile = mblk * 8 + nblk, n0 = nblk * 128;
    const int ks0 = late ? K2_SPLIT : 0, ks1 = late ? KSTEPS2 : K2_SPLIT;
    const int fr = lane & 15, fq = lane >> 4;

    const __amdgpu_buffer_rsrc_t sA = __builtin_amdgcn_make_buffer_rsrc((void*)(ws + WS_A2), (short)0, (int)WS_A2_BYTES, 0x00020000);
    const __amdgpu_buffer_rsrc_t sCr = __builtin_amdgcn_make_buffer_rsrc((void*)Cre, (short)0, DM * NP * 2, 0x00020000);
    const __amdgpu_buffer_rsrc_t sCi = __builtin_amdgcn_make_buffer_rsrc((void*)Cim, (short)0, DM * NP * 2, 0x00020000);
    const __amdgpu_buffer_rsrc_t sW = __builtin_amdgcn_make_buffer_rsrc((void*)Wd, (short)0, FF * DM * 2, 0x00020000);

    const int l3 = lane >> 3;
    const unsigned voffA = (unsigned)(lane * 16);
    const unsigned voffC = (unsigned)(l3 * 2048 + (((lane & 7) ^ (l3 & 6)) * 16));
    const unsigned voffW = (unsigned)(((l3 >> 2) * 8 + (l3 & 3)) * 2048 + ((((lane >> 1) & 3) ^ (l3 >> 1)) * 32) + (lane & 1) * 16);
    const unsigned soA = (unsigned)(mblk * 16384 + wid * 2048);
    const unsigned soC = (unsigned)((n0 + 16 * wid) * 2048);
    const unsigned soW = (unsigned)(((wid >> 1) * 16 + (wid & 1) * 4) * 2048 + n0 * 2);
    const unsigned mB = ldsb + wid * 2048;

    auto stage = [&](int ks, int slot) {
        const unsigned m = mB + slot * 32768;
        dma16(m, voffA, sA, soA + ks * (16 * 16384));
        dma16(m + 1024, voffA, sA, soA + ks * (16 * 16384) + 1024);
        if (ks < 32) {
            const unsigned so = soC + (ks >> 1) * 128;
            if (ks & 1) { dma16(m + 16384, voffC, sCi, so); dma16(m + 16384 + 1024, voffC, sCi, so + 8 * 2048); }
            else        { dma16(m + 16384, voffC, sCr, so); dma16(m + 16384 + 1024, voffC, sCr, so + 8 * 2048); }
        } else {
            const unsigned so = soW + (ks - 32) * (64 * 2048);
            dma16(m + 16384, voffW, sW, so); dma16(m + 16384 + 1024, voffW, sW, so + 128);
        }
    };
    auto waitv = [&](int s) {
        int last = s + K2_NS - 1; if (last > ks1 - 1) last = ks1 - 1;
        const int allow = last - (s + 1);
        if (K2_NS == 5 && allow >= 3) WAIT_V(12);
        else if (allow >= 2) WAIT_V(8);
        else if (allow == 1) WAIT_V(4);
        else WAIT_V(0);
    };

    asm volatile("s_nop 4");
#pragma unroll
    for (int i = 0; i < K2_NS - 1; ++i) stage(ks0 + i, i);

    LAS char* rA = lds + kk * 8192 + (lane >> 5) * 4096 + (lane & 31) * 8 + wm * 1024;
    LAS char* rC = lds + 16384 + (wn * 64 + fr) * 128 + (((4 * kk + fq) ^ (fr & 6)) * 16);
    LAS char* rWb = lds + 16384 + kk * 8192 + (lane >> 5) * 4096 + wn * 1024 + ((lane >> 2) & 7) * 128 + (lane & 3) * 8;
    const int rsw = (lane >> 3) & 3;

    f32x4 acc1[4][4], acc2[4][4];
#pragma unroll
    for (int n = 0; n < 4; ++n)
#pragma unroll
        for (int m = 0; m < 4; ++m) { acc1[n][m] = (f32x4){0.f, 0.f, 0.f, 0.f}; acc2[n][m] = (f32x4){0.f, 0.f, 0.f, 0.f}; }
    bf16x8 Hf[4], Wf[4];

    if (K2_NS == 5) WAIT_V(12); else WAIT_V(8);
    BAR();
    if (kk == 1) BAR();
    int slot = 0, s = ks0;
    for (; s < 32 && s < ks1; ++s) {
        const int so = slot * 32768;
#pragma unroll
        for (int n = 0; n < 4; ++n) Wf[n] = lds_b128(rC + so + n * 2048);
#pragma unroll
        for (int m = 0; m < 4; ++m) Hf[m] = lds_tr(rA + so + m * 256, rA + so + m * 256 + 2048);
        const int ps = slot == 0 ? K2_NS - 1 : slot - 1;
        if (s + K2_NS - 1 < ks1) stage(s + K2_NS - 1, ps);
        waitv(s);
        WAIT_L0(); BAR(); SCHED();
        __builtin_amdgcn_s_setprio(1);
#pragma unroll
        for (int n = 0; n < 4; ++n)
#pragma unroll
            for (int m = 0; m < 4; ++m) acc1[n][m] = MFMA(Wf[n], Hf[m], acc1[n][m]);
        __builtin_amdgcn_s_setprio(0);
        SCHED(); BAR();
        slot = slot == K2_NS - 1 ? 0 : slot + 1;
    }
    for (; s < ks1; ++s) {
        const int so = slot * 32768;
#pragma unroll
        for (int n = 0; n < 4; ++n) Wf[n] = lds_tr(rWb + so + ((n ^ rsw) * 32), rWb + so + ((n ^ rsw) * 32) + 2048);
#pragma unroll
        for (int m = 0; m < 4; ++m) Hf[m] = lds_tr(rA + so + m * 256, rA + so + m * 256 + 2048);
        const int ps = slot == 0 ? K2_NS - 1 : slot - 1;
        if (s + K2_NS - 1 < ks1) stage(s + K2_NS - 1, ps);
        waitv(s);
        WAIT_L0(); BAR(); SCHED();
        __builtin_amdgcn_s_setprio(1);
#pragma unroll
        for (int n = 0; n < 4; ++n)
#pragma unroll
            for (int m = 0; m < 4; ++m) acc2[n][m] = MFMA(Wf[n], Hf[m], acc2[n][m]);
        __builtin_amdgcn_s_setprio(0);
        SCHED(); BAR();
        slot = slot == K2_NS - 1 ? 0 : slot + 1;
    }
    if (kk == 0) BAR();

    const int tb = mblk * 128 + wm * 64 + 16 * (fr >> 3) + (fr & 7);
    f32x2 gg[4];
#pragma unroll
    for (int m = 0; m < 4; ++m) gg[m] = *(const f32x2*)(ws + WS_G + (size_t)(tb + 32 * (m & 1) + 8 * (m >> 1)) * 8);
#pragma unroll
    for (int n = 0; n < 4; ++n)
#pragma unroll
        for (int m = 0; m < 4; ++m) acc2[n][m] = acc2[n][m] * gg[m].y + acc1[n][m] * (2.f * gg[m].x);
    LAS f32x4* red = (LAS f32x4*)lds;
#pragma unroll
    for (int n = 0; n < 4; ++n)
#pragma unroll
        for (int i = 0; i < 2; ++i) red[(wid * 8 + n * 2 + i) * 64 + lane] = kk ? acc2[n][i] : acc2[n][2 + i];
    __syncthreads();
    f32x4 r[4][2];
#pragma unroll
    for (int n = 0; n < 4; ++n)
#pragma unroll
        for (int i = 0; i < 2; ++i) r[n][i] = (kk ? acc2[n][2 + i] : acc2[n][i]) + red[((wid ^ 4) * 8 + n * 2 + i) * 64 + lane];
    const int t0 = tb + 8 * kk;
    const int d0 = n0 + wn * 64 + 4 * fq;
    const __amdgpu_buffer_rsrc_t sP = __builtin_amdgcn_make_buffer_rsrc((void*)(ws + WS_PART + (size_t)tile * 65536), (short)0, 65536, 0x00020000);
    gu32* flag = (gu32*)(ws + WS_FLAG2 + tile * 128);
    if (!late) {
#pragma unroll
        for (int n = 0; n < 4; ++n) {
            const u32x2 dk = *(const u32x2*)(dskip + d0 + 16 * n);
#pragma unroll
            for (int i = 0; i < 2; ++i) {
                const u32x2 xv = *(const u32x2*)(x + (size_t)(t0 + 32 * i) * DM + d0 + 16 * n);
                const float g = kk ? gg[2 + i].x : gg[i].x;
                f32x4 v = r[n][i];
                v[0] += g * rbf(__uint_as_float(xv[0] << 16) * __uint_as_float(dk[0] << 16));
                v[1] += g * rbf(__uint_as_float(xv[0] & 0xffff0000u) * __uint_as_float(dk[0] & 0xffff0000u));
                v[2] += g * rbf(__uint_as_float(xv[1] << 16) * __uint_as_float(dk[1] << 16));
                v[3] += g * rbf(__uint_as_float(xv[1] & 0xffff0000u) * __uint_as_float(dk[1] & 0xffff0000u));
                __builtin_amdgcn_raw_buffer_store_b128(__builtin_bit_cast(u32x4, v), sP, ((wid * 8 + n * 2 + i) * 64 + lane) * 16, 0, 16);
            }
        }
        WAIT_V(0);
        __syncthreads();
        if (tid == 0) __hip_atomic_store(flag, 1u, RLX_AGENT);
    } else {
        if (wid == 0) {
            for (unsigned spins = 0; spins < (1u << 18); ++spins) {
                if (__builtin_amdgcn_readfirstlane(__hip_atomic_load(flag, RLX_AGENT)) == 1u) break;
                __builtin_amdgcn_s_sleep(1);
            }
        }
        __syncthreads();
        u32x4 pv[4][2];
#pragma unroll
        for (int n = 0; n < 4; ++n)
#pragma unroll
            for (int i = 0; i < 2; ++i) pv[n][i] = __builtin_amdgcn_raw_buffer_load_b128(sP, ((wid * 8 + n * 2 + i) * 64 + lane) * 16, 0, 16);
#pragma unroll
        for (int n = 0; n < 4; ++n)
#pragma unroll
            for (int i = 0; i < 2; ++i) *(f32x4*)(out + (size_t)(t0 + 32 * i) * DM + d0 + 16 * n) = r[n][i] + __builtin_bit_cast(f32x4, pv[n][i]);
    }
}

extern "C" void kernel_launch(void* const* d_in, const int* in_sizes, int n_in, void* d_out, int out_size, void* d_ws, size_t ws_size,
                              hipStream_t stream) {
    const u16* x = (const u16*)d_in[0];
    const u16* lre = (const u16*)d_in[1];
    const u16* lim = (const u16*)d_in[2];
    const u16* Bre = (const u16*)d_in[3];
    const u16* Bim = (const u16*)d_in[4];
    const u16* Cre = (const u16*)d_in[5];
    const u16* Cim = (const u16*)d_in[6];
    const u16* logdt = (const u16*)d_in[7];
    const u16* dskip = (const u16*)d_in[8];
    const u16* Wg = (const u16*)d_in[10];
    const u16* Wu = (const u16*)d_in[11];
    const u16* Wd = (const u16*)d_in[12];
    const u16* gw = (const u16*)d_in[13];
    char* ws = (char*)d_ws;
    (void)hipFuncSetAttribute((const void*)k1_gemm_scan, hipFuncAttributeMaxDynamicSharedMemorySize, K1_LDS);
    (void)hipFuncSetAttribute((const void*)k2_gemm_out, hipFuncAttributeMaxDynamicSharedMemorySize, K2_LDS);
    (void)hipMemsetAsync(ws + WS_CARRY, 0xFF, WS_CARRY_BYTES, stream);
    k1_gemm_scan<<<256, 512, K1_LDS, stream>>>(x, Wg, Wu, Bre, Bim, lre, lim, logdt, gw, ws);
    k2_gemm_out<<<256, 512, K2_LDS, stream>>>(x, Cre, Cim, Wd, dskip, ws, (float*)d_out);
}
```

```cpp
#include <hip/hip_runtime.h>
#include <hip/hip_bf16.h>

typedef unsigned short u16;
typedef __bf16 bf16x8 __attribute__((ext_vector_type(8)));
typedef short s16x4 __attribute__((ext_vector_type(4)));
typedef short s16x8 __attribute__((ext_vector_type(8)));
typedef float f32x4 __attribute__((ext_vector_type(4)));
typedef float f32x2 __attribute__((ext_vector_type(2)));
typedef unsigned u32x4 __attribute__((ext_vector_type(4)));
typedef unsigned u32x2 __attribute__((ext_vector_type(2)));
#define LAS __attribute__((address_space(3)))
#define GAS __attribute__((address_space(1)))
typedef GAS unsigned gu32;
typedef GAS unsigned long long gu64;
#define RLX_AGENT __ATOMIC_RELAXED, __HIP_MEMORY_SCOPE_AGENT

constexpr int SEQ = 2048, DM = 1024, NP = 1024, FF = 3584;
constexpr int KSTEPS2 = (2 * NP + FF) / 64;

constexpr size_t WS_CARRY = 0;
constexpr size_t WS_CARRY_BYTES = 8 * 1024 * 8;
constexpr size_t WS_FLAG2 = 65536;
constexpr size_t WS_G = 131072;
constexpr size_t WS_A2 = 1 << 20;
constexpr size_t WS_A2_BYTES = (size_t)KSTEPS2 * 16 * 16384;
constexpr size_t WS_PART = 32 << 20;

__device__ __forceinline__ float bf2f(u16 v) { return __uint_as_float(((unsigned)v) << 16); }
__device__ __forceinline__ float rbf(float f) { return (float)(__bf16)f; }
__device__ __forceinline__ unsigned pk2(float a, float b) {
    typedef __bf16 bf16x2 __attribute__((ext_vector_type(2)));
    bf16x2 v; v.x = (__bf16)a; v.y = (__bf16)b; return __builtin_bit_cast(unsigned, v);
}

#define STAMP(kid, i)
#define WAIT_V(n) asm volatile("s_waitcnt vmcnt(" #n ")" ::: "memory")
#define WAIT_L0() asm volatile("s_waitcnt lgkmcnt(0)" ::: "memory")
#define BAR() __builtin_amdgcn_s_barrier()
#define SCHED() __builtin_amdgcn_sched_barrier(0)

__device__ __forceinline__ void dma16(unsigned m0v, unsigned voff, __amdgpu_buffer_rsrc_t srd, unsigned soff) {
    asm volatile("s_mov_b32 m0, %0\n\ts_nop 0\n\tbuffer_load_dwordx4 %1, %2, %3 offen lds" ::"s"(m0v), "v"(voff), "s"(srd), "s"(soff) : "m0", "memory");
}
__device__ __forceinline__ bf16x8 lds_b128(LAS char* p) { return __builtin_bit_cast(bf16x8, *(LAS s16x8*)p); }
__device__ __forceinline__ bf16x8 lds_tr(LAS char* p0, LAS char* p1) {
    s16x4 a = __builtin_amdgcn_ds_read_tr16_b64_v4i16((LAS s16x4*)p0);
    s16x4 b = __builtin_amdgcn_ds_read_tr16_b64_v4i16((LAS s16x4*)p1);
    return __builtin_bit_cast(bf16x8, __builtin_shufflevector(a, b, 0, 1, 2, 3, 4, 5, 6, 7));
}
#define MFMA(a, b, c) __builtin_amdgcn_mfma_f32_16x16x32_bf16(a, b, c, 0, 0, 0)
__device__ __forceinline__ f32x2 cmul(f32x2 a, f32x2 b) { return (f32x2){a.x * b.x - a.y * b.y, a.x * b.y + a.y * b.x}; }
__device__ __forceinline__ f32x2 cfma(f32x2 a, f32x2 b, f32x2 c) { return (f32x2){a.x * b.x - a.y * b.y + c.x, a.x * b.y + a.y * b.x + c.y}; }

constexpr int K1_A = 0;
constexpr int K1_S = 65536;
constexpr int K1_W = 81920;
constexpr int K1_WBUF = 28704, K1_UOFF = 14352;
constexpr int K1_MISC = K1_W + 2 * K1_WBUF;
constexpr int K1_LDS = K1_MISC + 2048;

__global__ __launch_bounds__(512, 2) void k1_gemm_scan(const u16* __restrict__ x, const u16* __restrict__ Wg, const u16* __restrict__ Wu,
                                                       const u16* __restrict__ Bre, const u16* __restrict__ Bim, const u16* __restrict__ lre,
                                                       const u16* __restrict__ lim, const u16* __restrict__ logdt, const u16* __restrict__ gw,
                                                       char* __restrict__ ws) {
    extern __shared__ __attribute__((aligned(16))) char smem[];
    LAS char* lds = (LAS char*)smem;
    const unsigned ldsb = (unsigned)(size_t)lds;
    const int tid = threadIdx.x, lane = tid & 63;
    const int wid = __builtin_amdgcn_readfirstlane(tid >> 6);
    const int wr = wid & 3, wc = wid >> 2;
    const int bid = blockIdx.x, idx = bid >> 3, j = (bid & 7) * 4 + (idx & 3), pm = idx >> 2;
    const int fr = lane & 15, fq = lane >> 4;

    const __amdgpu_buffer_rsrc_t sX = __builtin_amdgcn_make_buffer_rsrc((void*)x, (short)0, SEQ * DM * 2, 0x00020000);
    const __amdgpu_buffer_rsrc_t sG = __builtin_amdgcn_make_buffer_rsrc((void*)Wg, (short)0, DM * FF * 2, 0x00020000);
    const __amdgpu_buffer_rsrc_t sU = __builtin_amdgcn_make_buffer_rsrc((void*)Wu, (short)0, DM * FF * 2, 0x00020000);
    const __amdgpu_buffer_rsrc_t sB = __builtin_amdgcn_make_buffer_rsrc((void*)(((wid >> 1) & 1) ? Bim : Bre), (short)0, NP * DM * 2, 0x00020000);

    const int l3 = lane >> 3;
    const unsigned voffX = (unsigned)(((l3 >> 2) * 16 + (l3 & 3)) * 2048 + (((lane & 7) ^ (l3 & 6)) * 16));
    const int ww = wid < 6 ? wid : 6;
    unsigned voffW;
    { const int gam = ww * 64 + lane, rho = gam / 14, c = gam - rho * 14, k = (rho & 0x13) | ((rho & 4) << 1) | ((rho & 8) >> 1);
      voffW = (unsigned)(k * (FF * 2) + c * 16); }
    const unsigned soX = (unsigned)((pm * 256 + (wid >> 1) * 64 + (wid & 1) * 8) * 2048);
    const unsigned soS = (unsigned)((32 * j + 8 * (wid >> 2) + 4 * (wid & 1)) * 2048);
    const unsigned soW = (unsigned)(j * 224);
    const unsigned mX = ldsb + K1_A + wid * 4096, mS = ldsb + K1_S + wid * 1024, mW = ldsb + K1_W + ww * 1024;

#define ST_X(t, b) do { _Pragma("unroll") for (int i = 0; i < 4; ++i) dma16(mX + (b) * 32768 + i * 1024, voffX, sX, soX + (i & 1) * 65536 + (i >> 1) * 8192 + (t) * 128); } while (0)
#define ST_S(t, b) dma16(mS + (b) * 8192, voffX, sB, soS + (t) * 128)
#define ST_G(t, b) do { dma16(mW + (b) * K1_WBUF, voffW, sG, soW + (t) * (64 * FF * 2)); dma16(mW + (b) * K1_WBUF + 7168, voffW, sG, soW + (t) * (64 * FF * 2) + 32 * FF * 2); } while (0)
#define ST_U(t, b) do { dma16(mW + (b) * K1_WBUF + K1_UOFF, voffW, sU, soW + (t) * (64 * FF * 2)); dma16(mW + (b) * K1_WBUF + K1_UOFF + 7168, voffW, sU, soW + (t) * (64 * FF * 2) + 32 * FF * 2); } while (0)

    STAMP(0, 0);
    asm volatile("s_nop 4");
    ST_X(0, 0); ST_S(0, 0); ST_G(0, 0); ST_U(0, 0);
    ST_X(1, 1); ST_S(1, 1); ST_G(1, 1);

    const int ch0 = (fq ^ (fr & 6)) * 16;
    LAS char* rA0 = lds + K1_A + wr * 8192 + fr * 128 + ch0;
    LAS char* rA1 = lds + K1_A + wr * 8192 + fr * 128 + (ch0 ^ 64);
    LAS char* rS0 = lds + K1_S + wc * 4096 + fr * 128 + ch0;
    LAS char* rS1 = lds + K1_S + wc * 4096 + fr * 128 + (ch0 ^ 64);
    const int trow = (16 * (fq >> 1) + 4 * (fq & 1) + ((lane & 15) >> 2)) * 224;
    LAS char* rW = lds + K1_W + trow + wc * 96 + (lane & 3) * 8;
    LAS char* rM = lds + K1_W + trow + 192 + wc * 16 + (lane & 1) * 8 + ((lane >> 1) & 1) * K1_UOFF;

    f32x4 acc[4][9];
#pragma unroll
    for (int m = 0; m < 4; ++m)
#pragma unroll
        for (int n = 0; n < 9; ++n) acc[m][n] = (f32x4){0.f, 0.f, 0.f, 0.f};
    bf16x8 Af[4][2], Bf[3][2];

#define RD_A(b) do { _Pragma("unroll") for (int m = 0; m < 4; ++m) { Af[m][0] = lds_b128(rA0 + (b) * 32768 + m * 2048); Af[m][1] = lds_b128(rA1 + (b) * 32768 + m * 2048); } } while (0)
#define RD_S(slot, ri, b) do { Bf[slot][0] = lds_b128(rS0 + (b) * 8192 + (ri) * 2048); Bf[slot][1] = lds_b128(rS1 + (b) * 8192 + (ri) * 2048); } while (0)
#define RD_W(slot, base, off) do { Bf[slot][0] = lds_tr((base) + (off), (base) + (off) + 1792); Bf[slot][1] = lds_tr((base) + (off) + 7168, (base) + (off) + 7168 + 1792); } while (0)
#define MMA3(n0, n1, n2) do { __builtin_amdgcn_s_setprio(1); \
        _Pragma("unroll") for (int kk = 0; kk < 2; ++kk) _Pragma("unroll") for (int m = 0; m < 4; ++m) { \
            acc[m][n0] = MFMA(Af[m][kk], Bf[0][kk], acc[m][n0]); acc[m][n1] = MFMA(Af[m][kk], Bf[1][kk], acc[m][n1]); acc[m][n2] = MFMA(Af[m][kk], Bf[2][kk], acc[m][n2]); } \
        __builtin_amdgcn_s_setprio(0); } while (0)
#define KSTEP(t, b, MODE) do { \
        RD_S(0, 0, b); RD_S(1, 1, b); RD_W(2, rW, (b) * K1_WBUF); RD_A(b); \
        if (MODE < 2) ST_U((t) + 1, (b) ^ 1); \
        if (MODE < 2) WAIT_V(9); else WAIT_V(0); \
        WAIT_L0(); BAR(); SCHED(); MMA3(7, 8, 0); SCHED(); BAR(); \
        RD_W(0, rW, (b) * K1_WBUF + 32); RD_W(1, rW, (b) * K1_WBUF + 64); RD_W(2, rM, (b) * K1_WBUF); \
        if (MODE == 0) ST_X((t) + 2, b); \
        WAIT_L0(); BAR(); SCHED(); MMA3(1, 2, 6); SCHED(); BAR(); \
        RD_W(0, rW, (b) * K1_WBUF + K1_UOFF); RD_W(1, rW, (b) * K1_WBUF + K1_UOFF + 32); RD_W(2, rW, (b) * K1_WBUF + K1_UOFF + 64); \
        if (MODE == 0) { ST_S((t) + 2, b); ST_G((t) + 2, b); WAIT_V(9); } \
        if (MODE == 1) WAIT_V(2); \
        WAIT_L0(); BAR(); SCHED(); MMA3(3, 4, 5); SCHED(); BAR(); \
    } while (0)

    WAIT_V(9); BAR();
    unsigned long long st1c = __builtin_amdgcn_s_memtime(), st1r = __builtin_amdgcn_s_memrealtime();
    __builtin_amdgcn_s_waitcnt(0xC07F);
    if (wc == 1) BAR();
    for (int t = 0; t < 14; t += 2) {
        KSTEP(t, 0, 0);
        KSTEP(t + 1, 1, 0);
    }
    KSTEP(14, 0, 1);
    KSTEP(15, 1, 2);
    if (wc == 0) BAR();
    STAMP(0, 2);

    const int ch = 32 * j + 8 * wc + 4 * (fr >> 3) + 16 * ((fr >> 2) & 1) + (fr & 3);
    const float lr = bf2f(lre[ch]), li = bf2f(lim[ch]), ldt = bf2f(logdt[ch]);
    const int grow = pm * 256 + j * 8 + wid;
    const u32x4 xa = *(const u32x4*)(x + (size_t)grow * DM + lane * 8), xb = *(const u32x4*)(x + (size_t)grow * DM + 512 + lane * 8);
    const u32x4 ga = *(const u32x4*)(gw + lane * 8), gb = *(const u32x4*)(gw + 512 + lane * 8);
    if (bid < 128 && tid == 0) *(unsigned*)(ws + WS_FLAG2 + bid * 128) = 0u;

    const float dt = rbf(expf(ldt));
    f32x2 a1, cf;
    { const float e = expf(dt * lr); float s, c; sincosf(dt * li, &s, &c); a1 = (f32x2){e * c, e * s};
      const float nr = a1.x - 1.f, ni = a1.y, den = lr * lr + li * li; cf = (f32x2){(nr * lr + ni * li) / den, (ni * lr - nr * li) / den}; }
    f32x2 h = {0.f, 0.f};
#pragma unroll
    for (int m = 0; m < 4; ++m)
#pragma unroll
        for (int q = 0; q < 4; ++q) {
            const f32x2 b = cmul(cf, (f32x2){acc[m][7][q], acc[m][8][q]});
            acc[m][7][q] = b.x; acc[m][8][q] = b.y; h = cfma(a1, h, b);
        }
    f32x2 a16 = cmul(a1, a1); a16 = cmul(a16, a16); a16 = cmul(a16, a16); a16 = cmul(a16, a16);
    const f32x2 a32 = cmul(a16, a16), a48 = cmul(a32, a16), a64 = cmul(a32, a32), a128 = cmul(a64, a64), a192 = cmul(a128, a64), a256 = cmul(a128, a128);
    f32x2 E[4];
#pragma unroll
    for (int f = 0; f < 4; ++f) E[f] = (f32x2){__shfl(h.x, fr + 16 * f), __shfl(h.y, fr + 16 * f)};
    const f32x2 P2 = cfma(a16, E[0], E[1]), P3 = cfma(a16, P2, E[2]), wag = cfma(a16, P3, E[3]);
    const f32x2 Pq = fq == 0 ? (f32x2){0.f, 0.f} : fq == 1 ? E[0] : fq == 2 ? P2 : P3;
    const f32x2 aq = fq == 0 ? (f32x2){1.f, 0.f} : fq == 1 ? a16 : fq == 2 ? a32 : a48;
    LAS f32x2* wtab = (LAS f32x2*)(lds + K1_MISC);
    if (fq == 0) wtab[wr * 32 + wc * 16 + fr] = wag;
    __syncthreads();
    const f32x2 W0 = wtab[wc * 16 + fr], W1 = wtab[32 + wc * 16 + fr], W2 = wtab[64 + wc * 16 + fr], W3 = wtab[96 + wc * 16 + fr];
    const f32x2 Q2 = cfma(a64, W0, W1), Q3 = cfma(a64, Q2, W2), gag = cfma(a64, Q3, W3);
    const f32x2 Qw = wr == 0 ? (f32x2){0.f, 0.f} : wr == 1 ? W0 : wr == 2 ? Q2 : Q3;
    const f32x2 aw = wr == 0 ? (f32x2){1.f, 0.f} : wr == 1 ? a64 : wr == 2 ? a128 : a192;
    gu64* carry = (gu64*)(ws + WS_CARRY);
    if (wr == 0 && fq == 0 && pm < 7) {
        unsigned ux = __float_as_uint(gag.x), uy = __float_as_uint(gag.y);
        if (ux == 0xFFFFFFFFu) ux = 0x7FC00000u;
        __hip_atomic_store(carry + pm * 1024 + ch, ((unsigned long long)uy << 32) | ux, RLX_AGENT);
    }

    STAMP(0, 3);
    char* a2 = ws + WS_A2;
    const int mblk = pm * 2 + (wr >> 1);
    {
        const unsigned lo = (unsigned)(((fr >> 2) & 1) * 2048 + (fq >> 1) * 256 + ((fr >> 3) * 4 + (fr & 3)) * 32 + (fq & 1) * 16);
#pragma unroll
        for (int n = 0; n < 3; ++n) {
            const int K16 = 128 + 7 * j + 3 * wc + n;
            char* dst = a2 + (size_t)(((K16 >> 2) * 16 + mblk) * 16384 + (K16 & 3) * 4096 + (wr & 1) * 1024) + lo;
#pragma unroll
            for (int hh = 0; hh < 2; ++hh) {
                float v[8];
#pragma unroll
                for (int e = 0; e < 8; ++e) v[e] = fmaxf(acc[2 * hh + (e >> 2)][n][e & 3], 0.f) * acc[2 * hh + (e >> 2)][3 + n][e & 3];
                *(u32x4*)(dst + hh * 512) = (u32x4){pk2(v[0], v[1]), pk2(v[2], v[3]), pk2(v[4], v[5]), pk2(v[6], v[7])};
            }
        }
        const int K16 = 128 + 7 * j + 6;
        const unsigned lm = (unsigned)(((fr >> 2) & 1) * 2048 + (fr >> 3) * 512 + (fq >> 1) * 256 + (wc * 4 + (fr & 3)) * 32 + (fq & 1) * 16);
        char* dst = a2 + (size_t)(((K16 >> 2) * 16 + mblk) * 16384 + (K16 & 3) * 4096 + (wr & 1) * 1024) + lm;
        float v[8];
        const bool lowh = fr < 8;
#pragma unroll
        for (int e = 0; e < 8; ++e) {
            const float v0 = acc[e >> 2][6][e & 3], v1 = acc[2 + (e >> 2)][6][e & 3];
            const float snd = lowh ? v1 : v0, own = lowh ? v0 : v1;
            const float rcv = __int_as_float(__builtin_amdgcn_mov_dpp(__float_as_int(snd), 0x128, 0xf, 0xf, true));
            v[e] = lowh ? fmaxf(own, 0.f) * rcv : fmaxf(rcv, 0.f) * own;
        }
        *(u32x4*)dst = (u32x4){pk2(v[0], v[1]), pk2(v[2], v[3]), pk2(v[4], v[5]), pk2(v[6], v[7])};
    }
    {
        float s = 0.f;
#pragma unroll
        for (int e = 0; e < 4; ++e) {
            s += rbf(__uint_as_float(xa[e] << 16) * __uint_as_float(ga[e] << 16)) + rbf(__uint_as_float(xa[e] & 0xffff0000u) * __uint_as_float(ga[e] & 0xffff0000u));
            s += rbf(__uint_as_float(xb[e] << 16) * __uint_as_float(gb[e] << 16)) + rbf(__uint_as_float(xb[e] & 0xffff0000u) * __uint_as_float(gb[e] & 0xffff0000u));
        }
#pragma unroll
        for (int o = 32; o > 0; o >>= 1) s += __shfl_xor(s, o);
        const float g = rbf(1.f / (1.f + expf(-rbf(s))));
        if (lane == 0) *(f32x2*)(ws + WS_G + grow * 8) = (f32x2){g, rbf(1.f - g)};
    }
    STAMP(0, 4);
    f32x2 cg = {0.f, 0.f};
    if (pm > 0) {
        unsigned long long gq[7];
        for (unsigned spins = 0;; ++spins) {
            bool ok = true;
#pragma unroll
            for (int q = 0; q < 7; ++q) {
                gq[q] = __hip_atomic_load(carry + (q < pm ? q : 0) * 1024 + ch, RLX_AGENT);
                ok &= (unsigned)gq[q] != 0xFFFFFFFFu;
            }
            if (__all(ok) || spins > (1u << 18)) break;
            __builtin_amdgcn_s_sleep(1);
        }
#pragma unroll
        for (int q = 0; q < 7; ++q)
            if (q < pm) cg = cfma(a256, cg, (f32x2){__uint_as_float((unsigned)gq[q]), __uint_as_float((unsigned)(gq[q] >> 32))});
    }
    STAMP(0, 5);
    h = cfma(aq, cfma(aw, cg, Qw), Pq);
    {
        const unsigned lo = (unsigned)(((fr >> 2) & 1) * 4096 + (fr >> 3) * 2048 + (fq >> 1) * 256 + (4 * wc + (fr & 3)) * 32 + (fq & 1) * 16);
        char* dst = a2 + (size_t)(((j >> 1) * 32 + mblk) * 16384 + (j & 1) * 8192 + (wr & 1) * 1024) + lo;
#pragma unroll
        for (int hh = 0; hh < 2; ++hh) {
            float vr[8], vi[8];
#pragma unroll
            for (int e = 0; e < 8; ++e) {
                h = cfma(a1, h, (f32x2){acc[2 * hh + (e >> 2)][7][e & 3], acc[2 * hh + (e >> 2)][8][e & 3]});
                vr[e] = h.x; vi[e] = -h.y;
            }
            *(u32x4*)(dst + hh * 512) = (u32x4){pk2(vr[0], vr[1]), pk2(vr[2], vr[3]), pk2(vr[4], vr[5]), pk2(vr[6], vr[7])};
            *(u32x4*)(dst + 16 * 16384 + hh * 512) = (u32x4){pk2(vi[0], vi[1]), pk2(vi[2], vi[3]), pk2(vi[4], vi[5]), pk2(vi[6], vi[7])};
        }
    }
    STAMP(0, 6);
    STAMP(0, 7);
}

#define K2_SPLIT 40
#define ABL_NOLOAD 0
#define ABL_NOREAD 0
#define ABL_NOMFMA 0
#define MAP 0
constexpr int K2_LDS = 5 * 32768;

__global__ __launch_bounds__(512, 2) void k2_gemm_out(const u16* __restrict__ x, const u16* __restrict__ Cre, const u16* __restrict__ Cim,
                                                      const u16* __restrict__ Wd, const u16* __restrict__ dskip, char* __restrict__ ws,
                                                      float* __restrict__ out) {
    extern __shared__ __attribute__((aligned(16))) char smem[];
    LAS char* lds = (LAS char*)smem;
    const unsigned ldsb = (unsigned)(size_t)lds;
    const int tid = threadIdx.x, lane = tid & 63;
    const int wid = __builtin_amdgcn_readfirstlane(tid >> 6);
    const int grp = wid >> 2, w4 = wid & 3, wm = (wid >> 1) & 1, wn = wid & 1;
    const int bid = blockIdx.x, xc = bid & 7, idx = bid >> 3;
    const int late = xc & 1, mblk = (xc >> 2) * 8 + (idx >> 2), nblk = ((xc >> 1) & 1) * 4 + (idx & 3);
    const int tile = mblk * 8 + nblk, n0 = nblk * 128;
    const int fr = lane & 15, fq = lane >> 4;

    const __amdgpu_buffer_rsrc_t sA = __builtin_amdgcn_make_buffer_rsrc((void*)(ws + WS_A2), (short)0, (int)WS_A2_BYTES, 0x00020000);
    const __amdgpu_buffer_rsrc_t sC = __builtin_amdgcn_make_buffer_rsrc((void*)(grp ? Cim : Cre), (short)0, DM * NP * 2, 0x00020000);
    const __amdgpu_buffer_rsrc_t sW = __builtin_amdgcn_make_buffer_rsrc((void*)Wd, (short)0, FF * DM * 2, 0x00020000);

    const int l3 = lane >> 3;
    const unsigned voffA = (unsigned)(lane * 16);
    const unsigned voffC = (unsigned)(l3 * 2048 + (((lane & 7) ^ (l3 & 6)) * 16));
    const unsigned voffW = (unsigned)(((l3 >> 2) * 8 + (l3 & 3)) * 2048 + ((((lane >> 1) & 3) ^ (l3 >> 1)) * 32) + (lane & 1) * 16);
    const unsigned soA = (unsigned)(mblk * 16384 + w4 * 4096);
    const unsigned soC = (unsigned)((n0 + 32 * w4) * 2048);
    const unsigned soW = (unsigned)(w4 * 16 * 2048 + n0 * 2) - 32u * (64 * 2048);
    const unsigned mB = ldsb + w4 * 4096;

#define DMA_A(i, ks, sl) dma16(mB + (sl) + (i) * 1024, voffA, sA, soA + (ks) * (16 * 16384) + (i) * 1024)
#define DMA_C(i, ks, sl) dma16(mB + (sl) + 16384 + (i) * 1024, voffC, sC, soC + ((ks) >> 1) * 128 + (i) * (8 * 2048))
#define DMA_W(i, ks, sl) dma16(mB + (sl) + 16384 + (i) * 1024, voffW, sW, soW + (ks) * (64 * 2048) + ((i) >> 1) * (4 * 2048) + ((i) & 1) * 128)
#define DMA_B(MODE, i, ks, sl) do { if (MODE == 0) DMA_C(i, ks, sl); else DMA_W(i, ks, sl); } while (0)

    STAMP(1, 0);
    int s = (late ? K2_SPLIT : 0) + grp;
    asm volatile("s_nop 4");
    if (late) {
#pragma unroll
        for (int p = 0; p < 2; ++p)
#pragma unroll
            for (int i = 0; i < 4; ++i) { DMA_A(i, s + 2 * p, (grp + 2 * p) * 32768); DMA_W(i, s + 2 * p, (grp + 2 * p) * 32768); }
    } else {
#pragma unroll
        for (int p = 0; p < 2; ++p)
#pragma unroll
            for (int i = 0; i < 4; ++i) { DMA_A(i, s + 2 * p, (grp + 2 * p) * 32768); DMA_C(i, s + 2 * p, (grp + 2 * p) * 32768); }
    }

    LAS char* rA = lds + (lane >> 5) * 4096 + (lane & 31) * 8 + wm * 1024;
    LAS char* rC0 = lds + 16384 + (wn * 64 + fr) * 128 + ((fq ^ (fr & 6)) * 16);
    LAS char* rC1 = lds + 16384 + (wn * 64 + fr) * 128 + (((4 + fq) ^ (fr & 6)) * 16);
    LAS char* rWb = lds + 16384 + (lane >> 5) * 4096 + wn * 1024 + ((lane >> 2) & 7) * 128 + (lane & 3) * 8;
    const int rsw = (lane >> 3) & 3;

    f32x4 acc1[4][4], acc2[4][4];
#pragma unroll
    for (int n = 0; n < 4; ++n)
#pragma unroll
        for (int m = 0; m < 4; ++m) { acc1[n][m] = (f32x4){0.f, 0.f, 0.f, 0.f}; acc2[n][m] = (f32x4){0.f, 0.f, 0.f, 0.f}; }
    bf16x8 Hf[4][2], Wf[4][2];
    int sl = grp * 32768;

#define ITER(RD, ST, ACC, WV) do { \
        const int ps = sl == 0 ? 4 * 32768 : sl - 32768; \
        _Pragma("unroll") for (int q = 0; q < 4; ++q) { \
            if (ST != 2 && !ABL_NOLOAD) DMA_A(q, s + 4, ps); \
            if (!ABL_NOREAD) { \
            if (RD == 0) { Wf[q][0] = lds_b128(rC0 + sl + q * 2048); Wf[q][1] = lds_b128(rC1 + sl + q * 2048); } \
            else { LAS char* p_ = rWb + sl + ((q ^ rsw) * 32); Wf[q][0] = lds_tr(p_, p_ + 2048); Wf[q][1] = lds_tr(p_ + 8192, p_ + 8192 + 2048); } } \
            if (ST != 2 && !ABL_NOLOAD) DMA_B(ST, q, s + 4, ps); \
            if (!ABL_NOREAD) { LAS char* p_ = rA + sl + q * 256; Hf[q][0] = lds_tr(p_, p_ + 2048); Hf[q][1] = lds_tr(p_ + 8192, p_ + 8192 + 2048); } \
        } \
        WAIT_L0(); BAR(); SCHED(); \
        __builtin_amdgcn_s_setprio(1); \
        _Pragma("unroll") for (int kk = 0; kk < 2; ++kk) _Pragma("unroll") for (int n = 0; n < 4; ++n) _Pragma("unroll") for (int m = 0; m < 4; ++m) { \
            if (!ABL_NOMFMA) ACC[n][m] = MFMA(Wf[n][kk], Hf[m][kk], ACC[n][m]); else asm volatile("" ::"v"(Wf[n][kk]), "v"(Hf[m][kk])); } \
        __builtin_amdgcn_s_setprio(0); \
        SCHED(); WAIT_V(WV); BAR(); \
        s += 2; sl = sl >= 3 * 32768 ? sl - 3 * 32768 : sl + 2 * 32768; \
    } while (0)

    WAIT_V(8);
    BAR();
    unsigned long long st1c = __builtin_amdgcn_s_memtime(), st1r = __builtin_amdgcn_s_memrealtime();
    __builtin_amdgcn_s_waitcnt(0xC07F);
    if (grp == 1) BAR();
    if (!late) {
        for (int i = 0; i < 14; ++i) ITER(0, 0, acc1, 8);
        for (int i = 0; i < 2; ++i) ITER(0, 1, acc1, 8);
    }
    const int nf = (late ? KSTEPS2 - K2_SPLIT : K2_SPLIT - 32) / 2 - 2;
    for (int i = 0; i < nf; ++i) ITER(1, 1, acc2, 8);
    for (int i = 0; i < 2; ++i) ITER(1, 2, acc2, 0);
    if (grp == 0) BAR();
    STAMP(1, 2);

    const int kk = grp;
    const int tb = mblk * 128 + wm * 64 + 16 * (fr >> 3) + (fr & 7);
    f32x2 gg[4];
#pragma unroll
    for (int m = 0; m < 4; ++m) gg[m] = *(const f32x2*)(ws + WS_G + (size_t)(tb + 32 * (m & 1) + 8 * (m >> 1)) * 8);
#pragma unroll
    for (int n = 0; n < 4; ++n)
#pragma unroll
        for (int m = 0; m < 4; ++m) acc2[n][m] = acc2[n][m] * gg[m].y + acc1[n][m] * (2.f * gg[m].x);
    LAS f32x4* red = (LAS f32x4*)lds;
#pragma unroll
    for (int n = 0; n < 4; ++n)
#pragma unroll
        for (int i = 0; i < 2; ++i) red[(wid * 8 + n * 2 + i) * 64 + lane] = kk ? acc2[n][i] : acc2[n][2 + i];
    __syncthreads();
    f32x4 r[4][2];
#pragma unroll
    for (int n = 0; n < 4; ++n)
#pragma unroll
        for (int i = 0; i < 2; ++i) r[n][i] = (kk ? acc2[n][2 + i] : acc2[n][i]) + red[((wid ^ 4) * 8 + n * 2 + i) * 64 + lane];
    const int t0 = tb + 8 * kk;
    const int d0 = n0 + wn * 64 + 4 * fq;
    const __amdgpu_buffer_rsrc_t sP = __builtin_amdgcn_make_buffer_rsrc((void*)(ws + WS_PART + (size_t)tile * 65536), (short)0, 65536, 0x00020000);
    gu32* flag = (gu32*)(ws + WS_FLAG2 + tile * 128);
    STAMP(1, 3);
    if (!late) {
#pragma unroll
        for (int n = 0; n < 4; ++n) {
            const u32x2 dk = *(const u32x2*)(dskip + d0 + 16 * n);
#pragma unroll
            for (int i = 0; i < 2; ++i) {
                const u32x2 xv = *(const u32x2*)(x + (size_t)(t0 + 32 * i) * DM + d0 + 16 * n);
                const float g = kk ? gg[2 + i].x : gg[i].x;
                f32x4 v = r[n][i];
                v[0] += g * rbf(__uint_as_float(xv[0] << 16) * __uint_as_float(dk[0] << 16));
                v[1] += g * rbf(__uint_as_float(xv[0] & 0xffff0000u) * __uint_as_float(dk[0] & 0xffff0000u));
                v[2] += g * rbf(__uint_as_float(xv[1] << 16) * __uint_as_float(dk[1] << 16));
                v[3] += g * rbf(__uint_as_float(xv[1] & 0xffff0000u) * __uint_as_float(dk[1] & 0xffff0000u));
                __builtin_amdgcn_raw_buffer_store_b128(__builtin_bit_cast(u32x4, v), sP, ((wid * 8 + n * 2 + i) * 64 + lane) * 16, 0, 16);
            }
        }
        WAIT_V(0);
        __syncthreads();
        if (tid == 0) __hip_atomic_store(flag, 1u, RLX_AGENT);
    } else {
        if (wid == 0) {
            for (unsigned spins = 0; spins < (1u << 18); ++spins) {
                if (__builtin_amdgcn_readfirstlane(__hip_atomic_load(flag, RLX_AGENT)) == 1u) break;
                __builtin_amdgcn_s_sleep(1);
            }
        }
        __syncthreads();
        STAMP(1, 4);
        u32x4 pv[4][2];
#pragma unroll
        for (int n = 0; n < 4; ++n)
#pragma unroll
            for (int i = 0; i < 2; ++i) pv[n][i] = __builtin_amdgcn_raw_buffer_load_b128(sP, ((wid * 8 + n * 2 + i) * 64 + lane) * 16, 0, 16);
#pragma unroll
        for (int n = 0; n < 4; ++n)
#pragma unroll
            for (int i = 0; i < 2; ++i) *(f32x4*)(out + (size_t)(t0 + 32 * i) * DM + d0 + 16 * n) = r[n][i] + __builtin_bit_cast(f32x4, pv[n][i]);
    }
    STAMP(1, 6);
    STAMP(1, 7);
}

__global__ void k0_empty(char* p) { if (p == nullptr) p[0] = 1; }
extern "C" void kernel_launch(void* const* d_in, const int* in_sizes, int n_in, void* d_out, int out_size, void* d_ws, size_t ws_size,
                              hipStream_t stream) {
    const u16* x = (const u16*)d_in[0];
    const u16* lre = (const u16*)d_in[1];
    const u16* lim = (const u16*)d_in[2];
    const u16* Bre = (const u16*)d_in[3];
    const u16* Bim = (const u16*)d_in[4];
    const u16* Cre = (const u16*)d_in[5];
    const u16* Cim = (const u16*)d_in[6];
    const u16* logdt = (const u16*)d_in[7];
    const u16* dskip = (const u16*)d_in[8];
    const u16* Wg = (const u16*)d_in[10];
    const u16* Wu = (const u16*)d_in[11];
    const u16* Wd = (const u16*)d_in[12];
    const u16* gw = (const u16*)d_in[13];
    char* ws = (char*)d_ws;
    (void)hipFuncSetAttribute((const void*)k1_gemm_scan, hipFuncAttributeMaxDynamicSharedMemorySize, K1_LDS);
    (void)hipFuncSetAttribute((const void*)k2_gemm_out, hipFuncAttributeMaxDynamicSharedMemorySize, K2_LDS);
    (void)hipMemsetAsync(ws + WS_CARRY, 0xFF, WS_CARRY_BYTES, stream);
    k1_gemm_scan<<<256, 512, K1_LDS, stream>>>(x, Wg, Wu, Bre, Bim, lre, lim, logdt, gw, ws);
    k2_gemm_out<<<256, 512, K2_LDS, stream>>>(x, Cre, Cim, Wd, dskip, ws, (float*)d_out);
}
```

```cpp
#include <hip/hip_runtime.h>
#include <hip/hip_bf16.h>

typedef unsigned short u16;
typedef __bf16 bf16x8 __attribute__((ext_vector_type(8)));
typedef __bf16 bf16x2 __attribute__((ext_vector_type(2)));
typedef short s16x4 __attribute__((ext_vector_type(4)));
typedef short s16x8 __attribute__((ext_vector_type(8)));
typedef float f32x4 __attribute__((ext_vector_type(4)));
typedef float f32x2 __attribute__((ext_vector_type(2)));
typedef unsigned u32x4 __attribute__((ext_vector_type(4)));
typedef unsigned u32x2 __attribute__((ext_vector_type(2)));
#define LAS __attribute__((address_space(3)))
#define GAS __attribute__((address_space(1)))
typedef GAS unsigned gu32;
typedef GAS unsigned long long gu64;
#define RLX_AGENT __ATOMIC_RELAXED, __HIP_MEMORY_SCOPE_AGENT

constexpr int SEQ = 2048, DM = 1024, NP = 1024, FF = 3584;
constexpr int KSTEPS2 = (2 * NP + FF) / 64;

constexpr size_t WS_CARRY = 0;
constexpr size_t WS_G = 7 * 8192;
constexpr size_t WS_INIT_BYTES = 65536;
constexpr size_t WS_FLAG2 = 65536;
constexpr size_t WS_A2 = 1 << 20;
constexpr size_t WS_A2_BYTES = (size_t)KSTEPS2 * 16 * 16384;
constexpr size_t WS_PART = 32 << 20;

__device__ __forceinline__ float bflo(unsigned v) { return __uint_as_float(v << 16); }
__device__ __forceinline__ float bfhi(unsigned v) { return __uint_as_float(v & 0xffff0000u); }
__device__ __forceinline__ float rbf(float f) { return (float)(__bf16)f; }
__device__ __forceinline__ unsigned pk2(float a, float b) {
    bf16x2 v; v.x = (__bf16)a; v.y = (__bf16)b; return __builtin_bit_cast(unsigned, v);
}
__device__ __forceinline__ float relu(float v) { return __builtin_amdgcn_fmed3f(v, 0.f, __builtin_inff()); }
__device__ __forceinline__ float dot2(unsigned a, unsigned b, float c) {
    return __builtin_amdgcn_fdot2_f32_bf16(__builtin_bit_cast(bf16x2, a), __builtin_bit_cast(bf16x2, b), c, false);
}

#define STAMP(kid, i)
#define STAMP_TAKE(v)
#define STAMP_PUT(kid, i, v)

#define WAIT_V(n) asm volatile("s_waitcnt vmcnt(" #n ")" ::: "memory")
#define WAIT_L0() asm volatile("s_waitcnt lgkmcnt(0)" ::: "memory")
#define BAR() __builtin_amdgcn_s_barrier()
#define SCHED() __builtin_amdgcn_sched_barrier(0)
#define SRD(p, bytes) __builtin_amdgcn_make_buffer_rsrc((void*)(p), (short)0, (int)(bytes), 0x00020000)

__device__ __forceinline__ void dma16(unsigned m0v, unsigned voff, __amdgpu_buffer_rsrc_t srd, unsigned soff) {
    asm volatile("s_mov_b32 m0, %0\n\ts_nop 0\n\tbuffer_load_dwordx4 %1, %2, %3 offen lds" ::"s"(m0v), "v"(voff), "s"(srd), "s"(soff) : "m0", "memory");
}
__device__ __forceinline__ bf16x8 lds_b128(LAS char* p) { return __builtin_bit_cast(bf16x8, *(LAS s16x8*)p); }
__device__ __forceinline__ bf16x8 lds_tr(LAS char* p0, LAS char* p1) {
    s16x4 a = __builtin_amdgcn_ds_read_tr16_b64_v4i16((LAS s16x4*)p0);
    s16x4 b = __builtin_amdgcn_ds_read_tr16_b64_v4i16((LAS s16x4*)p1);
    return __builtin_bit_cast(bf16x8, __builtin_shufflevector(a, b, 0, 1, 2, 3, 4, 5, 6, 7));
}
#define MFMA(a, b, c) __builtin_amdgcn_mfma_f32_16x16x32_bf16(a, b, c, 0, 0, 0)
__device__ __forceinline__ f32x2 cmul(f32x2 a, f32x2 b) { return (f32x2){__builtin_fmaf(a.x, b.x, -(a.y * b.y)), __builtin_fmaf(a.x, b.y, a.y * b.x)}; }
__device__ __forceinline__ f32x2 cfma(f32x2 a, f32x2 b, f32x2 c) {
    return (f32x2){__builtin_fmaf(a.x, b.x, __builtin_fmaf(-a.y, b.y, c.x)), __builtin_fmaf(a.x, b.y, __builtin_fmaf(a.y, b.x, c.y))};
}
__device__ __forceinline__ f32x2 sel4(int i, f32x2 a, f32x2 b, f32x2 c, f32x2 d) {
    const bool o = i & 1, u = i & 2;
    const float lx = o ? b.x : a.x, ly = o ? b.y : a.y, hx = o ? d.x : c.x, hy = o ? d.y : c.y;
    return (f32x2){u ? hx : lx, u ? hy : ly};
}

#define STPOL 16
#define PF_G0 3
#define PF_U0 10
#define OUTPOL 16

constexpr int K1_A = 0;
constexpr int K1_S = 65536;
constexpr int K1_W = 81920;
constexpr int K1_WBUF = 28704, K1_UOFF = 14352;
constexpr int K1_WTAB = K1_W + 2 * K1_WBUF;
constexpr int K1_CTAB = K1_WTAB + 1024;
constexpr int K1_SINK = K1_CTAB + 512;
constexpr int K1_GTAB = K1_SINK + 1024;
constexpr int K1_LDS = K1_GTAB + 8192;

__global__ __launch_bounds__(512, 2) void k1_gemm_scan(const u16* __restrict__ x, const u16* __restrict__ Wg, const u16* __restrict__ Wu,
                                                       const u16* __restrict__ Bre, const u16* __restrict__ Bim, const u16* __restrict__ lre,
                                                       const u16* __restrict__ lim, const u16* __restrict__ logdt, const u16* __restrict__ gw,
                                                       const u16* __restrict__ Cre, const u16* __restrict__ Cim, const u16* __restrict__ Wd,
                                                       char* __restrict__ ws) {
    extern __shared__ __attribute__((aligned(16))) char smem[];
    LAS char* lds = (LAS char*)smem;
    const unsigned ldsb = (unsigned)(size_t)lds;
    const int tid = threadIdx.x, lane = tid & 63;
    const int wid = __builtin_amdgcn_readfirstlane(tid >> 6);
    const int wr = wid & 3, wc = wid >> 2;
    const int bid = blockIdx.x, idx = bid >> 3, j = (bid & 7) * 4 + (idx & 3), pm = idx >> 2;
    const int fr = lane & 15, fq = lane >> 4;
    STAMP_TAKE(st0);

    const int ch = 32 * j + 8 * wc + 4 * (fr >> 3) + 16 * ((fr >> 2) & 1) + (fr & 3);
    const int grow = pm * 256 + j * 8 + wid;
    u32x4 xa, xb, ga, gb; unsigned plr, pli, pld;
    {
        const u16* xr = x + (size_t)grow * DM;
        const unsigned l16 = lane * 16, c2 = ch * 2;
        asm volatile("s_nop 4\n\tglobal_load_dwordx4 %0, %7, %9\n\tglobal_load_dwordx4 %1, %7, %9 offset:1024\n\t"
                     "global_load_dwordx4 %2, %7, %10\n\tglobal_load_dwordx4 %3, %7, %10 offset:1024\n\t"
                     "global_load_ushort %4, %8, %11\n\tglobal_load_ushort %5, %8, %12\n\tglobal_load_ushort %6, %8, %13"
                     : "=&v"(xa), "=&v"(xb), "=&v"(ga), "=&v"(gb), "=&v"(plr), "=&v"(pli), "=&v"(pld)
                     : "v"(l16), "v"(c2), "s"(xr), "s"(gw), "s"(lre), "s"(lim), "s"(logdt) : "memory");
    }

    const bool pf = wid == 7;
    const __amdgpu_buffer_rsrc_t sX = SRD(x, SEQ * DM * 2);
    const __amdgpu_buffer_rsrc_t sG = SRD((pf && (bid & 1)) ? Wu : Wg, DM * FF * 2);
    const __amdgpu_buffer_rsrc_t sU = SRD(!pf ? Wu : (bid & 1) ? Wd : (bid & 2) ? Cim : Cre, (pf && !(bid & 1)) ? DM * NP * 2 : DM * FF * 2);
    const __amdgpu_buffer_rsrc_t sB = SRD(((wid >> 1) & 1) ? Bim : Bre, NP * DM * 2);

    const int l3 = lane >> 3;
    const unsigned voffX = (unsigned)(((l3 >> 2) * 16 + (l3 & 3)) * 2048 + (((lane & 7) ^ (l3 & 6)) * 16));
    unsigned voffW;
    { const int gam = wid * 64 + lane, rho = gam / 14, c = gam - rho * 14, k = (rho & 0x13) | ((rho & 4) << 1) | ((rho & 8) >> 1);
      voffW = pf ? (unsigned)(lane * 128) : (unsigned)(k * (FF * 2) + c * 16); }
    const unsigned wsh = pf ? (1u << 20) : (unsigned)(32 * FF * 2), wst = 2 * wsh;
    const unsigned ush = (pf && !(bid & 1)) ? (1u << 19) : wsh, ust = 2 * ush;
    const unsigned soX = (unsigned)((pm * 256 + (wid >> 1) * 64 + (wid & 1) * 8) * 2048);
    const unsigned soS = (unsigned)((32 * j + 8 * (wid >> 2) + 4 * (wid & 1)) * 2048);
    const unsigned soW = pf ? (unsigned)((bid >> 1) * 8192) - (unsigned)(PF_G0) * wsh : (unsigned)(j * 224);
    const unsigned soU = pf ? (unsigned)((bid & 1) ? (bid >> 1) * 8192 : (bid >> 2) * 8192) - (unsigned)(PF_U0) * ush : (unsigned)(j * 224);
    const unsigned mX = ldsb + K1_A + wid * 4096, mS = ldsb + K1_S + wid * 1024, mW = ldsb + (pf ? K1_SINK : K1_W + wid * 1024);
    const unsigned mWb = pf ? 0u : (unsigned)K1_WBUF, mWu = pf ? 0u : (unsigned)K1_UOFF, mWh = pf ? 0u : 7168u;

#define ST_X(ko, b) do { _Pragma("unroll") for (int i = 0; i < 4; ++i) dma16(mX + (b) * 32768 + i * 1024, voffX, sX, soX + (i & 1) * 65536 + (i >> 1) * 8192 + (ko)); } while (0)
#define ST_S(ko, b) dma16(mS + (b) * 8192, voffX, sB, soS + (ko))
#define ST_G(t, b) do { dma16(mW + (b) * mWb, voffW, sG, soW + (t) * wst); dma16(mW + (b) * mWb + mWh, voffW, sG, soW + (t) * wst + wsh); } while (0)
#define ST_U(t, b) do { dma16(mW + (b) * mWb + mWu, voffW, sU, soU + (t) * ust); dma16(mW + (b) * mWb + mWu + mWh, voffW, sU, soU + (t) * ust + ush); } while (0)

    ST_X(0, 0); ST_S(0, 0); ST_G(0, 0); ST_U(0, 0);
    ST_X(128, 1); ST_S(128, 1); ST_G(1, 1);

    gu32* gtab = (gu32*)(ws + WS_G);
    asm volatile("s_waitcnt vmcnt(16)" : "+v"(xa), "+v"(xb), "+v"(ga), "+v"(gb), "+v"(plr), "+v"(pli), "+v"(pld) :: "memory");
    {
        float s = 0.f, s2 = 0.f;
#pragma unroll
        for (int e = 0; e < 4; ++e) { s = dot2(xa[e], ga[e], s); s2 = dot2(xb[e], gb[e], s2); }
        s += s2;
#pragma unroll
        for (int o = 32; o > 0; o >>= 1) s += __shfl_xor(s, o);
        const float g = rbf(1.f / (1.f + expf(-rbf(s))));
        unsigned w = pk2(g, 1.f - g);
        if (w == 0xFFFFFFFFu) w = 0x7FC07FC0u;
        if (lane == 0) __hip_atomic_store(gtab + grow, w, RLX_AGENT);
        if (bid < 128 && tid == 64) *(unsigned*)(ws + WS_FLAG2 + bid * 128) = 0u;
    }
    if (wr == 0) {
        const float lr = bflo(plr), li = bflo(pli), dt = rbf(expf(bflo(pld)));
        const float e = expf(dt * lr); float sn, cs; sincosf(dt * li, &sn, &cs);
        const float ar = e * cs, ai = e * sn, nr = ar - 1.f, den = lr * lr + li * li;
        if (fq == 0) *(LAS f32x4*)(lds + K1_CTAB + (wc * 16 + fr) * 16) = (f32x4){ar, ai, (nr * lr + ai * li) / den, (ai * lr - nr * li) / den};
    }

    const int ch0 = (fq ^ (fr & 6)) * 16;
    LAS char* rA0 = lds + K1_A + wr * 8192 + fr * 128 + ch0;
    LAS char* rA1 = lds + K1_A + wr * 8192 + fr * 128 + (ch0 ^ 64);
    LAS char* rS0 = lds + K1_S + wc * 4096 + fr * 128 + ch0;
    LAS char* rS1 = lds + K1_S + wc * 4096 + fr * 128 + (ch0 ^ 64);
    const int trow = (16 * (fq >> 1) + 4 * (fq & 1) + ((lane & 15) >> 2)) * 224;
    LAS char* rW = lds + K1_W + trow + wc * 96 + (lane & 3) * 8;
    LAS char* rM = lds + K1_W + trow + 192 + wc * 16 + (lane & 1) * 8 + ((lane >> 1) & 1) * K1_UOFF;

    f32x4 acc[4][9];
#pragma unroll
    for (int m = 0; m < 4; ++m)
#pragma unroll
        for (int n = 0; n < 9; ++n) { acc[m][n] = (f32x4){0.f, 0.f, 0.f, 0.f}; asm volatile("" : "+v"(acc[m][n])); }
    bf16x8 Af[4][2], Bf[3][2];

#define RD_A(b) do { _Pragma("unroll") for (int m = 0; m < 4; ++m) { Af[m][0] = lds_b128(rA0 + (b) * 32768 + m * 2048); Af[m][1] = lds_b128(rA1 + (b) * 32768 + m * 2048); } } while (0)
#define RD_S(slot, ri, b) do { Bf[slot][0] = lds_b128(rS0 + (b) * 8192 + (ri) * 2048); Bf[slot][1] = lds_b128(rS1 + (b) * 8192 + (ri) * 2048); } while (0)
#define RD_W(slot, base, off) do { Bf[slot][0] = lds_tr((base) + (off), (base) + (off) + 1792); Bf[slot][1] = lds_tr((base) + (off) + 7168, (base) + (off) + 7168 + 1792); } while (0)
#define MMA3(n0, n1, n2) do { __builtin_amdgcn_s_setprio(1); \
        _Pragma("unroll") for (int kk = 0; kk < 2; ++kk) _Pragma("unroll") for (int m = 0; m < 4; ++m) { \
            acc[m][n0] = MFMA(Af[m][kk], Bf[0][kk], acc[m][n0]); acc[m][n1] = MFMA(Af[m][kk], Bf[1][kk], acc[m][n1]); acc[m][n2] = MFMA(Af[m][kk], Bf[2][kk], acc[m][n2]); } \
        __builtin_amdgcn_s_setprio(0); } while (0)
#define RD_P1(b) do { RD_S(0, 0, b); RD_S(1, 1, b); RD_W(2, rW, (b) * K1_WBUF); RD_A(b); } while (0)
#define RD_P2(b) do { RD_W(0, rW, (b) * K1_WBUF + 32); RD_W(1, rW, (b) * K1_WBUF + 64); RD_W(2, rM, (b) * K1_WBUF); } while (0)
#define RD_P3(b) do { RD_W(0, rW, (b) * K1_WBUF + K1_UOFF); RD_W(1, rW, (b) * K1_WBUF + K1_UOFF + 32); RD_W(2, rW, (b) * K1_WBUF + K1_UOFF + 64); } while (0)
#define GO(n0, n1, n2) do { WAIT_L0(); BAR(); SCHED(); MMA3(n0, n1, n2); SCHED(); } while (0)

#define RELANE() int tz = threadIdx.x; asm volatile("" : "+v"(tz)); const int er = tz & 15, eq = (tz >> 4) & 3; \
    const int ech = 32 * j + 8 * wc + 4 * (er >> 3) + 16 * ((er >> 2) & 1) + (er & 3); (void)ech; (void)eq
    gu64* carry = (gu64*)(ws + WS_CARRY);
    LAS f32x2* wtab = (LAS f32x2*)(lds + K1_WTAB);
    f32x2 Pq = {0.f, 0.f}, aq = {0.f, 0.f}, ap = {0.f, 0.f};

    WAIT_V(9); WAIT_L0();
    if (wc == 1) BAR();
    STAMP_TAKE(st1);
    for (int t = 0; t < 16; t += 2) {
        const bool last = t == 14;
        const unsigned ko = last ? 0x80000000u : (unsigned)(t + 2) * 128u;
        BAR(); RD_P1(0); ST_U(t + 1, 1); WAIT_V(9); GO(7, 8, 0);
        BAR(); RD_P2(0); ST_X(ko, 0); GO(1, 2, 6);
        BAR(); RD_P3(0); ST_S(ko, 0); ST_G(t + 2, 0);
        if (last) { const __amdgpu_buffer_rsrc_t sGt = SRD(ws + WS_G, 8192); RELANE(); dma16(ldsb + K1_GTAB + wid * 1024, (unsigned)((tz & 63) * 16), sGt, (unsigned)(pm * 1024)); }
        WAIT_V(9); GO(3, 4, 5);
        BAR(); RD_P1(1); ST_U(t + 2, 0); WAIT_V(9); GO(7, 8, 0);
        BAR(); RD_P2(1); ST_X(ko + 128, 1);
        if (last) {
            RELANE();
            const f32x2 a1 = *(LAS f32x2*)(lds + K1_CTAB + (wc * 16 + er) * 16);
            f32x2 h = {acc[0][7][0], acc[0][8][0]};
#pragma unroll
            for (int e = 1; e < 16; ++e) h = cfma(a1, h, (f32x2){acc[e >> 2][7][e & 3], acc[e >> 2][8][e & 3]});
            f32x2 a16 = cmul(a1, a1); a16 = cmul(a16, a16); a16 = cmul(a16, a16); a16 = cmul(a16, a16);
            const f32x2 a32 = cmul(a16, a16);
            f32x2 E[4];
#pragma unroll
            for (int f = 0; f < 4; ++f) E[f] = (f32x2){__shfl(h.x, er + 16 * f), __shfl(h.y, er + 16 * f)};
            const f32x2 P2 = cfma(a16, E[0], E[1]), P3 = cfma(a16, P2, E[2]), wag = cfma(a16, P3, E[3]);
            Pq = sel4(eq, (f32x2){0.f, 0.f}, E[0], P2, P3);
            aq = sel4(eq, (f32x2){1.f, 0.f}, a16, a32, cmul(a32, a16));
            ap = cmul(a32, a32);
            wtab[wr * 32 + wc * 16 + er] = wag;
        }
        GO(1, 2, 6);
        BAR(); RD_P3(1); ST_S(ko + 128, 1); ST_G(t + 3, 1);
        if (last) {
            RELANE();
            const f32x2 W0 = wtab[wc * 16 + er], W1 = wtab[32 + wc * 16 + er], W2 = wtab[64 + wc * 16 + er], W3 = wtab[96 + wc * 16 + er];
            const f32x2 Q2 = cfma(ap, W0, W1), Q3 = cfma(ap, Q2, W2), gag = cfma(ap, Q3, W3);
            if (wr == 0 && pm < 7 && eq == 0) {
                unsigned ux = __float_as_uint(gag.x), uy = __float_as_uint(gag.y);
                if (ux == 0xFFFFFFFFu) ux = 0x7FC00000u;
                __hip_atomic_store(carry + pm * 1024 + ech, ((unsigned long long)uy << 32) | ux, RLX_AGENT);
            }
            const f32x2 a128 = cmul(ap, ap);
            Pq = cfma(aq, sel4(wr, (f32x2){0.f, 0.f}, W0, Q2, Q3), Pq);
            aq = cmul(aq, sel4(wr, (f32x2){1.f, 0.f}, ap, a128, cmul(a128, ap)));
            ap = cmul(a128, a128);
        }
        WAIT_V(9); GO(3, 4, 5);
    }
    if (wc == 0) BAR();
    RELANE();
    STAMP_PUT(0, 0, st0); STAMP_PUT(0, 1, st1);
    STAMP(0, 2);

    unsigned long long gq[7];
#pragma unroll
    for (int q = 0; q < 7; ++q) gq[q] = __hip_atomic_load(carry + (q < pm ? q : 0) * 1024 + ech, RLX_AGENT);
    u32x4 gt[4];
    {
        bool ok = true;
#pragma unroll
        for (int m = 0; m < 4; ++m) {
            gt[m] = *(LAS u32x4*)(lds + K1_GTAB + wid * 1024 + wr * 256 + eq * 64 + m * 16);
#pragma unroll
            for (int q = 0; q < 4; ++q) ok &= gt[m][q] != 0xFFFFFFFFu;
        }
        if (!__all(ok)) {
            unsigned spins = 0;
            do {
                ok = true;
#pragma unroll
                for (int m = 0; m < 4; ++m)
#pragma unroll
                    for (int q = 0; q < 4; ++q) {
                        gt[m][q] = __hip_atomic_load(gtab + pm * 256 + wr * 64 + eq * 16 + m * 4 + q, RLX_AGENT);
                        ok &= gt[m][q] != 0xFFFFFFFFu;
                    }
            } while (!__all(ok) && ++spins < (1u << 18));
        }
    }
    STAMP(0, 3);
    const __amdgpu_buffer_rsrc_t sA2 = SRD(ws + WS_A2, WS_A2_BYTES);
#define ST16(voff, ...) __builtin_amdgcn_raw_buffer_store_b128((u32x4){__VA_ARGS__}, sA2, voff, 0, STPOL)
    const int mblk = pm * 2 + (wr >> 1);
    {
        const unsigned lo = (unsigned)(((er >> 2) & 1) * 2048 + (eq >> 1) * 256 + ((er >> 3) * 4 + (er & 3)) * 32 + (eq & 1) * 16);
#pragma unroll
        for (int n = 0; n < 3; ++n) {
            const int K16 = 128 + 7 * j + 3 * wc + n;
            const unsigned dst = ((K16 >> 2) * 16 + mblk) * 16384 + (K16 & 3) * 4096 + (wr & 1) * 1024 + lo;
#pragma unroll
            for (int hh = 0; hh < 2; ++hh) {
                float v[8];
#pragma unroll
                for (int e = 0; e < 8; ++e) { const int m = 2 * hh + (e >> 2), q = e & 3; v[e] = relu(acc[m][n][q]) * (acc[m][3 + n][q] * bfhi(gt[m][q])); }
                ST16(dst + hh * 512, pk2(v[0], v[1]), pk2(v[2], v[3]), pk2(v[4], v[5]), pk2(v[6], v[7]));
            }
        }
        const int K16 = 128 + 7 * j + 6;
        const unsigned lm = (unsigned)(((er >> 2) & 1) * 2048 + (er >> 3) * 512 + (eq >> 1) * 256 + (wc * 4 + (er & 3)) * 32 + (eq & 1) * 16);
        const unsigned dst = ((K16 >> 2) * 16 + mblk) * 16384 + (K16 & 3) * 4096 + (wr & 1) * 1024 + lm;
        float v[8];
        const bool lowh = er < 8;
#pragma unroll
        for (int e = 0; e < 8; ++e) {
            const int m = e >> 2, q = e & 3;
            const float v0 = lowh ? relu(acc[m][6][q]) : acc[m][6][q] * bfhi(gt[m][q]);
            const float v1 = lowh ? relu(acc[2 + m][6][q]) : acc[2 + m][6][q] * bfhi(gt[2 + m][q]);
            const float rcv = __int_as_float(__builtin_amdgcn_mov_dpp(__float_as_int(lowh ? v1 : v0), 0x128, 0xf, 0xf, true));
            v[e] = (lowh ? v0 : v1) * rcv;
        }
        ST16(dst, pk2(v[0], v[1]), pk2(v[2], v[3]), pk2(v[4], v[5]), pk2(v[6], v[7]));
    }
    STAMP(0, 4);
    f32x2 cg = {0.f, 0.f};
    if (pm > 0) {
        bool ok = true;
#pragma unroll
        for (int q = 0; q < 7; ++q) ok &= (unsigned)gq[q] != 0xFFFFFFFFu;
        if (!__all(ok)) {
            unsigned spins = 0;
            do {
                ok = true;
#pragma unroll
                for (int q = 0; q < 7; ++q) {
                    gq[q] = __hip_atomic_load(carry + (q < pm ? q : 0) * 1024 + ech, RLX_AGENT);
                    ok &= (unsigned)gq[q] != 0xFFFFFFFFu;
                }
            } while (!__all(ok) && ++spins < (1u << 18));
        }
#pragma unroll
        for (int q = 0; q < 7; ++q)
            if (q < pm) cg = cfma(ap, cg, (f32x2){__uint_as_float((unsigned)gq[q]), __uint_as_float((unsigned)(gq[q] >> 32))});
    }
    STAMP(0, 5);
    f32x2 h = cfma(aq, cg, Pq);
    const f32x4 cst = *(LAS f32x4*)(lds + K1_CTAB + (wc * 16 + er) * 16);
    const f32x2 a1 = {cst[0], cst[1]}, cf = {2.f * cst[2], 2.f * cst[3]};
    {
        const unsigned lo = (unsigned)(((er >> 2) & 1) * 4096 + (er >> 3) * 2048 + (eq >> 1) * 256 + (4 * wc + (er & 3)) * 32 + (eq & 1) * 16);
        const unsigned dst = ((j >> 1) * 32 + mblk) * 16384 + (j & 1) * 8192 + (wr & 1) * 1024 + lo;
#pragma unroll
        for (int hh = 0; hh < 2; ++hh) {
            float vr[8], vi[8];
#pragma unroll
            for (int e = 0; e < 8; ++e) {
                const int m = 2 * hh + (e >> 2), q = e & 3;
                h = cfma(a1, h, (f32x2){acc[m][7][q], acc[m][8][q]});
                const float g2 = bflo(gt[m][q]);
                const f32x2 hs = cmul(cf, h);
                vr[e] = hs.x * g2; vi[e] = -hs.y * g2;
            }
            ST16(dst + hh * 512, pk2(vr[0], vr[1]), pk2(vr[2], vr[3]), pk2(vr[4], vr[5]), pk2(vr[6], vr[7]));
            ST16(dst + 16 * 16384 + hh * 512, pk2(vi[0], vi[1]), pk2(vi[2], vi[3]), pk2(vi[4], vi[5]), pk2(vi[6], vi[7]));
        }
    }
    STAMP(0, 6);
    STAMP(0, 7);
}

#define K2_SPLIT 40
#define MAP 2
#define ABL_NOLOAD 0
#define ABL_NOREAD 0
#define ABL_NOMFMA 0
#define DIST 5
constexpr int K2_LDS = 5 * 32768;

__device__ __forceinline__ void mfma_acc(f32x4& c, bf16x8 a, bf16x8 b) {
    asm volatile("v_mfma_f32_16x16x32_bf16 %0, %1, %2, %0" : "+v"(c) : "v"(a), "v"(b) : "memory");
}
__device__ __forceinline__ s16x4 tr4(LAS char* p) { return __builtin_amdgcn_ds_read_tr16_b64_v4i16((LAS s16x4*)p); }
__device__ __forceinline__ bf16x8 cat8(s16x4 a, s16x4 b) { return __builtin_bit_cast(bf16x8, __builtin_shufflevector(a, b, 0, 1, 2, 3, 4, 5, 6, 7)); }

__global__ __launch_bounds__(512, 2) void k2_gemm_out(const u16* __restrict__ x, const u16* __restrict__ Cre, const u16* __restrict__ Cim,
                                                      const u16* __restrict__ Wd, const u16* __restrict__ dskip, char* __restrict__ ws,
                                                      float* __restrict__ out) {
    extern __shared__ __attribute__((aligned(16))) char smem[];
    LAS char* lds = (LAS char*)smem;
    const unsigned ldsb = (unsigned)(size_t)lds;
    const int tid = threadIdx.x, lane = tid & 63;
    const int wid = __builtin_amdgcn_readfirstlane(tid >> 6);
    const int bid = blockIdx.x, xc = bid & 7, idx = bid >> 3;
    const int late = idx & 1, mblk = (xc >> 1) * 4 + (idx >> 3), nblk = (xc & 1) * 4 + ((idx >> 1) & 3);
    const int tile = mblk * 8 + nblk, n0 = nblk * 128;
    const int ks0 = late ? K2_SPLIT : 0, ks1 = late ? KSTEPS2 : K2_SPLIT;
    STAMP_TAKE(st0);

    if (wid >= 4) {
        const int w4 = wid & 3;
        const __amdgpu_buffer_rsrc_t sA = SRD(ws + WS_A2, WS_A2_BYTES);
        const __amdgpu_buffer_rsrc_t sCr = SRD(Cre, DM * NP * 2), sCi = SRD(Cim, DM * NP * 2);
        const __amdgpu_buffer_rsrc_t sW = SRD(Wd, FF * DM * 2);
        const int l3 = lane >> 3;
        const unsigned voffA = (unsigned)(lane * 16);
        const unsigned voffC = (unsigned)(l3 * 2048 + (((lane & 7) ^ (l3 & 6)) * 16));
        const unsigned voffW = (unsigned)(((l3 >> 2) * 8 + (l3 & 3)) * 2048 + ((((lane >> 1) & 3) ^ (l3 >> 1)) * 32) + (lane & 1) * 16);
        const unsigned soA = (unsigned)(mblk * 16384 + w4 * 4096);
        const unsigned soC = (unsigned)((n0 + 32 * w4) * 2048);
        const unsigned soW = (unsigned)(w4 * 16 * 2048 + n0 * 2) - 32u * (64 * 2048);
        const unsigned mB = ldsb + w4 * 4096;
        int ks = ks0, sl = 0;
#define NEXT_SLOT() do { ++ks; sl = sl == 4 * 32768 ? 0 : sl + 32768; } while (0)
#define STAGE_C(srd) do { if (!ABL_NOLOAD) _Pragma("unroll") for (int i = 0; i < 4; ++i) { \
            dma16(mB + sl + i * 1024, voffA, sA, soA + ks * (16 * 16384) + i * 1024); \
            dma16(mB + sl + 16384 + i * 1024, voffC, srd, soC + (ks >> 1) * 128 + i * (8 * 2048)); } NEXT_SLOT(); } while (0)
#define STAGE_W() do { if (!ABL_NOLOAD) _Pragma("unroll") for (int i = 0; i < 4; ++i) { \
            dma16(mB + sl + i * 1024, voffA, sA, soA + ks * (16 * 16384) + i * 1024); \
            dma16(mB + sl + 16384 + i * 1024, voffW, sW, soW + ks * (64 * 2048) + (i >> 1) * (4 * 2048) + (i & 1) * 128); } NEXT_SLOT(); } while (0)
#define LSYNC() do { WAIT_V(24); BAR(); } while (0)
        asm volatile("s_nop 4");
        if (!late) {
            STAGE_C(sCr); STAGE_C(sCi);
            STAGE_C(sCr); STAGE_C(sCi); LSYNC();
            while (ks < 32) { STAGE_C(sCr); LSYNC(); STAGE_C(sCi); LSYNC(); }
        } else {
            STAGE_W(); STAGE_W();
            STAGE_W();
        }
        while (ks < ks1) { STAGE_W(); LSYNC(); }
        WAIT_V(16); BAR();
        WAIT_V(8); BAR();
        WAIT_V(0); BAR();
        BAR();
        if (!late) BAR();
        return;
    }

    const int wn = wid & 1, kk = wid >> 1;
    const int fr = lane & 15, fq = lane >> 4;
    LAS char* rHl = lds + kk * 8192 + (lane >> 5) * 4096 + (lane & 31) * 8 + kk * 1024;
    LAS char* rHh = lds + kk * 8192 + (lane >> 5) * 4096 + (lane & 31) * 8 + (kk ^ 1) * 1024;
    LAS char* rC = lds + 16384 + (wn * 64 + fr) * 128 + (((4 * kk + fq) ^ (fr & 6)) * 16);
    LAS char* rWb = lds + 16384 + kk * 8192 + (lane >> 5) * 4096 + wn * 1024 + ((lane >> 2) & 7) * 128 + (lane & 3) * 8;
    const int rsw = (lane >> 3) & 3;
    const int t0 = mblk * 128 + kk * 64 + 16 * (fr >> 3) + (fr & 7);
    const int d0 = n0 + wn * 64 + 4 * fq;

    f32x4 acc[4][8];
#pragma unroll
    for (int n = 0; n < 4; ++n)
#pragma unroll
        for (int m = 0; m < 8; ++m) acc[n][m] = (f32x4){0.f, 0.f, 0.f, 0.f};
    if (!late) {
        u32x2 dk[4];
#pragma unroll
        for (int n = 0; n < 4; ++n) dk[n] = *(const u32x2*)(dskip + d0 + 16 * n);
#pragma unroll
        for (int m = 0; m < 4; ++m) {
            const int t = t0 + 32 * (m & 1) + 8 * (m >> 1);
            const float g = bflo(*(const unsigned*)(ws + WS_G + (size_t)t * 4));
#pragma unroll
            for (int n = 0; n < 4; ++n) {
                const u32x2 xv = *(const u32x2*)(x + (size_t)t * DM + d0 + 16 * n);
                acc[n][m] = (f32x4){g * rbf(bflo(xv[0]) * bflo(dk[n][0])), g * rbf(bfhi(xv[0]) * bfhi(dk[n][0])),
                                    g * rbf(bflo(xv[1]) * bflo(dk[n][1])), g * rbf(bfhi(xv[1]) * bfhi(dk[n][1]))};
            }
        }
    }
    s16x4 HX[8][2], HY[8][2], WX[4][2], WY[4][2];
    int sl = 0;

#define RD1(RD, i, H, W) do { \
        if ((i) < 8) { \
            if (RD == 1) W[(i) >> 1][(i) & 1] = tr4(rWb + sl + ((((i) >> 1) ^ rsw) * 32) + ((i) & 1) * 2048); \
            else if (((i) & 1) == 0) { const s16x8 v_ = *(LAS s16x8*)(rC + sl + ((i) >> 1) * 2048); \
                W[(i) >> 1][0] = __builtin_shufflevector(v_, v_, 0, 1, 2, 3); W[(i) >> 1][1] = __builtin_shufflevector(v_, v_, 4, 5, 6, 7); } \
        } else H[((i) - 8) >> 1][(i) & 1] = tr4(((i) < 16 ? rHl : rHh) + sl + ((((i) - 8) >> 1) & 3) * 256 + ((i) & 1) * 2048); \
    } while (0)
#define LGKM0() __builtin_amdgcn_s_waitcnt(0xC07F)
#define STEP(RD, HC, WC, HN, WN) do { \
        _Pragma("unroll") for (int i = 0; i < 32; ++i) { \
            if (!ABL_NOMFMA) mfma_acc(acc[i & 3][i >> 2], cat8(WC[i & 3][0], WC[i & 3][1]), cat8(HC[i >> 2][0], HC[i >> 2][1])); \
            else asm volatile("" :: "v"(cat8(WC[i & 3][0], WC[i & 3][1])), "v"(cat8(HC[i >> 2][0], HC[i >> 2][1]))); \
            if (!ABL_NOREAD && RD < 2 && i < 24) RD1(RD, i, HN, WN); \
        } \
        if (RD < 2) { sl = sl == 4 * 32768 ? 0 : sl + 32768; LGKM0(); } \
    } while (0)

    BAR();
    STAMP_TAKE(st1);
    if (!late) {
#pragma unroll
        for (int i = 0; i < 24; ++i) RD1(0, i, HX, WX);
        sl = 32768; LGKM0();
        for (int i = 0; i < 15; ++i) { BAR(); STEP(0, HX, WX, HY, WY); BAR(); STEP(0, HY, WY, HX, WX); }
        BAR(); STEP(0, HX, WX, HY, WY); BAR(); STEP(1, HY, WY, HX, WX);
    } else {
#pragma unroll
        for (int i = 0; i < 24; ++i) RD1(1, i, HX, WX);
        sl = 32768; LGKM0();
    }
    const int nf = (late ? KSTEPS2 - K2_SPLIT : K2_SPLIT - 32) / 2 - 1;
    for (int i = 0; i < nf; ++i) { BAR(); STEP(1, HX, WX, HY, WY); BAR(); STEP(1, HY, WY, HX, WX); }

    const __amdgpu_buffer_rsrc_t sP = SRD(ws + WS_PART + (size_t)tile * 65536, 65536);
    gu32* flag = (gu32*)(ws + WS_FLAG2 + tile * 128);
    unsigned fl = 0;
    if (late) fl = __hip_atomic_load(flag, RLX_AGENT);
    BAR(); STEP(1, HX, WX, HY, WY);
    STAMP_TAKE(st4);
    f32x4 ex[3][4];
    if (late) {
        for (unsigned spins = 0; __builtin_amdgcn_readfirstlane(fl) != 1u && spins < (1u << 18); ++spins) {
            __builtin_amdgcn_s_sleep(1);
            fl = __hip_atomic_load(flag, RLX_AGENT);
        }
        __builtin_amdgcn_fence(__ATOMIC_ACQUIRE, "wavefront");
#pragma unroll
        for (int n = 0; n < 3; ++n)
#pragma unroll
            for (int m = 0; m < 4; ++m) ex[n][m] = __builtin_bit_cast(f32x4, __builtin_amdgcn_raw_buffer_load_b128(sP, ((wid * 16 + n * 4 + m) * 64 + lane) * 16, 0, 16));
    }
    STEP(2, HY, WY, HX, WX);
    asm volatile("s_nop 15" ::: "memory");
    STAMP_PUT(1, 0, st0); STAMP_PUT(1, 1, st1); STAMP_PUT(1, 4, st4);
    STAMP(1, 2);

    f32x4 ex3[4];
    if (late) {
#pragma unroll
        for (int m = 0; m < 4; ++m) ex3[m] = __builtin_bit_cast(f32x4, __builtin_amdgcn_raw_buffer_load_b128(sP, ((wid * 16 + 12 + m) * 64 + lane) * 16, 0, 16));
    }
    LAS f32x4* red = (LAS f32x4*)(lds + (sl >= 4 * 32768 ? 0 : sl));
#pragma unroll
    for (int n = 0; n < 4; ++n)
#pragma unroll
        for (int m = 0; m < 4; ++m) red[(wid * 16 + n * 4 + m) * 64 + lane] = acc[n][4 + m];
    WAIT_L0(); BAR(); asm volatile("" ::: "memory");
#pragma unroll
    for (int n = 0; n < 4; ++n)
#pragma unroll
        for (int m = 0; m < 4; ++m) acc[n][m] += red[((wid ^ 2) * 16 + n * 4 + m) * 64 + lane];
    STAMP(1, 3);
    if (!late) {
#pragma unroll
        for (int n = 0; n < 4; ++n)
#pragma unroll
            for (int m = 0; m < 4; ++m) __builtin_amdgcn_raw_buffer_store_b128(__builtin_bit_cast(u32x4, acc[n][m]), sP, ((wid * 16 + n * 4 + m) * 64 + lane) * 16, 0, 16);
        WAIT_V(0);
        BAR();
        if (tid == 0) __hip_atomic_store(flag, 1u, RLX_AGENT);
    } else {
        const __amdgpu_buffer_rsrc_t sO = SRD(out, SEQ * DM * 4);
#pragma unroll
        for (int n = 0; n < 4; ++n)
#pragma unroll
            for (int m = 0; m < 4; ++m) {
                const f32x4 r = acc[n][m] + (n < 3 ? ex[n < 3 ? n : 0][m] : ex3[m]);
                __builtin_amdgcn_raw_buffer_store_b128(__builtin_bit_cast(u32x4, r), sO, ((t0 + 32 * (m & 1) + 8 * (m >> 1)) * DM + d0 + 16 * n) * 4, 0, OUTPOL);
            }
    }
    STAMP(1, 6);
    STAMP(1, 7);
}

extern "C" void kernel_launch(void* const* d_in, const int* in_sizes, int n_in, void* d_out, int out_size, void* d_ws, size_t ws_size,
                              hipStream_t stream) {
    const u16* x = (const u16*)d_in[0];
    const u16* lre = (const u16*)d_in[1];
    const u16* lim = (const u16*)d_in[2];
    const u16* Bre = (const u16*)d_in[3];
    const u16* Bim = (const u16*)d_in[4];
    const u16* Cre = (const u16*)d_in[5];
    const u16* Cim = (const u16*)d_in[6];
    const u16* logdt = (const u16*)d_in[7];
    const u16* dskip = (const u16*)d_in[8];
    const u16* Wg = (const u16*)d_in[10];
    const u16* Wu = (const u16*)d_in[11];
    const u16* Wd = (const u16*)d_in[12];
    const u16* gw = (const u16*)d_in[13];
    char* ws = (char*)d_ws;
    (void)hipFuncSetAttribute((const void*)k1_gemm_scan, hipFuncAttributeMaxDynamicSharedMemorySize, K1_LDS);
    (void)hipFuncSetAttribute((const void*)k2_gemm_out, hipFuncAttributeMaxDynamicSharedMemorySize, K2_LDS);
    (void)hipMemsetAsync(ws, 0xFF, WS_INIT_BYTES, stream);
    k1_gemm_scan<<<256, 512, K1_LDS, stream>>>(x, Wg, Wu, Bre, Bim, lre, lim, logdt, gw, Cre, Cim, Wd, ws);
    k2_gemm_out<<<256, 512, K2_LDS, stream>>>(x, Cre, Cim, Wd, dskip, ws, (float*)d_out);
}
```

```cpp
#include <hip/hip_runtime.h>
#include <hip/hip_bf16.h>

typedef unsigned short u16;
typedef __bf16 bf16x8 __attribute__((ext_vector_type(8)));
typedef __bf16 bf16x2 __attribute__((ext_vector_type(2)));
typedef short s16x4 __attribute__((ext_vector_type(4)));
typedef short s16x8 __attribute__((ext_vector_type(8)));
typedef float f32x4 __attribute__((ext_vector_type(4)));
typedef float f32x2 __attribute__((ext_vector_type(2)));
typedef unsigned u32x4 __attribute__((ext_vector_type(4)));
typedef unsigned u32x2 __attribute__((ext_vector_type(2)));
#define LAS __attribute__((address_space(3)))
#define GAS __attribute__((address_space(1)))
typedef GAS unsigned gu32;
typedef GAS unsigned long long gu64;
#define RLX_AGENT __ATOMIC_RELAXED, __HIP_MEMORY_SCOPE_AGENT

constexpr int SEQ = 2048, DM = 1024, NP = 1024, FF = 3584;
constexpr int KSTEPS2 = (2 * NP + FF) / 64;

constexpr size_t WS_CARRY = 0;
constexpr size_t WS_G = 7 * 8192;
constexpr size_t WS_RDY = 65536;
constexpr size_t WS_FLAG2 = 65536 + 1024;
constexpr size_t WS_INIT_BYTES = 65536 + 1024 + 16384;
constexpr size_t WS_A2 = 1 << 20;
constexpr unsigned A2S = 262144 + 128;
constexpr size_t WS_A2_BYTES = (size_t)(KSTEPS2 + 32) * A2S;

__device__ __forceinline__ float bflo(unsigned v) { return __uint_as_float(v << 16); }
__device__ __forceinline__ float bfhi(unsigned v) { return __uint_as_float(v & 0xffff0000u); }
__device__ __forceinline__ float rbf(float f) { return (float)(__bf16)f; }
__device__ __forceinline__ unsigned pk2(float a, float b) {
    bf16x2 v; v.x = (__bf16)a; v.y = (__bf16)b; return __builtin_bit_cast(unsigned, v);
}
__device__ __forceinline__ float relu(float v) { return __builtin_amdgcn_fmed3f(v, 0.f, __builtin_inff()); }
__device__ __forceinline__ float dot2(unsigned a, unsigned b, float c) {
    return __builtin_amdgcn_fdot2_f32_bf16(__builtin_bit_cast(bf16x2, a), __builtin_bit_cast(bf16x2, b), c, false);
}

constexpr size_t WS_DBG = 200u << 20;
#define STAMP(kid, i)
#define STAMP_TAKE(v)
#define STAMP_PUT(kid, i, v)

#define FSTAMP(i)
#define FDUMP()
#define WAIT_V(n) asm volatile("s_waitcnt vmcnt(" #n ")" ::: "memory")
#define WAIT_L0() asm volatile("s_waitcnt lgkmcnt(0)" ::: "memory")
#define BAR() __builtin_amdgcn_s_barrier()
#define SCHED() __builtin_amdgcn_sched_barrier(0)
#define SRD(p, bytes) __builtin_amdgcn_make_buffer_rsrc((void*)(p), (short)0, (int)(bytes), 0x00020000)

__device__ __forceinline__ void dma16(unsigned m0v, unsigned voff, __amdgpu_buffer_rsrc_t srd, unsigned soff) {
    asm volatile("s_mov_b32 m0, %0\n\ts_nop 0\n\tbuffer_load_dwordx4 %1, %2, %3 offen lds" ::"s"(m0v), "v"(voff), "s"(srd), "s"(soff) : "m0", "memory");
}
__device__ __forceinline__ bf16x8 lds_b128(LAS char* p) { return __builtin_bit_cast(bf16x8, *(LAS s16x8*)p); }
__device__ __forceinline__ bf16x8 lds_tr(LAS char* p0, LAS char* p1) {
    s16x4 a = __builtin_amdgcn_ds_read_tr16_b64_v4i16((LAS s16x4*)p0);
    s16x4 b = __builtin_amdgcn_ds_read_tr16_b64_v4i16((LAS s16x4*)p1);
    return __builtin_bit_cast(bf16x8, __builtin_shufflevector(a, b, 0, 1, 2, 3, 4, 5, 6, 7));
}
#define MFMA(a, b, c) __builtin_amdgcn_mfma_f32_16x16x32_bf16(a, b, c, 0, 0, 0)
__device__ __forceinline__ f32x2 cmul(f32x2 a, f32x2 b) { return (f32x2){__builtin_fmaf(a.x, b.x, -(a.y * b.y)), __builtin_fmaf(a.x, b.y, a.y * b.x)}; }
__device__ __forceinline__ f32x2 cfma(f32x2 a, f32x2 b, f32x2 c) {
    return (f32x2){__builtin_fmaf(a.x, b.x, __builtin_fmaf(-a.y, b.y, c.x)), __builtin_fmaf(a.x, b.y, __builtin_fmaf(a.y, b.x, c.y))};
}
__device__ __forceinline__ f32x2 sel4(int i, f32x2 a, f32x2 b, f32x2 c, f32x2 d) {
    const bool o = i & 1, u = i & 2;
    const float lx = o ? b.x : a.x, ly = o ? b.y : a.y, hx = o ? d.x : c.x, hy = o ? d.y : c.y;
    return (f32x2){u ? hx : lx, u ? hy : ly};
}

#define STPOL 16
#define PF_W0 1
#define PF_D0 4
#define OUTPOL 16

#define K2_SPLIT 40
#define MAP 2
#define ABL_NOLOAD 0
#define ABL_NOREAD 0
#define ABL_NOMFMA 0
#define CODE_T 10
#define LATE_FLIP 0
#define DIST 5
constexpr int K2_LDS = 5 * 32768;

__device__ __forceinline__ void dma16c(unsigned m0v, unsigned voff, __amdgpu_buffer_rsrc_t srd, unsigned soff) {
    asm volatile("s_mov_b32 m0, %0\n\ts_nop 0\n\tbuffer_load_dwordx4 %1, %2, %3 offen sc1 lds" ::"s"(m0v), "v"(voff), "s"(srd), "s"(soff) : "m0", "memory");
}
__device__ __forceinline__ void mfma_acc(f32x4& c, bf16x8 a, bf16x8 b) {
    asm volatile("v_mfma_f32_16x16x32_bf16 %0, %1, %2, %0" : "+v"(c) : "v"(a), "v"(b) : "memory");
}
__device__ __forceinline__ s16x4 tr4(LAS char* p) { return __builtin_amdgcn_ds_read_tr16_b64_v4i16((LAS s16x4*)p); }
__device__ __forceinline__ bf16x8 cat8(s16x4 a, s16x4 b) { return __builtin_bit_cast(bf16x8, __builtin_shufflevector(a, b, 0, 1, 2, 3, 4, 5, 6, 7)); }

constexpr int K1_ASB = 40960;
constexpr int K1_W = 81920;
constexpr int K1_WBUF = 28704, K1_UOFF = 14352;
constexpr int K1_ETAB = K1_W + 2 * K1_WBUF;
constexpr int K1_CTAB = K1_ETAB + 32 * 144;
constexpr int K1_SINK = K1_CTAB + 32 * 96;
constexpr int K1_GTAB = K1_SINK + 1024;

__global__ __launch_bounds__(512, 2) void k1_fused(const u16* __restrict__ x, const u16* __restrict__ Wg, const u16* __restrict__ Wu,
                                                       const u16* __restrict__ Bre, const u16* __restrict__ Bim, const u16* __restrict__ lre,
                                                       const u16* __restrict__ lim, const u16* __restrict__ logdt, const u16* __restrict__ gw,
                                                       const u16* __restrict__ Cre, const u16* __restrict__ Cim, const u16* __restrict__ Wd,
                                                       const u16* __restrict__ dskip, char* __restrict__ ws, float* __restrict__ out) {
    extern __shared__ __attribute__((aligned(16))) char smem[];
    LAS char* lds = (LAS char*)smem;
    const unsigned ldsb = (unsigned)(size_t)lds;
    const int tid = threadIdx.x, lane = tid & 63;
    const int wid = __builtin_amdgcn_readfirstlane(tid >> 6);
    const int wr = wid & 3, wc = wid >> 2;
    const int bid = blockIdx.x, idx = bid >> 3, j = (bid & 7) * 4 + (idx & 3), pm = idx >> 2;
    const int fr = lane & 15, fq = lane >> 4;
    STAMP_TAKE(st0);
    FSTAMP(0);

    const char* code0; unsigned codeA, codeB;
    asm volatile("s_getpc_b64 %0\n.Lk1_ref:\n\ts_mov_b32 %1, .Lk1_end_a-.Lk1_ref\n\ts_mov_b32 %2, .Lk1_end_b-.Lk1_ref" : "=s"(code0), "=s"(codeA), "=s"(codeB));
    const unsigned codeN = codeA > codeB ? codeA : codeB;
    if (wid == 7) {
        const __amdgpu_buffer_rsrc_t sI = SRD(code0, codeN);
        dma16(ldsb + K1_SINK, (unsigned)(lane * 128), sI, 0u); dma16(ldsb + K1_SINK, (unsigned)(lane * 128), sI, 8192u);
    }

    const int ch = 32 * j + 8 * (fq & 1) + 4 * (fr >> 3) + 16 * ((fr >> 2) & 1) + (fr & 3);
    const int grow = pm * 256 + j * 8 + wid;
    u32x4 xa, xb, ga, gb; unsigned plr, pli, pld;
    {
        const u16* xr = x + (size_t)grow * DM;
        const unsigned l16 = lane * 16, c2 = ch * 2;
        asm volatile("s_nop 4\n\tglobal_load_dwordx4 %0, %7, %9\n\tglobal_load_dwordx4 %1, %7, %9 offset:1024\n\t"
                     "global_load_dwordx4 %2, %7, %10\n\tglobal_load_dwordx4 %3, %7, %10 offset:1024\n\t"
                     "global_load_ushort %4, %8, %11\n\tglobal_load_ushort %5, %8, %12\n\tglobal_load_ushort %6, %8, %13"
                     : "=&v"(xa), "=&v"(xb), "=&v"(ga), "=&v"(gb), "=&v"(plr), "=&v"(pli), "=&v"(pld)
                     : "v"(l16), "v"(c2), "s"(xr), "s"(gw), "s"(lre), "s"(lim), "s"(logdt) : "memory");
    }

    const bool pf = wid == 7;
    const int q4 = wid + 28, q5 = wid + 35;
    const __amdgpu_buffer_rsrc_t sX = SRD(!pf || !(bid & 32) ? x : (bid & 16) ? Bim : Bre, !pf || !(bid & 32) ? SEQ * DM * 2 : NP * DM * 2);
    const int pb = (bid >> 3) | ((bid & 7) << 5);
    const __amdgpu_buffer_rsrc_t sG = SRD(!pf ? Wg : (pb & 1) ? Wd : (pb & 2) ? Cim : Cre, (pf && !(pb & 1)) ? DM * NP * 2 : DM * FF * 2);
    const __amdgpu_buffer_rsrc_t sU = SRD((pf && (pb & 1)) ? Wg : Wu, DM * FF * 2);
    const __amdgpu_buffer_rsrc_t s4 = SRD(q4 < 32 ? x : (q4 & 2) ? Bim : Bre, q4 < 32 ? SEQ * DM * 2 : NP * DM * 2);
    const __amdgpu_buffer_rsrc_t s5 = SRD((q5 & 2) ? Bim : Bre, q5 < 40 ? NP * DM * 2 : 0);

    const int l3 = lane >> 3;
    const unsigned voffX = pf ? (unsigned)(lane * 2048) : (unsigned)(((l3 >> 2) * 16 + (l3 & 3)) * 2048 + (((lane & 7) ^ (l3 & 6)) * 16));
    unsigned voffW;
    { const int gam = wid * 64 + lane, rho = gam / 14, c = gam - rho * 14, k = (rho & 0x13) | ((rho & 4) << 1) | ((rho & 8) >> 1);
      voffW = pf ? (unsigned)(lane * 128) : (unsigned)(k * (FF * 2) + c * 16); }
#define XROW(q) (pm * 256 + ((q) >> 3) * 64 + (((q) >> 2) & 1) * 8 + ((q) & 1) * 32 + (((q) >> 1) & 1) * 4)
#define BROW(q) (32 * j + 8 * (((q) >> 2) & 1) + 4 * ((q) & 1))
    const unsigned so0 = (unsigned)(XROW(wid) * 2048), so1 = (unsigned)(XROW(wid + 7) * 2048), so2 = (unsigned)(XROW(wid + 14) * 2048),
                   so3 = (unsigned)(XROW(wid + 21) * 2048), so4 = (unsigned)((q4 < 32 ? XROW(q4) : BROW(q4)) * 2048), so5 = (unsigned)(BROW(q5) * 2048);
    const unsigned soW = (unsigned)(j * 224);
    const unsigned mX = ldsb + wid * 1024, mW = ldsb + K1_W + wid * 1024, mSink = ldsb + K1_SINK;
    const unsigned m50 = q5 < 40 ? mX + 35 * 1024 : mSink, m51 = q5 < 40 ? mX + 35 * 1024 + K1_ASB : mSink;
    constexpr unsigned WST = 64 * FF * 2, WSH = 32 * FF * 2;

    const unsigned mXa = pf ? mSink : mX, mXb = pf ? mSink : mX + K1_ASB;
    const unsigned mGa = pf ? mSink : mW, mGb = pf ? mSink : mW + K1_WBUF, mUa = pf ? mSink : mW + K1_UOFF, mUb = pf ? mSink : mW + K1_WBUF + K1_UOFF;
    const unsigned sx0 = pf ? (unsigned)((bid & ((bid & 32) ? 15 : 31)) * 131072 + (bid >> 6) * 128) : so0;
    const unsigned ust = pf ? (1u << 20) : WST, gst = !pf ? WST : (pb & 1) ? (1u << 20) : (1u << 19);
    const unsigned su0 = pf ? (unsigned)((pb >> 1) * 8192) + (unsigned)(PF_W0) * ust : soW;
    const unsigned sg0 = pf ? (unsigned)(((pb & 1) ? pb >> 1 : pb >> 2) * 8192) - (unsigned)(PF_D0 + 2) * gst : soW;
#define ST_X1(k0, b) dma16((b) ? mXb : mXa, voffX, sX, sx0 + (k0))
#define ST_X3(ko, b) do { dma16(mX + (b) * K1_ASB + 7 * 1024, voffX, sX, so1 + (ko)); \
        dma16(mX + (b) * K1_ASB + 14 * 1024, voffX, sX, so2 + (ko)); dma16(mX + (b) * K1_ASB + 21 * 1024, voffX, sX, so3 + (ko)); } while (0)
#define ST_S(ko, b) do { dma16(mX + (b) * K1_ASB + 28 * 1024, voffX, s4, so4 + (ko)); dma16((b) ? m51 : m50, voffX, s5, so5 + (ko)); } while (0)
#define ST_G1(t, b) dma16((b) ? mGb : mGa, voffW, sG, sg0 + (t) * gst)
#define ST_G2(t, b) dma16(mW + (b) * K1_WBUF + 7168, voffW, sG, soW + (t) * WST + WSH)
#define ST_U1(t, b) dma16((b) ? mUb : mUa, voffW, sU, su0 + (t) * ust)
#define ST_U2(t, b) dma16(mW + (b) * K1_WBUF + K1_UOFF + 7168, voffW, sU, soW + (t) * WST + WSH)

    ST_X1(0, 0);
    if (!pf) { ST_X3(0, 0); ST_S(0, 0); ST_G1(0, 0); ST_G2(0, 0); ST_U2(0, 0); }
    ST_U1(0, 0);
    FSTAMP(1);

    const int ch0 = (fq ^ (fr & 6)) * 16;
    LAS char* rA0 = lds + wr * 8192 + fr * 128 + ch0;
    LAS char* rA1 = lds + wr * 8192 + fr * 128 + (ch0 ^ 64);
    LAS char* rS0 = lds + 32768 + wc * 4096 + fr * 128 + ch0;
    LAS char* rS1 = lds + 32768 + wc * 4096 + fr * 128 + (ch0 ^ 64);
    const int trow = (16 * (fq >> 1) + 4 * (fq & 1) + ((lane & 15) >> 2)) * 224;
    LAS char* rW = lds + K1_W + trow + wc * 96 + (lane & 3) * 8;
    LAS char* rM = lds + K1_W + trow + 192 + wc * 16 + (lane & 1) * 8 + ((lane >> 1) & 1) * K1_UOFF;

    f32x4 acc[4][9];
#pragma unroll
    for (int m = 0; m < 4; ++m)
#pragma unroll
        for (int n = 0; n < 9; ++n) { acc[m][n] = (f32x4){0.f, 0.f, 0.f, 0.f}; asm volatile("" : "+v"(acc[m][n])); }
    bf16x8 Af[4][2], Bf[3][2];

#define RD_A(b) do { _Pragma("unroll") for (int m = 0; m < 4; ++m) { Af[m][0] = lds_b128(rA0 + (b) * K1_ASB + m * 2048); Af[m][1] = lds_b128(rA1 + (b) * K1_ASB + m * 2048); } } while (0)
#define RD_S(slot, ri, b) do { Bf[slot][0] = lds_b128(rS0 + (b) * K1_ASB + (ri) * 2048); Bf[slot][1] = lds_b128(rS1 + (b) * K1_ASB + (ri) * 2048); } while (0)
#define RD_W(slot, base, off) do { Bf[slot][0] = lds_tr((base) + (off), (base) + (off) + 1792); Bf[slot][1] = lds_tr((base) + (off) + 7168, (base) + (off) + 7168 + 1792); } while (0)
#define MMA3(n0, n1, n2) do { __builtin_amdgcn_s_setprio(1); \
        _Pragma("unroll") for (int kk = 0; kk < 2; ++kk) _Pragma("unroll") for (int m = 0; m < 4; ++m) { \
            acc[m][n0] = MFMA(Af[m][kk], Bf[0][kk], acc[m][n0]); acc[m][n1] = MFMA(Af[m][kk], Bf[1][kk], acc[m][n1]); acc[m][n2] = MFMA(Af[m][kk], Bf[2][kk], acc[m][n2]); } \
        __builtin_amdgcn_s_setprio(0); } while (0)
#define RD_P1(b) do { RD_S(0, 0, b); RD_S(1, 1, b); RD_W(2, rW, (b) * K1_WBUF); RD_A(b); } while (0)
#define RD_P2(b) do { RD_W(0, rW, (b) * K1_WBUF + 32); RD_W(1, rW, (b) * K1_WBUF + 64); RD_W(2, rM, (b) * K1_WBUF); } while (0)
#define RD_P3(b) do { RD_W(0, rW, (b) * K1_WBUF + K1_UOFF); RD_W(1, rW, (b) * K1_WBUF + K1_UOFF + 32); RD_W(2, rW, (b) * K1_WBUF + K1_UOFF + 64); } while (0)
#define GO(n0, n1, n2) do { WAIT_L0(); BAR(); SCHED(); MMA3(n0, n1, n2); SCHED(); } while (0)

#define RELANE() int tz = threadIdx.x; asm volatile("" : "+v"(tz)); const int er = tz & 15, eq = (tz >> 4) & 3; \
    const int ech = 32 * j + 8 * wc + 4 * (er >> 3) + 16 * ((er >> 2) & 1) + (er & 3); (void)ech; (void)eq
    asm volatile("" : "+v"(rA0), "+v"(rA1), "+v"(rS0), "+v"(rS1), "+v"(rW), "+v"(rM));
    gu32* gtab = (gu32*)(ws + WS_G);
    WAIT_V(10);
    if (pf) WAIT_V(2);
    asm volatile("" : "+v"(xa), "+v"(xb), "+v"(ga), "+v"(gb), "+v"(plr), "+v"(pli), "+v"(pld) :: "memory");
    FSTAMP(2);
    {
        float s = 0.f, s2 = 0.f;
#pragma unroll
        for (int e = 0; e < 4; ++e) { s = dot2(xa[e], ga[e], s); s2 = dot2(xb[e], gb[e], s2); }
        s += s2;
#define DPP_ADD(ctrl, rmask) s += __int_as_float(__builtin_amdgcn_update_dpp(0, __float_as_int(s), ctrl, rmask, 0xf, true))
        DPP_ADD(0x111, 0xf); DPP_ADD(0x112, 0xf); DPP_ADD(0x114, 0xf); DPP_ADD(0x118, 0xf); DPP_ADD(0x142, 0xa); DPP_ADD(0x143, 0xc);
        const float g = rbf(__builtin_amdgcn_rcpf(1.f + __builtin_amdgcn_exp2f(-1.4426950408889634f * rbf(s))));
        unsigned w = pk2(g, 1.f - g);
        if (w == 0xFFFFFFFFu) w = 0x7FC07FC0u;
        if (lane == 63) __hip_atomic_store(gtab + grow, w, RLX_AGENT);
    }
    FSTAMP(3);
    if (!pf) { ST_X1(128, 1); ST_X3(128, 1); ST_S(128, 1); ST_G1(1, 1); ST_G2(1, 1); FSTAMP(4); WAIT_V(11); }
    else {
        const float lr = bflo(plr), li = bflo(pli), dt = rbf(expf(bflo(pld)));
        const float e = expf(dt * lr); float sn, cs; sincosf(dt * li, &sn, &cs);
        const f32x2 a1 = {e * cs, e * sn};
        const float nr = a1.x - 1.f, den = lr * lr + li * li;
        f32x2 a16 = cmul(a1, a1); a16 = cmul(a16, a16); a16 = cmul(a16, a16); a16 = cmul(a16, a16);
        const f32x2 a32 = cmul(a16, a16), a48 = cmul(a32, a16), a64 = cmul(a32, a32), a128 = cmul(a64, a64), a192 = cmul(a128, a64), a256 = cmul(a128, a128);
        if (lane < 32) {
            LAS f32x4* ct = (LAS f32x4*)(lds + K1_CTAB + lane * 96);
            ct[0] = (f32x4){a1.x, a1.y, 2.f * ((nr * lr + a1.y * li) / den), 2.f * ((a1.y * lr - nr * li) / den)};
            ct[1] = (f32x4){1.f, 0.f, a16.x, a16.y}; ct[2] = (f32x4){a32.x, a32.y, a48.x, a48.y};
            ct[3] = (f32x4){1.f, 0.f, a64.x, a64.y}; ct[4] = (f32x4){a128.x, a128.y, a192.x, a192.y};
            ct[5] = (f32x4){a256.x, a256.y, 0.f, 0.f};
        }
    }
    FSTAMP(5);
    if (wc == 1) BAR();
    STAMP_TAKE(st1);
    FSTAMP(6);
#pragma nounroll
    for (int t = 0; t < 16; t += 2) {
        asm volatile("" : "+s"(t));
        const unsigned ko = t == 14 ? 0x80000000u : (unsigned)(t + 2) * 128u;
        const unsigned kp = t < 3 ? (unsigned)(t + 1) * 512u : 0x80000000u, kq = t < 2 ? (unsigned)(t + 2) * 512u : 0x80000000u;
        const unsigned k0 = pf ? kp : ko, k1 = pf ? kq : ko + 128;
        BAR(); FSTAMP(8 + t); RD_P1(0);
        ST_U1(t + 1, 1); if (!pf) { ST_U2(t + 1, 1); WAIT_V(10); }
        GO(7, 8, 0);
        BAR(); RD_P2(0);
        ST_X1(k0, 0); if (!pf) ST_X3(ko, 0);
        GO(1, 2, 6);
        BAR(); RD_P3(0);
        ST_G1(t + 2, 0); if (!pf) { ST_G2(t + 2, 0); ST_S(ko, 0); WAIT_V(10); }
        GO(3, 4, 5);
        BAR(); FSTAMP(9 + t); RD_P1(1);
        ST_U1(t + 2, 0); if (!pf) { ST_U2(t + 2, 0); WAIT_V(10); }
        GO(7, 8, 0);
        BAR(); RD_P2(1);
        ST_X1(k1, 1); if (!pf) ST_X3(ko + 128, 1);
        GO(1, 2, 6);
        BAR(); RD_P3(1);
        ST_G1(t + 3, 1); if (!pf) { ST_G2(t + 3, 1); ST_S(ko + 128, 1); WAIT_V(10); }
        if (pf) {
            if (t >= 12) {
                WAIT_V(0);
                const __amdgpu_buffer_rsrc_t sGt = SRD(ws + WS_G, 8192);
                dma16c(ldsb + K1_GTAB, voffW >> 3, sGt, (unsigned)(pm * 1024));
            }
            if (t == CODE_T) {
                const __amdgpu_buffer_rsrc_t sI = SRD(code0, codeN);
#pragma unroll
                for (int i = 1; i < 5; ++i) dma16(mSink, voffW, sI, (unsigned)(i * 8192));
            }
            if (t == 4) {
                const __amdgpu_buffer_rsrc_t sA = SRD(ws + WS_A2, WS_A2_BYTES), sO = SRD(out, SEQ * DM * 4);
                const unsigned vo = (voffW >> 7) * A2S;
                dma16(mSink, vo, sA, A2S - 128); dma16(mSink, vo, sA, 65 * A2S - 128); dma16(mSink, voffW << 10, sO, 0u);
            }
        }
        GO(3, 4, 5);
    }
    if (wc == 0) BAR();
    RELANE();
    STAMP_PUT(0, 0, st0); STAMP_PUT(0, 1, st1);
    STAMP(0, 2);
    FSTAMP(24);
    typedef __attribute__((address_space(4))) const unsigned long long* kptr;
    kptr ka = (kptr)__builtin_amdgcn_kernarg_segment_ptr(); asm volatile("" : "+s"(ka));
#define KARG(T, i) ((T)ka[i])
    char* const ws2 = KARG(char*, 13);
    gu32* const gtb = (gu32*)(ws2 + WS_G);

    LAS char* ctab = lds + K1_CTAB + (wc * 16 + er) * 96;
    LAS char* etab = lds + K1_ETAB + (wc * 16 + er) * 144;
    const f32x4 cst = *(LAS f32x4*)ctab;
    const f32x2 a1 = {cst[0], cst[1]}, cf = {cst[2], cst[3]};
    u32x4 gt[4];
#pragma unroll
    for (int m = 0; m < 4; ++m) gt[m] = *(LAS u32x4*)(lds + K1_GTAB + wr * 256 + eq * 64 + m * 16);
    {
        unsigned mx = 0;
#pragma unroll
        for (int m = 0; m < 4; ++m) mx = max(max(mx, max(gt[m][0], gt[m][1])), max(gt[m][2], gt[m][3]));
        if (__any(mx == 0xFFFFFFFFu)) {
            unsigned spins = 0;
            do {
                mx = 0;
#pragma unroll
                for (int m = 0; m < 4; ++m)
#pragma unroll
                    for (int q = 0; q < 4; ++q) {
                        gt[m][q] = __hip_atomic_load(gtb + pm * 256 + wr * 64 + eq * 16 + m * 4 + q, RLX_AGENT);
                        mx = max(mx, gt[m][q]);
                    }
            } while (__any(mx == 0xFFFFFFFFu) && ++spins < (1u << 18));
        }
    }
    FSTAMP(25);
    const __amdgpu_buffer_rsrc_t sA2 = SRD(ws2 + WS_A2, WS_A2_BYTES);
#define ST16(voff, v) __builtin_amdgcn_raw_buffer_store_b128(v, sA2, voff, 0, STPOL)
#define PK8(v) (u32x4){pk2(v[0], v[1]), pk2(v[2], v[3]), pk2(v[4], v[5]), pk2(v[6], v[7])}
    const int mblk = pm * 2 + (wr >> 1);
#define IMG_Q(r, q) (unsigned)(((q) >> 1) * 256 + ((q) & 1) * 16 + ((r) & 3) * 32 + mblk * 16384 + (wr & 1) * 1024)
#define IMG_T(r, q) (IMG_Q(r, q) + (unsigned)((((r) >> 2) & 1) * 2048 + ((r) >> 3) * 128 + (j >> 2) * A2S + (j & 3) * 4096))
#define IMG_M(r, q) (IMG_Q(r, q) + (unsigned)((((r) >> 2) & 1) * 2048 + ((r) >> 3) * 512 + (24 + (j >> 2)) * A2S + (j & 3) * 4096))
#define IMG_S(r, q) (IMG_Q(r, q) + (unsigned)((((r) >> 2) & 1) * 4096 + ((r) >> 3) * 2048 + (32 + (j >> 1)) * A2S + (j & 1) * 8192))
    const unsigned lo = IMG_T(er, eq);
#define GLU_HALF(n, hh, o) do { float v[8]; \
        _Pragma("unroll") for (int e = 0; e < 8; ++e) { const int m = 2 * (hh) + (e >> 2), q = e & 3; v[e] = relu(acc[m][n][q]) * (acc[m][3 + (n)][q] * bfhi(gt[m][q])); } \
        o = PK8(v); } while (0)
#define GLU_TILE(n) do { u32x4 o_; GLU_HALF(n, 0, o_); ST16(lo + (wc * 48 + 8 * (n)) * A2S, o_); GLU_HALF(n, 1, o_); ST16(lo + (wc * 48 + 8 * (n)) * A2S + 512, o_); } while (0)
    GLU_TILE(0);
    GLU_TILE(1);
    asm volatile("" ::: "memory");
    {
        f32x2 h = {acc[0][7][0], acc[0][8][0]};
#pragma unroll
        for (int e = 1; e < 16; ++e) h = cfma(a1, h, (f32x2){acc[e >> 2][7][e & 3], acc[e >> 2][8][e & 3]});
        *(LAS f32x2*)(etab + (wr * 4 + eq) * 8) = h;
    }
    WAIT_V(0); WAIT_L0();
    FSTAMP(26);
    BAR(); asm volatile("" ::: "memory");
    gu32* rdy = (gu32*)(ws2 + WS_RDY);
    if (tz == 192) __hip_atomic_store(rdy + pm * 32 + j, 1u, RLX_AGENT);
    gu64* carry = (gu64*)(ws2 + WS_CARRY);
#define CHAN(c, r) (32 * j + 8 * (c) + 4 * ((r) >> 3) + 16 * (((r) >> 2) & 1) + ((r) & 3))
    if (wid < 2 && pm < 7) {
        const f32x2 a16 = *(LAS f32x2*)(lds + K1_CTAB + (wid * 16 + er) * 96 + 24);
        f32x4 E[8];
#pragma unroll
        for (int i = 0; i < 8; ++i) E[i] = *(LAS f32x4*)(lds + K1_ETAB + (wid * 16 + er) * 144 + i * 16);
        f32x2 run = {E[0][0], E[0][1]};
#pragma unroll
        for (int i = 1; i < 16; ++i) run = cfma(a16, run, (f32x2){E[i >> 1][(i & 1) * 2], E[i >> 1][(i & 1) * 2 + 1]});
        unsigned ux = __float_as_uint(run.x), uy = __float_as_uint(run.y);
        if (ux == 0xFFFFFFFFu) ux = 0x7FC00000u;
        if (eq == 0) __hip_atomic_store(carry + pm * 1024 + CHAN(wid, er), ((unsigned long long)uy << 32) | ux, RLX_AGENT);
    }
    STAMP(0, 3);
    FSTAMP(27);
    GLU_TILE(2);
    {
        float v[8];
        const bool lowh = er < 8;
#pragma unroll
        for (int e = 0; e < 8; ++e) {
            const int m = e >> 2, q = e & 3;
            const float v0 = lowh ? relu(acc[m][6][q]) : acc[m][6][q] * bfhi(gt[m][q]);
            const float v1 = lowh ? relu(acc[2 + m][6][q]) : acc[2 + m][6][q] * bfhi(gt[2 + m][q]);
            const float rcv = __int_as_float(__builtin_amdgcn_mov_dpp(__float_as_int(lowh ? v1 : v0), 0x128, 0xf, 0xf, true));
            v[e] = (lowh ? v0 : v1) * rcv;
        }
        ST16(IMG_M(er, eq) + wc * 128, PK8(v));
    }
    LAS f32x4* park = (LAS f32x4*)(lds + 3 * 32768 + wr * 8192 + (tz & 63) * 16);
    if (wc == 1) {
#pragma unroll
        for (int m = 0; m < 4; ++m) { park[m * 128] = acc[m][7]; park[m * 128 + 64] = acc[m][8]; }
    }
    STAMP(0, 4);
    WAIT_L0();
    FSTAMP(28);
    BAR(); asm volatile("" ::: "memory");
    FSTAMP(29);

    const int xc = bid & 7;
    const int late = idx & 1, mb2 = (xc >> 1) * 4 + (idx >> 3), nblk = (xc & 1) * 4 + ((idx >> 1) & 3);
    const int tile = mb2 * 8 + nblk, n0 = nblk * 128;
    const int rs0 = late ? 3 : 0;

    if (wc == 1) {
        int ty = threadIdx.x; asm volatile("" : "+v"(ty) :: "memory");
        const int ln = ty & 63, e3 = ln >> 3;
#define LEVEL(lvl) do { if (ty == 256) __hip_atomic_store(rdy + pm * 32 + j, (unsigned)(lvl), RLX_AGENT); } while (0)
        const __amdgpu_buffer_rsrc_t sC = SRD(late ? KARG(const u16*, 10) : KARG(const u16*, 9), DM * NP * 2), sW = SRD(KARG(const u16*, 11), FF * DM * 2);
        const unsigned voffA = (unsigned)(ln * 16);
        const unsigned voffC = (unsigned)(e3 * 2048 + (((ln & 7) ^ (e3 & 6)) * 16));
        const unsigned voffD = (unsigned)(((e3 >> 2) * 8 + (e3 & 3)) * 2048 + ((((ln >> 1) & 3) ^ (e3 >> 1)) * 32) + (ln & 1) * 16);
        const unsigned soA = (unsigned)((late ? 0 : 48) * A2S + mb2 * 16384 + wr * 4096);
        const unsigned soC = (unsigned)((n0 + 32 * wr) * 2048);
        const unsigned soD = (unsigned)(((late ? 0 : 48) + 112 * wr) * 2048 + n0 * 2);
        const unsigned mB = ldsb + wr * 4096;
#define DMA_A(i, k, sl) dma16c(mB + (sl) + (i) * 1024, voffA, sA2, soA + (k) * A2S + (i) * 1024)
#define DMA_C(i, p, sl) dma16(mB + (sl) + 16384 + (i) * 1024, voffC, sC, soC + (p) * 128 + (i) * (8 * 2048))
#define DMA_D(i, row, sl) dma16(mB + (sl) + 16384 + (i) * 1024, voffD, sW, soD + (row) * 2048 + ((i) >> 1) * (4 * 2048) + ((i) & 1) * 128)
#define POLL(v, p) asm volatile("global_load_dword %0, %1, off sc1\n\ts_waitcnt vmcnt(0)" : "=v"(v) : "v"(p) : "memory")
#define ASK(v, p) asm volatile("global_load_dword %0, %1, off sc1" : "=v"(v) : "v"(p) : "memory")
#define NEED(v, p, lvl) do { asm volatile("" : "+v"(v) :: "memory"); if (!__all((int)(v) >= (lvl))) { unsigned spins_ = 0, w_; \
            do { __builtin_amdgcn_s_sleep(1); POLL(w_, p); } while (!__all((int)w_ >= (lvl)) && ++spins_ < (1u << 18)); } } while (0)
        gu32* rl = rdy + (mb2 >> 1) * 32 + (ln & 31);
#pragma unroll
        for (int p = 0; p < 3; ++p)
#pragma unroll
            for (int i = 0; i < 4; ++i) DMA_D(i, 448 * p, p * 32768);
        unsigned fv;
        POLL(fv, rl);
        NEED(fv, rl, 1);
        FSTAMP(31);
#pragma unroll
        for (int p = 0; p < 3; ++p)
#pragma unroll
            for (int i = 0; i < 4; ++i) DMA_A(i, p, p * 32768);
        WAIT_V(8); FSTAMP(32); BAR(); FSTAMP(33);
        int k = 3, sl = 3 * 32768;
#define STAGE_W() do { const int row_ = 448 * (k & 7) + ((k >> 3) == 3 ? 96 : 16 * (k >> 3)); \
            _Pragma("unroll") for (int i = 0; i < 4; ++i) { DMA_A(i, k, sl); DMA_D(i, row_, sl); } ++k; sl = sl == 4 * 32768 ? 0 : sl + 32768; } while (0)
#define LSYNC() do { WAIT_V(16); BAR(); } while (0)
        STAGE_W(); WAIT_V(12); BAR();
#pragma nounroll
        while (k < 10) { STAGE_W(); LSYNC(); }
        LEVEL(2);
#pragma nounroll
        while (k < 13) { STAGE_W(); LSYNC(); }
        ASK(fv, rl);
        STAGE_W(); LSYNC(); STAGE_W(); LSYNC(); STAGE_W(); LSYNC();
        NEED(fv, rl, 2);
        const int nw = late ? 32 : 24;
#pragma nounroll
        while (k < nw) { STAGE_W(); LSYNC(); }
#pragma nounroll
        for (int p = 0; p < 16; ++p) {
#pragma unroll
            for (int i = 0; i < 4; ++i) { DMA_A(i, k, sl); DMA_C(i, p, sl); }
            ++k; sl = sl == 4 * 32768 ? 0 : sl + 32768;
            LSYNC();
        }
        gu32* flag = (gu32*)(ws2 + WS_FLAG2 + tile * 128);
        WAIT_V(8); BAR();
        ASK(fv, flag);
        WAIT_V(0); BAR();
        const int tb = mb2 * 128 + (wr >> 1) * 64 + (wr & 1) * 8 + (ln >> 5), dcol = n0 + (ln & 31) * 4;
#define ROW(i) (tb + ((i) >> 3) * 32 + (((i) >> 2) & 1) * 16 + ((i) & 3) * 2)
        const __amdgpu_buffer_rsrc_t sP = SRD(ws2 + WS_A2 + (size_t)(KSTEPS2 + (tile >> 2)) * A2S + (tile & 3) * 65536, 65536);
        f32x4 ex[16];
        if (late) {
            NEED(fv, flag, 1);
#pragma unroll
            for (int i = 0; i < 16; ++i) ex[i] = __builtin_bit_cast(f32x4, __builtin_amdgcn_raw_buffer_load_b128(sP, (wr * 16 + i) * 1024 + ln * 16, 0, 16));
        } else {
            const u16* xk = KARG(const u16*, 0);
            const u32x2 dk = *(const GAS u32x2*)(KARG(const u16*, 12) + dcol);
            u32x2 xv[16]; unsigned gwd[16];
#pragma unroll
            for (int i = 0; i < 16; ++i) { gwd[i] = __hip_atomic_load(gtb + ROW(i), RLX_AGENT); xv[i] = *(const GAS u32x2*)(xk + (size_t)ROW(i) * DM + dcol); }
#pragma unroll
            for (int i = 0; i < 16; ++i) {
                const float g = bflo(gwd[i]);
                ex[i] = (f32x4){g * rbf(bflo(xv[i][0]) * bflo(dk[0])), g * rbf(bfhi(xv[i][0]) * bfhi(dk[0])),
                                g * rbf(bflo(xv[i][1]) * bflo(dk[1])), g * rbf(bfhi(xv[i][1]) * bfhi(dk[1]))};
            }
        }
        const int wn1 = (ln >> 4) & 1, s0 = rs0 + wn1, s1 = rs0 + 2 + wn1;
        const int inp = wr * 8192 + ((ln >> 2) & 3) * 1024 + (ln & 3) * 256, sw = (ln & 15) ^ (ln >> 5);
        LAS char* q0 = lds + (s0 >= 5 ? s0 - 5 : s0) * 32768 + inp;
        LAS char* q1 = lds + (s1 >= 5 ? s1 - 5 : s1) * 32768 + inp;
        BAR(); asm volatile("" ::: "memory");
        STAMP(1, 3);
#pragma unroll
        for (int i = 0; i < 16; ++i) {
            const int o = (i >> 3) * 4096 + ((sw ^ (2 * (i & 7))) * 16);
            ex[i] += *(LAS f32x4*)(q0 + o) + *(LAS f32x4*)(q1 + o);
        }
        if (!late) {
#pragma unroll
            for (int i = 0; i < 16; ++i) __builtin_amdgcn_raw_buffer_store_b128(__builtin_bit_cast(u32x4, ex[i]), sP, (wr * 16 + i) * 1024 + ln * 16, 0, 16);
            WAIT_V(0);
            BAR();
            if (ty == 256) __hip_atomic_store(flag, 1u, RLX_AGENT);
        } else {
            const __amdgpu_buffer_rsrc_t sO = SRD(KARG(float*, 14), SEQ * DM * 4);
#pragma unroll
            for (int i = 0; i < 16; ++i) __builtin_amdgcn_raw_buffer_store_b128(__builtin_bit_cast(u32x4, ex[i]), sO, (ROW(i) * DM + dcol) * 4, 0, OUTPOL);
        }
        STAMP(1, 6);
        STAMP(1, 7);
        FSTAMP(34);
        FDUMP();
        asm volatile(".Lk1_end_a:");
        return;
    }

    {
        unsigned long long gq[2][7];
#pragma unroll
        for (int c = 0; c < 2; ++c)
#pragma unroll
            for (int q = 0; q < 7; ++q) gq[c][q] = __hip_atomic_load(carry + (q < pm ? q : 0) * 1024 + CHAN(c, er), RLX_AGENT);
        f32x2 h[2];
        const int own = wr * 4 + eq;
#pragma unroll
        for (int c = 0; c < 2; ++c) {
            const f32x2 a16 = *(LAS f32x2*)(ctab + c * (16 * 96) + 24);
            f32x4 E[8];
#pragma unroll
            for (int i = 0; i < 8; ++i) E[i] = *(LAS f32x4*)(etab + c * (16 * 144) + i * 16);
            f32x2 run = {E[0][0], E[0][1]}, P = {0.f, 0.f};
#pragma unroll
            for (int i = 1; i < 16; ++i) {
                P.x = own == i ? run.x : P.x; P.y = own == i ? run.y : P.y;
                if (i < 15) run = cfma(a16, run, (f32x2){E[i >> 1][(i & 1) * 2], E[i >> 1][(i & 1) * 2 + 1]});
            }
            h[c] = P;
        }
        f32x4 ur[4], ui[4];
#pragma unroll
        for (int m = 0; m < 4; ++m) { ur[m] = park[m * 128]; ui[m] = park[m * 128 + 64]; }
        const f32x4 cs1 = *(LAS f32x4*)(ctab + 16 * 96);
        FSTAMP(30);
        if (pm > 0) {
            unsigned mx = 0;
#pragma unroll
            for (int q = 0; q < 7; ++q) mx = max(mx, max((unsigned)gq[0][q], (unsigned)gq[1][q]));
            if (__any(mx == 0xFFFFFFFFu)) {
                unsigned spins = 0;
                do {
                    mx = 0;
#pragma unroll
                    for (int c = 0; c < 2; ++c)
#pragma unroll
                        for (int q = 0; q < 7; ++q) {
                            gq[c][q] = __hip_atomic_load(carry + (q < pm ? q : 0) * 1024 + CHAN(c, er), RLX_AGENT);
                            mx = max(mx, (unsigned)gq[c][q]);
                        }
                } while (__any(mx == 0xFFFFFFFFu) && ++spins < (1u << 18));
            }
#pragma unroll
            for (int c = 0; c < 2; ++c) {
                LAS char* ct = ctab + c * (16 * 96);
                const f32x2 a256 = *(LAS f32x2*)(ct + 80), aq = cmul(*(LAS f32x2*)(ct + 16 + eq * 8), *(LAS f32x2*)(ct + 48 + wr * 8));
                f32x2 cg = {0.f, 0.f};
#pragma unroll
                for (int q = 0; q < 7; ++q)
                    if (q < pm) cg = cfma(a256, cg, (f32x2){__uint_as_float((unsigned)gq[c][q]), __uint_as_float((unsigned)(gq[c][q] >> 32))});
                h[c] = cfma(aq, cg, h[c]);
            }
        }
        STAMP(0, 5);
        FSTAMP(31);
        const unsigned ls = IMG_S(er, eq);
        const f32x2 b1 = {cs1[0], cs1[1]}, bf = {cs1[2], cs1[3]};
#pragma unroll
        for (int hh = 0; hh < 2; ++hh) {
            float vr[8], vi[8], wr8[8], wi8[8];
#pragma unroll
            for (int e = 0; e < 8; ++e) {
                const int m = 2 * hh + (e >> 2), q = e & 3;
                h[0] = cfma(a1, h[0], (f32x2){acc[m][7][q], acc[m][8][q]});
                h[1] = cfma(b1, h[1], (f32x2){ur[m][q], ui[m][q]});
                const float g2 = bflo(gt[m][q]);
                const f32x2 s0 = cmul(cf, h[0]), s1 = cmul(bf, h[1]);
                vr[e] = s0.x * g2; vi[e] = -s0.y * g2; wr8[e] = s1.x * g2; wi8[e] = -s1.y * g2;
            }
            ST16(ls + 40 * A2S + hh * 512, PK8(vr)); ST16(ls + 40 * A2S + hh * 512 + 128, PK8(wr8));
            ST16(ls + hh * 512, PK8(vi)); ST16(ls + hh * 512 + 128, PK8(wi8));
        }
    }
    STAMP(0, 6);
    int ty = threadIdx.x; asm volatile("" : "+v"(ty) :: "memory");
    const int ln = ty & 63, fr2 = ty & 15, fq2 = (ty >> 4) & 3;
    const int wn = wid & 1, kk = wid >> 1;
    LAS char* rH = lds + kk * 8192 + (ln >> 5) * 4096 + (ln & 31) * 8;
    LAS char* rC = lds + 16384 + (wn * 64 + fr2) * 128 + (((4 * kk + fq2) ^ (fr2 & 6)) * 16);
    LAS char* rWb = lds + 16384 + kk * 8192 + (ln >> 5) * 4096 + wn * 1024 + ((ln >> 2) & 7) * 128 + (ln & 3) * 8;
    const int rsw = (ln >> 3) & 3;
    f32x4 ac2[4][8];
#pragma unroll
    for (int n = 0; n < 4; ++n)
#pragma unroll
        for (int m = 0; m < 8; ++m) { ac2[n][m] = (f32x4){0.f, 0.f, 0.f, 0.f}; asm volatile("" : "+v"(ac2[n][m])); }
    STAMP(0, 7);
    FSTAMP(32);
    FDUMP();
    s16x4 HX[8][2], HY[8][2], WX[4][2], WY[4][2];
    int sl = 0;

#define RD1(RD, i, H, W) do { \
        if ((i) < 8) { \
            if (RD == 1) W[(i) >> 1][(i) & 1] = tr4(rWb + sl + ((((i) >> 1) ^ rsw) * 32) + ((i) & 1) * 2048); \
            else if (((i) & 1) == 0) { const s16x8 v_ = *(LAS s16x8*)(rC + sl + ((i) >> 1) * 2048); \
                W[(i) >> 1][0] = __builtin_shufflevector(v_, v_, 0, 1, 2, 3); W[(i) >> 1][1] = __builtin_shufflevector(v_, v_, 4, 5, 6, 7); } \
        } else H[((i) - 8) >> 1][(i) & 1] = tr4(rH + sl + (((i) - 8) >> 1) * 256 + ((i) & 1) * 2048); \
    } while (0)
#define LGKM0() __builtin_amdgcn_s_waitcnt(0xC07F)
#define MM(i, HC, WC) mfma_acc(ac2[(i) & 3][(i) >> 2], cat8(WC[(i) & 3][0], WC[(i) & 3][1]), cat8(HC[(i) >> 2][0], HC[(i) >> 2][1]))
#define STEP(RD, HC, WC, HN, WN) do { \
        _Pragma("unroll") for (int i = 0; i < 32; ++i) { MM(i, HC, WC); if (i < 24) RD1(RD, i, HN, WN); } \
        sl = sl == 4 * 32768 ? 0 : sl + 32768; LGKM0(); \
    } while (0)

    LGKM0();
    BAR();
    STAMP_TAKE(st9);
#pragma unroll
    for (int i = 0; i < 24; ++i) RD1(1, i, HX, WX);
    sl = 32768; LGKM0();
    const int nf = late ? 15 : 11;
#pragma nounroll
    for (int i = 0; i < nf; ++i) {
        if (i == 3) WAIT_V(0);
        BAR(); STEP(1, HX, WX, HY, WY); BAR(); STEP(1, HY, WY, HX, WX);
    }
    BAR(); STEP(1, HX, WX, HY, WY); BAR(); STEP(0, HY, WY, HX, WX);
#pragma nounroll
    for (int i = 0; i < 7; ++i) { BAR(); STEP(0, HX, WX, HY, WY); BAR(); STEP(0, HY, WY, HX, WX); }
    BAR(); STEP(0, HX, WX, HY, WY);
    {
        const int sw = rs0 + wid;
        LAS char* dmp = lds + (sw >= 5 ? sw - 5 : sw) * 32768 + fq2 * 256;
#define DUMP(i) *(LAS f32x4*)(dmp + (i) * 1024 + ((fr2 ^ (4 * ((i) & 3) + fq2)) * 16)) = ac2[(i) & 3][(i) >> 2]
#pragma unroll
        for (int i = 0; i < 32; ++i) { MM(i, HY, WY); if (i >= 12) DUMP(i - 12); }
        asm volatile("s_nop 15" ::: "memory");
#pragma unroll
        for (int i = 20; i < 32; ++i) DUMP(i);
    }
    WAIT_L0();
    STAMP_PUT(1, 0, st0); STAMP_PUT(1, 1, st9);
    STAMP(1, 2);
    BAR();
    if (!late) BAR();
    asm volatile(".Lk1_end_b:");
}

extern "C" void kernel_launch(void* const* d_in, const int* in_sizes, int n_in, void* d_out, int out_size, void* d_ws, size_t ws_size,
                              hipStream_t stream) {
    const u16* x = (const u16*)d_in[0];
    const u16* lre = (const u16*)d_in[1];
    const u16* lim = (const u16*)d_in[2];
    const u16* Bre = (const u16*)d_in[3];
    const u16* Bim = (const u16*)d_in[4];
    const u16* Cre = (const u16*)d_in[5];
    const u16* Cim = (const u16*)d_in[6];
    const u16* logdt = (const u16*)d_in[7];
    const u16* dskip = (const u16*)d_in[8];
    const u16* Wg = (const u16*)d_in[10];
    const u16* Wu = (const u16*)d_in[11];
    const u16* Wd = (const u16*)d_in[12];
    const u16* gw = (const u16*)d_in[13];
    char* ws = (char*)d_ws;
    (void)hipFuncSetAttribute((const void*)k1_fused, hipFuncAttributeMaxDynamicSharedMemorySize, K2_LDS);
    (void)hipMemsetAsync(ws, 0xFF, WS_INIT_BYTES, stream);
    k1_fused<<<256, 512, K2_LDS, stream>>>(x, Wg, Wu, Bre, Bim, lre, lim, logdt, gw, Cre, Cim, Wd, dskip, ws, (float*)d_out);
}
```

```cpp
#include <hip/hip_runtime.h>
#include <hip/hip_bf16.h>

typedef unsigned short u16;
typedef __bf16 bf16x8 __attribute__((ext_vector_type(8)));
typedef __bf16 bf16x2 __attribute__((ext_vector_type(2)));
typedef short s16x4 __attribute__((ext_vector_type(4)));
typedef short s16x8 __attribute__((ext_vector_type(8)));
typedef float f32x4 __attribute__((ext_vector_type(4)));
typedef float f32x2 __attribute__((ext_vector_type(2)));
typedef unsigned u32x4 __attribute__((ext_vector_type(4)));
typedef unsigned u32x2 __attribute__((ext_vector_type(2)));
#define LAS __attribute__((address_space(3)))
#define GAS __attribute__((address_space(1)))
typedef GAS unsigned gu32;
typedef GAS unsigned long long gu64;
typedef __attribute__((address_space(4))) const unsigned long long* kptr;
#define RLX_AGENT __ATOMIC_RELAXED, __HIP_MEMORY_SCOPE_AGENT

constexpr int SEQ = 2048, DM = 1024, NP = 1024, FF = 3584;
constexpr int KSTEPS2 = (2 * NP + FF) / 64;

constexpr unsigned WS_CARRY = 0;
constexpr unsigned WS_G = 7 * 8192;
constexpr unsigned WS_RDY = 65536;
constexpr unsigned WS_FLAG2 = WS_RDY + 1024;
constexpr unsigned WS_HQ = WS_FLAG2 + 16384;
constexpr unsigned WS_HA = WS_HQ + 1024;
constexpr unsigned WS_HR = WS_HA + 1024;
constexpr unsigned WS_SYNC_BYTES = WS_HR + 2048;
constexpr size_t WS_A2 = 1 << 20;
constexpr unsigned A2S = 262144 + 128;
constexpr size_t WS_A2_BYTES = (size_t)(KSTEPS2 + 32) * A2S;

__device__ __forceinline__ float bflo(unsigned v) { return __uint_as_float(v << 16); }
__device__ __forceinline__ float bfhi(unsigned v) { return __uint_as_float(v & 0xffff0000u); }
__device__ __forceinline__ float rbf(float f) { return (float)(__bf16)f; }
__device__ __forceinline__ unsigned pk2(float a, float b) {
    bf16x2 v; v.x = (__bf16)a; v.y = (__bf16)b; return __builtin_bit_cast(unsigned, v);
}
__device__ __forceinline__ float relu(float v) { return __int_as_float(max(__float_as_int(v), 0)); }
__device__ __forceinline__ float dot2(unsigned a, unsigned b, float c) {
    return __builtin_amdgcn_fdot2_f32_bf16(__builtin_bit_cast(bf16x2, a), __builtin_bit_cast(bf16x2, b), c, false);
}

constexpr size_t WS_DBG = 200u << 20;
#define STAMP(kid, i)
#define STAMP_TAKE(v)
#define STAMP_PUT(kid, i, v)

#define FSTAMP(i)
#define FDUMP()
#define WAIT_V(n) asm volatile("s_waitcnt vmcnt(" #n ")" ::: "memory")
#define WAIT_L0() asm volatile("s_waitcnt lgkmcnt(0)" ::: "memory")
#define BAR() __builtin_amdgcn_s_barrier()
#define SCHED() __builtin_amdgcn_sched_barrier(0)
#define SRD(p, bytes) __builtin_amdgcn_make_buffer_rsrc((void*)(p), (short)0, (int)(bytes), 0x00020000)

__device__ __forceinline__ void dma16(unsigned m0v, unsigned voff, __amdgpu_buffer_rsrc_t srd, unsigned soff) {
    asm volatile("s_mov_b32 m0, %0\n\ts_nop 0\n\tbuffer_load_dwordx4 %1, %2, %3 offen lds" ::"s"(m0v), "v"(voff), "s"(srd), "s"(soff) : "m0", "memory");
}
__device__ __forceinline__ bf16x8 lds_b128(LAS char* p) { return __builtin_bit_cast(bf16x8, *(LAS s16x8*)p); }
__device__ __forceinline__ bf16x8 lds_tr(LAS char* p0, LAS char* p1) {
    s16x4 a = __builtin_amdgcn_ds_read_tr16_b64_v4i16((LAS s16x4*)p0);
    s16x4 b = __builtin_amdgcn_ds_read_tr16_b64_v4i16((LAS s16x4*)p1);
    return __builtin_bit_cast(bf16x8, __builtin_shufflevector(a, b, 0, 1, 2, 3, 4, 5, 6, 7));
}
#define MFMA(a, b, c) __builtin_amdgcn_mfma_f32_16x16x32_bf16(a, b, c, 0, 0, 0)
__device__ __forceinline__ f32x2 cmul(f32x2 a, f32x2 b) { return (f32x2){__builtin_fmaf(a.x, b.x, -(a.y * b.y)), __builtin_fmaf(a.x, b.y, a.y * b.x)}; }
__device__ __forceinline__ f32x2 cfma(f32x2 a, f32x2 b, f32x2 c) {
    return (f32x2){__builtin_fmaf(a.x, b.x, __builtin_fmaf(-a.y, b.y, c.x)), __builtin_fmaf(a.x, b.y, __builtin_fmaf(a.y, b.x, c.y))};
}
__device__ __forceinline__ f32x2 sel4(int i, f32x2 a, f32x2 b, f32x2 c, f32x2 d) {
    const bool o = i & 1, u = i & 2;
    const float lx = o ? b.x : a.x, ly = o ? b.y : a.y, hx = o ? d.x : c.x, hy = o ? d.y : c.y;
    return (f32x2){u ? hx : lx, u ? hy : ly};
}

#define STPOL 16
#define PF_W0 1
#define PF_D0 4
#define OUTPOL 16

#define K2_SPLIT 40
#define MAP 0
#define ABL_NOLOAD 0
#define ABL_NOREAD 0
#define ABL_NOMFMA 0
#define CODE_T 10
#define LATE_FLIP 0
#define DIST 5
constexpr int K2_LDS = 5 * 32768;

__device__ __forceinline__ void dma16c(unsigned m0v, unsigned voff, __amdgpu_buffer_rsrc_t srd, unsigned soff) {
    asm volatile("s_mov_b32 m0, %0\n\ts_nop 0\n\tbuffer_load_dwordx4 %1, %2, %3 offen sc1 lds" ::"s"(m0v), "v"(voff), "s"(srd), "s"(soff) : "m0", "memory");
}
__device__ __forceinline__ void mfma_acc(f32x4& c, bf16x8 a, bf16x8 b) {
    asm volatile("v_mfma_f32_16x16x32_bf16 %0, %1, %2, %0" : "+v"(c) : "v"(a), "v"(b) : "memory");
}
__device__ __forceinline__ s16x4 tr4(LAS char* p) { return __builtin_amdgcn_ds_read_tr16_b64_v4i16((LAS s16x4*)p); }
__device__ __forceinline__ bf16x8 cat8(s16x4 a, s16x4 b) { return __builtin_bit_cast(bf16x8, __builtin_shufflevector(a, b, 0, 1, 2, 3, 4, 5, 6, 7)); }

constexpr int K1_ASB = 40960;
constexpr int K1_W = 81920;
constexpr int K1_WBUF = 28704, K1_UOFF = 14352;
constexpr int K1_ETAB = K1_W + 2 * K1_WBUF;
constexpr int K1_CTAB = K1_ETAB + 32 * 144;
constexpr int K1_SINK = K1_CTAB + 32 * 96;
constexpr int K1_GTAB = K1_SINK + 1024;
constexpr int K1_HS = K1_GTAB + 1024;

__global__ __launch_bounds__(512, 2) void k1_fused(const u16* __restrict__ x, const u16* __restrict__ Wg, const u16* __restrict__ Wu,
                                                       const u16* __restrict__ Bre, const u16* __restrict__ Bim, const u16* __restrict__ lre,
                                                       const u16* __restrict__ lim, const u16* __restrict__ logdt, const u16* __restrict__ gw,
                                                       const u16* __restrict__ Cre, const u16* __restrict__ Cim, const u16* __restrict__ Wd,
                                                       const u16* __restrict__ dskip, char* __restrict__ ws, float* __restrict__ out) {
    extern __shared__ __attribute__((aligned(16))) char smem[];
    LAS char* lds = (LAS char*)smem;
    const unsigned ldsb = (unsigned)(size_t)lds;
    const int tid = threadIdx.x, lane = tid & 63;
    const int wid = __builtin_amdgcn_readfirstlane(tid >> 6);
    const int wr = wid & 3, wc = wid >> 2;
    const int bid = blockIdx.x, idx = bid >> 3, j = (bid & 7) * 4 + (idx & 3), pm = idx >> 2;
    const int fr = lane & 15, fq = lane >> 4;
#define ROLE(b_, i_) const int xc = (b_) & 7, late = (xc & 1) ^ LATE_FLIP, mb2 = (xc >> 2) * 8 + ((i_) >> 2), nblk = ((xc >> 1) & 1) * 4 + ((i_) & 3), tile = mb2 * 8 + nblk
    STAMP_TAKE(st0);
#define LT(n)
    FSTAMP(0);

    const char* code0; unsigned codeA, codeB;
    asm volatile("s_getpc_b64 %0\n.Lk1_ref:\n\ts_mov_b32 %1, .Lk1_end_a-.Lk1_ref\n\ts_mov_b32 %2, .Lk1_end_b-.Lk1_ref" : "=s"(code0), "=s"(codeA), "=s"(codeB));
    const unsigned codeN = (codeA > codeB ? codeA : codeB) + 192;
    if (wid == 7) {
        const __amdgpu_buffer_rsrc_t sI = SRD(code0, codeN);
        dma16(ldsb + K1_SINK, (unsigned)(lane * 128), sI, 0u); dma16(ldsb + K1_SINK, (unsigned)(lane * 128), sI, 8192u);
    }

    const int ch = 32 * j + 8 * (fq & 1) + 4 * (fr >> 3) + 16 * ((fr >> 2) & 1) + (fr & 3);
    const int grow = pm * 256 + j * 8 + wid;
    u32x4 xa, xb, ga, gb; unsigned plr, pli, pld;
    {
        const u16* xr = x + (size_t)grow * DM;
        const unsigned l16 = lane * 16, c2 = ch * 2;
        asm volatile("s_nop 4\n\tglobal_load_dwordx4 %0, %7, %9\n\tglobal_load_dwordx4 %1, %7, %9 offset:1024\n\t"
                     "global_load_dwordx4 %2, %7, %10\n\tglobal_load_dwordx4 %3, %7, %10 offset:1024\n\t"
                     "global_load_ushort %4, %8, %11\n\tglobal_load_ushort %5, %8, %12\n\tglobal_load_ushort %6, %8, %13"
                     : "=&v"(xa), "=&v"(xb), "=&v"(ga), "=&v"(gb), "=&v"(plr), "=&v"(pli), "=&v"(pld)
                     : "v"(l16), "v"(c2), "s"(xr), "s"(gw), "s"(lre), "s"(lim), "s"(logdt) : "memory");
    }

    const bool pf = wid == 7;
    const int q4 = wid + 28, q5 = wid + 35;
    const __amdgpu_buffer_rsrc_t sX = SRD(!pf || !(bid & 32) ? x : (bid & 16) ? Bim : Bre, !pf || !(bid & 32) ? SEQ * DM * 2 : NP * DM * 2);
    const int pb = (bid >> 3) | ((bid & 7) << 5);
    const __amdgpu_buffer_rsrc_t sG = SRD(!pf ? Wg : (pb & 1) ? Wd : (pb & 2) ? Cim : Cre, (pf && !(pb & 1)) ? DM * NP * 2 : DM * FF * 2);
    const __amdgpu_buffer_rsrc_t sU = SRD((pf && (pb & 1)) ? Wg : Wu, DM * FF * 2);
    const __amdgpu_buffer_rsrc_t s4 = SRD(pf ? (const u16*)ws : q4 < 32 ? x : (q4 & 2) ? Bim : Bre, pf ? WS_SYNC_BYTES : q4 < 32 ? SEQ * DM * 2 : NP * DM * 2);
    const __amdgpu_buffer_rsrc_t s5 = SRD(pf ? (const u16*)code0 : (q5 & 2) ? Bim : Bre, pf ? codeN : q5 < 40 ? NP * DM * 2 : 0);

    const int l3 = lane >> 3;
    const unsigned voffX = pf ? (unsigned)(lane * 2048) : (unsigned)(((l3 >> 2) * 16 + (l3 & 3)) * 2048 + (((lane & 7) ^ (l3 & 6)) * 16));
    unsigned voffW;
    { const int gam = wid * 64 + lane, rho = gam / 14, c = gam - rho * 14, k = (rho & 0x13) | ((rho & 4) << 1) | ((rho & 8) >> 1);
      voffW = pf ? (unsigned)(lane * 128) : (unsigned)(k * (FF * 2) + c * 16); }
#define XROW(q) (pm * 256 + ((q) >> 3) * 64 + (((q) >> 2) & 1) * 8 + ((q) & 1) * 32 + (((q) >> 1) & 1) * 4)
#define BROW(q) (32 * j + 8 * (((q) >> 2) & 1) + 4 * ((q) & 1))
    const unsigned so0 = (unsigned)(XROW(wid) * 2048), so1 = (unsigned)(XROW(wid + 7) * 2048), so2 = (unsigned)(XROW(wid + 14) * 2048),
                   so3 = (unsigned)(XROW(wid + 21) * 2048), so4 = (unsigned)((q4 < 32 ? XROW(q4) : BROW(q4)) * 2048), so5 = (unsigned)(BROW(q5) * 2048);
    const unsigned soW = (unsigned)(j * 224);
    const unsigned mX = ldsb + wid * 1024, mW = ldsb + K1_W + wid * 1024, mSink = ldsb + K1_SINK;
    const unsigned m50 = q5 < 40 ? mX + 35 * 1024 : mSink, m51 = q5 < 40 ? mX + 35 * 1024 + K1_ASB : mSink;
    constexpr unsigned WST = 64 * FF * 2, WSH = 32 * FF * 2;

    const unsigned mXa = pf ? mSink : mX, mXb = pf ? mSink : mX + K1_ASB;
    const unsigned mGa = pf ? mSink : mW, mGb = pf ? mSink : mW + K1_WBUF, mUa = pf ? mSink : mW + K1_UOFF, mUb = pf ? mSink : mW + K1_WBUF + K1_UOFF;
    const unsigned sx0 = pf ? (unsigned)((bid & ((bid & 32) ? 15 : 31)) * 131072 + (bid >> 6) * 128) : so0;
    const unsigned ust = pf ? (1u << 20) : WST, gst = !pf ? WST : (pb & 1) ? (1u << 20) : (1u << 19);
    const unsigned su0 = pf ? (unsigned)((pb >> 1) * 8192) + (unsigned)(PF_W0) * ust : soW;
    const unsigned sg0 = pf ? (unsigned)(((pb & 1) ? pb >> 1 : pb >> 2) * 8192) - (unsigned)(PF_D0 + 2) * gst : soW;
#define ST_X1(k0, b) dma16((b) ? mXb : mXa, voffX, sX, sx0 + (k0))
#define ST_X3(ko, b) do { dma16(mX + (b) * K1_ASB + 7 * 1024, voffX, sX, so1 + (ko)); \
        dma16(mX + (b) * K1_ASB + 14 * 1024, voffX, sX, so2 + (ko)); dma16(mX + (b) * K1_ASB + 21 * 1024, voffX, sX, so3 + (ko)); } while (0)
#define ST_S(ko, b) do { dma16(mX + (b) * K1_ASB + 28 * 1024, voffX, s4, so4 + (ko)); dma16((b) ? m51 : m50, voffX, s5, so5 + (ko)); } while (0)
#define ST_G1(t, b) dma16((b) ? mGb : mGa, voffW, sG, sg0 + (t) * gst)
#define ST_G2(t, b) dma16(mW + (b) * K1_WBUF + 7168, voffW, sG, soW + (t) * WST + WSH)
#define ST_U1(t, b) dma16((b) ? mUb : mUa, voffW, sU, su0 + (t) * ust)
#define ST_U2(t, b) dma16(mW + (b) * K1_WBUF + K1_UOFF + 7168, voffW, sU, soW + (t) * WST + WSH)

    ST_X1(0, 0);
    if (!pf) { ST_X3(0, 0); ST_S(0, 0); ST_G1(0, 0); ST_G2(0, 0); ST_U2(0, 0); }
    ST_U1(0, 0);
    FSTAMP(1);

    const int ch0 = (fq ^ (fr & 6)) * 16;
    LAS char* rA0 = lds + wr * 8192 + fr * 128 + ch0;
    LAS char* rA1 = lds + wr * 8192 + fr * 128 + (ch0 ^ 64);
    LAS char* rS0 = lds + 32768 + wc * 4096 + fr * 128 + ch0;
    LAS char* rS1 = lds + 32768 + wc * 4096 + fr * 128 + (ch0 ^ 64);
    const int trow = (16 * (fq >> 1) + 4 * (fq & 1) + ((lane & 15) >> 2)) * 224;
    LAS char* rW = lds + K1_W + trow + wc * 96 + (lane & 3) * 8;
    LAS char* rM = lds + K1_W + trow + 192 + wc * 16 + (lane & 1) * 8 + ((lane >> 1) & 1) * K1_UOFF;

    f32x4 acc[4][9];
#pragma unroll
    for (int m = 0; m < 4; ++m)
#pragma unroll
        for (int n = 0; n < 9; ++n) { acc[m][n] = (f32x4){0.f, 0.f, 0.f, 0.f}; asm volatile("" : "+v"(acc[m][n])); }
    bf16x8 Af[4][2], Bf[3][2];

#define RD_A(b) do { _Pragma("unroll") for (int m = 0; m < 4; ++m) { Af[m][0] = lds_b128(rA0 + (b) * K1_ASB + m * 2048); Af[m][1] = lds_b128(rA1 + (b) * K1_ASB + m * 2048); } } while (0)
#define RD_S(slot, ri, b) do { Bf[slot][0] = lds_b128(rS0 + (b) * K1_ASB + (ri) * 2048); Bf[slot][1] = lds_b128(rS1 + (b) * K1_ASB + (ri) * 2048); } while (0)
#define RD_W(slot, base, off) do { Bf[slot][0] = lds_tr((base) + (off), (base) + (off) + 1792); Bf[slot][1] = lds_tr((base) + (off) + 7168, (base) + (off) + 7168 + 1792); } while (0)
#define MMA3(n0, n1, n2) do { __builtin_amdgcn_s_setprio(1); \
        _Pragma("unroll") for (int kk = 0; kk < 2; ++kk) _Pragma("unroll") for (int m = 0; m < 4; ++m) { \
            acc[m][n0] = MFMA(Af[m][kk], Bf[0][kk], acc[m][n0]); acc[m][n1] = MFMA(Af[m][kk], Bf[1][kk], acc[m][n1]); acc[m][n2] = MFMA(Af[m][kk], Bf[2][kk], acc[m][n2]); } \
        __builtin_amdgcn_s_setprio(0); } while (0)
#define RD_P1(b) do { RD_S(0, 0, b); RD_S(1, 1, b); RD_W(2, rW, (b) * K1_WBUF); RD_A(b); } while (0)
#define RD_P2(b) do { RD_W(0, rW, (b) * K1_WBUF + 32); RD_W(1, rW, (b) * K1_WBUF + 64); RD_W(2, rM, (b) * K1_WBUF); } while (0)
#define RD_P3(b) do { RD_W(0, rW, (b) * K1_WBUF + K1_UOFF); RD_W(1, rW, (b) * K1_WBUF + K1_UOFF + 32); RD_W(2, rW, (b) * K1_WBUF + K1_UOFF + 64); } while (0)
#define GO(n0, n1, n2) do { WAIT_L0(); BAR(); SCHED(); MMA3(n0, n1, n2); SCHED(); } while (0)

#define RELANE() int tz = threadIdx.x; asm volatile("" : "+v"(tz)); const int er = tz & 15, eq = (tz >> 4) & 3; \
    const int ech = 32 * j + 8 * wc + 4 * (er >> 3) + 16 * ((er >> 2) & 1) + (er & 3); (void)ech; (void)eq
    asm volatile("" : "+v"(rA0), "+v"(rA1), "+v"(rS0), "+v"(rS1), "+v"(rW), "+v"(rM));
    gu32* gtab = (gu32*)(ws + WS_G);
    WAIT_V(10);
    if (pf) WAIT_V(2);
    asm volatile("" : "+v"(xa), "+v"(xb), "+v"(ga), "+v"(gb), "+v"(plr), "+v"(pli), "+v"(pld) :: "memory");
    FSTAMP(2);
    {
        float s = 0.f, s2 = 0.f;
#pragma unroll
        for (int e = 0; e < 4; ++e) { s = dot2(xa[e], ga[e], s); s2 = dot2(xb[e], gb[e], s2); }
        s += s2;
#define DPP_ADD(ctrl, rmask) s += __int_as_float(__builtin_amdgcn_update_dpp(0, __float_as_int(s), ctrl, rmask, 0xf, true))
        DPP_ADD(0x111, 0xf); DPP_ADD(0x112, 0xf); DPP_ADD(0x114, 0xf); DPP_ADD(0x118, 0xf); DPP_ADD(0x142, 0xa); DPP_ADD(0x143, 0xc);
        const float g = rbf(__builtin_amdgcn_rcpf(1.f + __builtin_amdgcn_exp2f(-1.4426950408889634f * rbf(s))));
        if (lane == 63) __hip_atomic_store(gtab + grow, pk2(g, 1.f - g), RLX_AGENT);
    }
    FSTAMP(3);
    if (!pf) { ST_X1(128, 1); ST_X3(128, 1); ST_S(128, 1); ST_G1(1, 1); ST_G2(1, 1); FSTAMP(4); WAIT_V(11); }
    else {
        dma16c(ldsb + K1_HS + 1024, (unsigned)(lane * 16), s4, WS_HR + (bid >> 7) * 1024);
        dma16c(ldsb + K1_HS, (unsigned)(lane * 16), s4, bid == 0 ? WS_HA : 0x80000000u);
        const float lr = bflo(plr), li = bflo(pli), dt = rbf(expf(bflo(pld)));
        const float e = expf(dt * lr); float sn, cs; sincosf(dt * li, &sn, &cs);
        const f32x2 a1 = {e * cs, e * sn};
        const float nr = a1.x - 1.f, den = lr * lr + li * li;
        f32x2 a16 = cmul(a1, a1); a16 = cmul(a16, a16); a16 = cmul(a16, a16); a16 = cmul(a16, a16);
        const f32x2 a32 = cmul(a16, a16), a48 = cmul(a32, a16), a64 = cmul(a32, a32), a128 = cmul(a64, a64), a192 = cmul(a128, a64), a256 = cmul(a128, a128);
        if (lane < 32) {
            LAS f32x4* ct = (LAS f32x4*)(lds + K1_CTAB + lane * 96);
            ct[0] = (f32x4){a1.x, a1.y, 2.f * ((nr * lr + a1.y * li) / den), 2.f * ((a1.y * lr - nr * li) / den)};
            ct[1] = (f32x4){1.f, 0.f, a16.x, a16.y}; ct[2] = (f32x4){a32.x, a32.y, a48.x, a48.y};
            ct[3] = (f32x4){1.f, 0.f, a64.x, a64.y}; ct[4] = (f32x4){a128.x, a128.y, a192.x, a192.y};
            ct[5] = (f32x4){a256.x, a256.y, 0.f, 0.f};
        }
    }
    FSTAMP(5);
    if (wc == 1) BAR();
    STAMP_TAKE(st1);
    LT(1);
    FSTAMP(6);
#pragma nounroll
    for (int t = 0; t < 16; t += 2) {
        asm volatile("" : "+s"(t));
        const unsigned ko = t == 14 ? 0x80000000u : (unsigned)(t + 2) * 128u;
        const unsigned kp = t < 3 ? (unsigned)(t + 1) * 512u : 0x80000000u, kq = t < 2 ? (unsigned)(t + 2) * 512u : 0x80000000u;
        const unsigned k0 = pf ? kp : ko, k1 = pf ? kq : ko + 128;
        BAR(); FSTAMP(8 + t); RD_P1(0);
        ST_U1(t + 1, 1); if (!pf) { ST_U2(t + 1, 1); WAIT_V(10); }
        GO(7, 8, 0);
        BAR(); RD_P2(0);
        ST_X1(k0, 0); if (!pf) ST_X3(ko, 0);
        GO(1, 2, 6);
        BAR(); RD_P3(0);
        ST_G1(t + 2, 0); if (!pf) { ST_G2(t + 2, 0); ST_S(ko, 0); WAIT_V(10); }
        GO(3, 4, 5);
        BAR(); FSTAMP(9 + t); RD_P1(1);
        ST_U1(t + 2, 0); if (!pf) { ST_U2(t + 2, 0); WAIT_V(10); }
        GO(7, 8, 0);
        BAR(); RD_P2(1);
        ST_X1(k1, 1); if (!pf) ST_X3(ko + 128, 1);
        GO(1, 2, 6);
        BAR(); RD_P3(1);
        ST_G1(t + 3, 1); if (!pf) { ST_G2(t + 3, 1); ST_S(ko + 128, 1); WAIT_V(10); }
        else {
            unsigned l128 = voffW; int hb = bid; kptr kh = (kptr)__builtin_amdgcn_kernarg_segment_ptr();
            asm volatile("" : "+v"(l128)); asm volatile("" : "+s"(hb), "+s"(kh));
            const unsigned l16 = l128 >> 3, OUT = 0x80000000u;
            const int hi_ = hb >> 3, hj = (hb & 7) * 4 + (hi_ & 3), hp = hi_ >> 2;
            LAS unsigned* hst = (LAS unsigned*)(lds + K1_HS + 2048);
#define RFL(v) ((unsigned)__builtin_amdgcn_readfirstlane(v))
#define HDMA(m, vo, srd, so) asm volatile("s_nop 4\n\ts_mov_b32 m0, %0\n\ts_nop 0\n\tbuffer_load_dwordx4 %1, %2, %3 offen sc1 lds" ::"s"((unsigned)(m)), "v"(vo), "s"(srd), "s"((unsigned)(so)) : "m0", "memory")
#define HLD(op, v, vo) asm volatile("s_nop 4\n\t" op " %0, %1, %2, 0 offen sc1\n\ts_waitcnt vmcnt(0)" : "=v"(v) : "v"(vo), "s"(s4) : "memory")
#define HST32(off, v) __builtin_amdgcn_raw_buffer_store_b32((unsigned)(v), s4, off, 0, 16)
#define HST64(off, lo_, hi2_) do { __builtin_amdgcn_raw_buffer_store_b64((u32x2){(unsigned)(lo_), (unsigned)(hi2_)}, s4, off, 0, 16); asm volatile("" ::: "memory"); } while (0)
#define REPLY(q_, tag_) do { _Pragma("unroll") for (int i = 0; i < 4; ++i) HST64(WS_HR + l16 * 2 + i * 8, (q_)[i], tag_); } while (0)
#define LANE0(off) (l16 == 0 ? (unsigned)(off) : OUT)
            switch (t) {
            case 0: {
                WAIT_V(6);
                const unsigned ask = *(LAS unsigned*)(lds + K1_HS + 1024 + (hb & 127) * 8) + 1u;
                const u32x4 old = *(LAS u32x4*)(lds + K1_HS + l16);
                ROLE(hb, hi_);
                HST32(LANE0(WS_HQ + hb * 4), ask);
                HST64((hp < 7 ? WS_CARRY + hp * 8192 + hj * 256 : OUT) + (l16 < 32 * 16 ? l16 >> 1 : OUT >> 1), ~0u, ~0u);
                HST32(LANE0(WS_RDY + (hp * 32 + hj) * 4), ~0u);
                HST32((late ? OUT : WS_FLAG2 + tile * 128) + (l16 < 4 * 16 ? l16 * 2 : OUT >> 1), ~0u);
                unsigned tag = RFL(old[0]) + 1u;
                while (__any(old[0] == tag || old[1] == tag || old[2] == tag || old[3] == tag)) ++tag;
                hst[0] = ask; hst[1] = tag; hst[2] = 0u;
            } break;
            case 2: HDMA(ldsb + K1_HS, l16, s4, hb == 0 ? WS_HQ : OUT); break;
            case 4: {
                if (hb == 0) { WAIT_V(6); const u32x4 q = *(LAS u32x4*)(lds + K1_HS + l16); const unsigned tag = hst[1]; REPLY(q, tag); }
                const __amdgpu_buffer_rsrc_t sA = SRD((char*)kh[13] + WS_A2, WS_A2_BYTES), sO = SRD(kh[14], SEQ * DM * 4);
                const unsigned vo = (l128 >> 7) * A2S, vo2 = l128 << 10;
                HDMA(ldsb + K1_SINK, vo, sA, A2S - 128); HDMA(ldsb + K1_SINK, vo, sA, 65 * A2S - 128); HDMA(ldsb + K1_SINK, vo2, sO, 0u);
            } break;
            case 6: HDMA(ldsb + K1_HS + 1024, l16, s4, WS_HR + (hb >> 7) * 1024); break;
            case 8: {
                WAIT_V(6);
                const u32x2 r = *(LAS u32x2*)(lds + K1_HS + 1024 + (hb & 127) * 8);
                if (RFL(r[0]) == RFL(hst[0])) { HST32(LANE0(WS_HA + hb * 4), r[1]); hst[1] = r[1]; hst[2] = 1u; }
            } break;
            case 10:
                HDMA(ldsb + K1_HS, l16, s4, WS_HA);
#pragma unroll
                for (int i = 0; i < 4; ++i) HDMA(ldsb + K1_SINK, l128, s5, 4096 + i * 8192);
                break;
            case 12: {
                WAIT_V(6);
                u32x4 ans = *(LAS u32x4*)(lds + K1_HS + l16);
                const unsigned ask = RFL(hst[0]); unsigned tag = RFL(hst[1]), done = RFL(hst[2]), spins = 0;
#pragma nounroll
                while (!(done && __all(ans[0] == tag && ans[1] == tag && ans[2] == tag && ans[3] == tag)) && ++spins < (1u << 18)) {
                    if (hb == 0) { u32x4 q; HLD("buffer_load_dwordx4", q, WS_HQ + l16); REPLY(q, tag); }
                    u32x2 r; HLD("buffer_load_dwordx2", r, WS_HR + hb * 8);
                    if (!done && RFL(r[0]) == ask) { tag = RFL(r[1]); done = 1u; HST32(LANE0(WS_HA + hb * 4), tag); }
                    HLD("buffer_load_dwordx4", ans, WS_HA + l16);
                }
                HDMA(ldsb + K1_GTAB, l16, s4, WS_G + hp * 1024);
            } break;
            default: WAIT_V(0);
            }
        }
        GO(3, 4, 5);
    }
    if (wc == 0) BAR();
    RELANE();
    STAMP_PUT(0, 0, st0); STAMP_PUT(0, 1, st1);
    STAMP(0, 2);
    LT(2);
    FSTAMP(24);
    kptr ka = (kptr)__builtin_amdgcn_kernarg_segment_ptr(); asm volatile("" : "+s"(ka));
#define KARG(T, i) ((T)ka[i])
    char* const ws2 = KARG(char*, 13);

    LAS char* ctab = lds + K1_CTAB + (wc * 16 + er) * 96;
    LAS char* etab = lds + K1_ETAB + (wc * 16 + er) * 144;
    const f32x4 cst = *(LAS f32x4*)ctab;
    const f32x2 a1 = {cst[0], cst[1]}, cf = {cst[2], cst[3]};
    u32x4 gt[4];
#pragma unroll
    for (int m = 0; m < 4; ++m) gt[m] = *(LAS u32x4*)(lds + K1_GTAB + wr * 256 + eq * 64 + m * 16);
    FSTAMP(25);
    const __amdgpu_buffer_rsrc_t sA2 = SRD(ws2 + WS_A2, WS_A2_BYTES);
#define ST16(voff, v) __builtin_amdgcn_raw_buffer_store_b128(v, sA2, voff, 0, STPOL)
#define PK8(v) (u32x4){pk2(v[0], v[1]), pk2(v[2], v[3]), pk2(v[4], v[5]), pk2(v[6], v[7])}
    const int mblk = pm * 2 + (wr >> 1);
#define IMG_Q(r, q) (unsigned)(((q) >> 1) * 256 + ((q) & 1) * 16 + ((r) & 3) * 32 + mblk * 16384 + (wr & 1) * 1024)
#define IMG_T(r, q) (IMG_Q(r, q) + (unsigned)((((r) >> 2) & 1) * 2048 + ((r) >> 3) * 128 + (j >> 2) * A2S + (j & 3) * 4096))
#define IMG_M(r, q) (IMG_Q(r, q) + (unsigned)((((r) >> 2) & 1) * 2048 + ((r) >> 3) * 512 + (24 + (j >> 2)) * A2S + (j & 3) * 4096))
#define IMG_S(r, q) (IMG_Q(r, q) + (unsigned)((((r) >> 2) & 1) * 4096 + ((r) >> 3) * 2048 + (32 + (j >> 1)) * A2S + (j & 1) * 8192))
    const unsigned lo = IMG_T(er, eq);
#define GLU_HALF(n, hh, o) do { float v[8]; \
        _Pragma("unroll") for (int e = 0; e < 8; ++e) { const int m = 2 * (hh) + (e >> 2), q = e & 3; v[e] = relu(acc[m][n][q]) * (acc[m][3 + (n)][q] * bfhi(gt[m][q])); } \
        o = PK8(v); } while (0)
#define GLU_TILE(n) do { u32x4 o_; GLU_HALF(n, 0, o_); ST16(lo + (wc * 48 + 8 * (n)) * A2S, o_); GLU_HALF(n, 1, o_); ST16(lo + (wc * 48 + 8 * (n)) * A2S + 512, o_); } while (0)
    GLU_TILE(0);
    GLU_TILE(1);
    asm volatile("" ::: "memory");
    {
        f32x2 h = {acc[0][7][0], acc[0][8][0]};
#pragma unroll
        for (int e = 1; e < 16; ++e) h = cfma(a1, h, (f32x2){acc[e >> 2][7][e & 3], acc[e >> 2][8][e & 3]});
        *(LAS f32x2*)(etab + (wr * 4 + eq) * 8) = h;
    }
    WAIT_V(0); WAIT_L0();
    FSTAMP(26);
    BAR(); asm volatile("" ::: "memory");
    gu32* rdy = (gu32*)(ws2 + WS_RDY);
    if (tz == 192) __hip_atomic_store(rdy + pm * 32 + j, 1u, RLX_AGENT);
    gu64* carry = (gu64*)(ws2 + WS_CARRY);
#define CHAN(c, r) (32 * j + 8 * (c) + 4 * ((r) >> 3) + 16 * (((r) >> 2) & 1) + ((r) & 3))
    if (wid < 2 && pm < 7) {
        const f32x2 a16 = *(LAS f32x2*)(lds + K1_CTAB + (wid * 16 + er) * 96 + 24);
        f32x4 E[8];
#pragma unroll
        for (int i = 0; i < 8; ++i) E[i] = *(LAS f32x4*)(lds + K1_ETAB + (wid * 16 + er) * 144 + i * 16);
        f32x2 run = {E[0][0], E[0][1]};
#pragma unroll
        for (int i = 1; i < 16; ++i) run = cfma(a16, run, (f32x2){E[i >> 1][(i & 1) * 2], E[i >> 1][(i & 1) * 2 + 1]});
        unsigned ux = __float_as_uint(run.x), uy = __float_as_uint(run.y);
        if (ux == 0xFFFFFFFFu) ux = 0x7FC00000u;
        if (eq == 0) __hip_atomic_store(carry + pm * 1024 + CHAN(wid, er), ((unsigned long long)uy << 32) | ux, RLX_AGENT);
    }
    STAMP(0, 3);
    FSTAMP(27);
    GLU_TILE(2);
    {
        float v[8];
        const bool lowh = er < 8;
#pragma unroll
        for (int e = 0; e < 8; ++e) {
            const int m = e >> 2, q = e & 3;
            const float v0 = lowh ? relu(acc[m][6][q]) : acc[m][6][q] * bfhi(gt[m][q]);
            const float v1 = lowh ? relu(acc[2 + m][6][q]) : acc[2 + m][6][q] * bfhi(gt[2 + m][q]);
            const float rcv = __int_as_float(__builtin_amdgcn_mov_dpp(__float_as_int(lowh ? v1 : v0), 0x128, 0xf, 0xf, true));
            v[e] = (lowh ? v0 : v1) * rcv;
        }
        ST16(IMG_M(er, eq) + wc * 128, PK8(v));
    }
    LAS f32x4* park = (LAS f32x4*)(lds + 3 * 32768 + wr * 8192 + (tz & 63) * 16);
    if (wc == 1) {
#pragma unroll
        for (int m = 0; m < 4; ++m) { park[m * 128] = acc[m][7]; park[m * 128 + 64] = acc[m][8]; }
    }
    STAMP(0, 4);
    WAIT_L0();
    FSTAMP(28);
    BAR(); asm volatile("" ::: "memory");
    FSTAMP(29);

    ROLE(bid, idx);
    const int n0 = nblk * 128;
    const int rs0 = late ? 3 : 0;

    if (wc == 1) {
        int ty = threadIdx.x; asm volatile("" : "+v"(ty) :: "memory");
        const int ln = ty & 63, e3 = ln >> 3;
#define LEVEL(lvl) do { if (ty == 256) __hip_atomic_store(rdy + pm * 32 + j, (unsigned)(lvl), RLX_AGENT); } while (0)
        const __amdgpu_buffer_rsrc_t sC = SRD(late ? KARG(const u16*, 10) : KARG(const u16*, 9), DM * NP * 2), sW = SRD(KARG(const u16*, 11), FF * DM * 2);
        const unsigned voffA = (unsigned)(ln * 16);
        const unsigned voffC = (unsigned)(e3 * 2048 + (((ln & 7) ^ (e3 & 6)) * 16));
        const unsigned voffD = (unsigned)(((e3 >> 2) * 8 + (e3 & 3)) * 2048 + ((((ln >> 1) & 3) ^ (e3 >> 1)) * 32) + (ln & 1) * 16);
        const unsigned soA = (unsigned)((late ? 0 : 48) * A2S + mb2 * 16384 + wr * 4096);
        const unsigned soC = (unsigned)((n0 + 32 * wr) * 2048);
        const unsigned soD = (unsigned)(((late ? 0 : 48) + 112 * wr) * 2048 + n0 * 2);
        const unsigned mB = ldsb + wr * 4096;
#define DMA_A(i, k, sl) dma16c(mB + (sl) + (i) * 1024, voffA, sA2, soA + (k) * A2S + (i) * 1024)
#define DMA_C(i, p, sl) dma16(mB + (sl) + 16384 + (i) * 1024, voffC, sC, soC + (p) * 128 + (i) * (8 * 2048))
#define DMA_D(i, row, sl) dma16(mB + (sl) + 16384 + (i) * 1024, voffD, sW, soD + (row) * 2048 + ((i) >> 1) * (4 * 2048) + ((i) & 1) * 128)
#define POLL(v, p) asm volatile("global_load_dword %0, %1, off sc1\n\ts_waitcnt vmcnt(0)" : "=v"(v) : "v"(p) : "memory")
#define ASK(v, p) asm volatile("global_load_dword %0, %1, off sc1" : "=v"(v) : "v"(p) : "memory")
#define NEED(v, p, lvl) do { asm volatile("" : "+v"(v) :: "memory"); if (!__all((int)(v) >= (lvl))) { unsigned spins_ = 0, w_; \
            do { __builtin_amdgcn_s_sleep(1); POLL(w_, p); } while (!__all((int)w_ >= (lvl)) && ++spins_ < (1u << 18)); } } while (0)
        gu32* rl = rdy + (mb2 >> 1) * 32 + (ln & 31);
#pragma unroll
        for (int p = 0; p < 3; ++p)
#pragma unroll
            for (int i = 0; i < 4; ++i) DMA_D(i, 448 * p, p * 32768);
        unsigned fv;
        POLL(fv, rl);
        NEED(fv, rl, 1);
        FSTAMP(31);
#pragma unroll
        for (int p = 0; p < 3; ++p)
#pragma unroll
            for (int i = 0; i < 4; ++i) DMA_A(i, p, p * 32768);
        WAIT_V(8); FSTAMP(32); BAR(); FSTAMP(33); LT(3);
        int k = 3, sl = 3 * 32768;
#define STAGE_W() do { const int row_ = 448 * (k & 7) + ((k >> 3) == 3 ? 96 : 16 * (k >> 3)); \
            _Pragma("unroll") for (int i = 0; i < 4; ++i) { DMA_A(i, k, sl); DMA_D(i, row_, sl); } ++k; sl = sl == 4 * 32768 ? 0 : sl + 32768; } while (0)
#define LSYNC() do { WAIT_V(16); BAR(); } while (0)
        STAGE_W(); WAIT_V(12); BAR();
#pragma nounroll
        while (k < 10) { STAGE_W(); LSYNC(); }
        LEVEL(2);
#pragma nounroll
        while (k < 13) { STAGE_W(); LSYNC(); }
        ASK(fv, rl);
        STAGE_W(); LSYNC(); STAGE_W(); LSYNC(); STAGE_W(); LSYNC();
        NEED(fv, rl, 2);
        const int nw = late ? 32 : 24;
#pragma nounroll
        while (k < nw) { STAGE_W(); LSYNC(); }
#pragma nounroll
        for (int p = 0; p < 16; ++p) {
#pragma unroll
            for (int i = 0; i < 4; ++i) { DMA_A(i, k, sl); DMA_C(i, p, sl); }
            ++k; sl = sl == 4 * 32768 ? 0 : sl + 32768;
            LSYNC();
        }
        const int tb = mb2 * 128 + (wr >> 1) * 64 + (wr & 1) * 8 + (ln >> 5) * 16, dcol = n0 + (ln & 31) * 4;
#define ROW(i) (tb + ((i) >> 3) * 32 + ((i) & 7))
        LT(4);
        FSTAMP(40);
        gu32* flag = (gu32*)(ws2 + WS_FLAG2 + tile * 128 + wr * 32);
        const __amdgpu_buffer_rsrc_t sP = SRD(ws2 + WS_A2 + (size_t)(KSTEPS2 + (tile >> 2)) * A2S + (tile & 3) * 65536, 65536);
        const int wn1 = (ln >> 4) & 1, s0 = rs0 + wn1, s1 = rs0 + 2 + wn1;
        const int inp = wr * 8192 + ((ln >> 2) & 3) * 1024 + (ln & 3) * 256, sw = (ln & 15) ^ ((ln >> 5) * 8);
        LAS char* q0 = lds + (s0 >= 5 ? s0 - 5 : s0) * 32768 + inp;
        LAS char* q1 = lds + (s1 >= 5 ? s1 - 5 : s1) * 32768 + inp;
#define SUMS(i) (*(LAS f32x4*)(q0 + ((i) >> 3) * 4096 + ((sw ^ ((i) & 7)) * 16)) + *(LAS f32x4*)(q1 + ((i) >> 3) * 4096 + ((sw ^ ((i) & 7)) * 16)))
        f32x4 ex[16];
        if (late) {
            const __amdgpu_buffer_rsrc_t sO = SRD(KARG(float*, 14), SEQ * DM * 4);
            WAIT_V(8); BAR();
            FSTAMP(41);
            ASK(fv, flag);
            WAIT_V(0); BAR();
            FSTAMP(42);
            NEED(fv, flag, 1);
            FSTAMP(43);
#pragma unroll
            for (int i = 0; i < 16; ++i) ex[i] = __builtin_bit_cast(f32x4, __builtin_amdgcn_raw_buffer_load_b128(sP, (wr * 16 + i) * 1024 + ln * 16, 0, 16));
            FSTAMP(44);
            BAR(); asm volatile("" ::: "memory");
            FSTAMP(45);
            STAMP(1, 3);
#pragma unroll
            for (int i = 0; i < 16; ++i) __builtin_amdgcn_raw_buffer_store_b128(__builtin_bit_cast(u32x4, ex[i] + SUMS(i)), sO, (ROW(i) * DM + dcol) * 4, 0, OUTPOL);
        } else {
            const __amdgpu_buffer_rsrc_t sXk = SRD(KARG(const u16*, 0), SEQ * DM * 2), sDk = SRD(KARG(const u16*, 12), DM * 2), sGk = SRD(ws2 + WS_G, 8192);
            WAIT_V(8); BAR();
            FSTAMP(41);
            u32x2 dk = __builtin_amdgcn_raw_buffer_load_b64(sDk, dcol * 2, 0, 0), xv[16]; u32x4 gwd[4];
#pragma unroll
            for (int i = 0; i < 4; ++i) gwd[i] = __builtin_amdgcn_raw_buffer_load_b128(sGk, ROW(4 * i) * 4, 0, 16);
#pragma unroll
            for (int i = 0; i < 16; ++i) xv[i] = __builtin_amdgcn_raw_buffer_load_b64(sXk, (tb * DM + dcol) * 2 + (i & 1) * 2048, (((i) >> 3) * 32 + ((i) & 6)) * 2048, 0);
            WAIT_V(21); BAR();
            FSTAMP(42);
            asm volatile("" : "+v"(dk), "+v"(gwd[0]), "+v"(gwd[1]), "+v"(gwd[2]), "+v"(gwd[3]));
#pragma unroll
            for (int i = 0; i < 16; ++i) {
                const float g = bflo(gwd[i >> 2][i & 3]);
                ex[i] = (f32x4){g * rbf(bflo(xv[i][0]) * bflo(dk[0])), g * rbf(bfhi(xv[i][0]) * bfhi(dk[0])),
                                g * rbf(bflo(xv[i][1]) * bflo(dk[1])), g * rbf(bfhi(xv[i][1]) * bfhi(dk[1]))};
                asm volatile("" : "+v"(ex[i]));
            }
            FSTAMP(44);
            BAR(); asm volatile("" ::: "memory");
            FSTAMP(45);
            STAMP(1, 3);
#pragma unroll
            for (int i = 0; i < 16; ++i) __builtin_amdgcn_raw_buffer_store_b128(__builtin_bit_cast(u32x4, ex[i] + SUMS(i)), sP, (wr * 16 + i) * 1024 + ln * 16, 0, 16);
            FSTAMP(46);
            WAIT_V(0);
            if (ln == 0) __hip_atomic_store(flag, 1u, RLX_AGENT);
        }
        STAMP(1, 6);
        STAMP(1, 7);
        FSTAMP(47);
        FSTAMP(48);
        FDUMP();
        asm volatile(".Lk1_end_a:");
        return;
    }

    {
        unsigned long long gq[2][7];
#pragma unroll
        for (int c = 0; c < 2; ++c)
#pragma unroll
            for (int q = 0; q < 7; ++q) gq[c][q] = __hip_atomic_load(carry + (q + pm >= 7 ? q + pm - 7 : 0) * 1024 + CHAN(c, er), RLX_AGENT);
        f32x2 h[2];
        {
            f32x2 a16[2], run[2]; f32x4 E[2][6], F0[2]; f32x2 F1[2];
#pragma unroll
            for (int c = 0; c < 2; ++c) {
                a16[c] = *(LAS f32x2*)(ctab + c * (16 * 96) + 24);
#pragma unroll
                for (int i = 0; i < 6; ++i) E[c][i] = *(LAS f32x4*)(etab + c * (16 * 144) + i * 16);
                F0[c] = *(LAS f32x4*)(etab + c * (16 * 144) + wr * 32); F1[c] = *(LAS f32x2*)(etab + c * (16 * 144) + wr * 32 + 16);
                run[c] = (f32x2){0.f, 0.f};
            }
#pragma unroll
            for (int w = 0; w < 3; ++w)
                if (wr > w) {
#pragma unroll
                    for (int i = 0; i < 4; ++i)
#pragma unroll
                        for (int c = 0; c < 2; ++c) run[c] = cfma(a16[c], run[c], (f32x2){E[c][2 * w + (i >> 1)][(i & 1) * 2], E[c][2 * w + (i >> 1)][(i & 1) * 2 + 1]});
                }
#pragma unroll
            for (int c = 0; c < 2; ++c) {
                const f32x2 r1 = cfma(a16[c], run[c], (f32x2){F0[c][0], F0[c][1]}), r2 = cfma(a16[c], r1, (f32x2){F0[c][2], F0[c][3]}), r3 = cfma(a16[c], r2, F1[c]);
                h[c] = sel4(eq, run[c], r1, r2, r3);
            }
        }
        f32x4 ur[4], ui[4];
#pragma unroll
        for (int m = 0; m < 4; ++m) { ur[m] = park[m * 128]; ui[m] = park[m * 128 + 64]; }
        const f32x4 cs1 = *(LAS f32x4*)(ctab + 16 * 96);
        FSTAMP(30);
        f32x2 a256[2], aq[2];
#pragma unroll
        for (int c = 0; c < 2; ++c) {
            LAS char* ct = ctab + c * (16 * 96);
            a256[c] = *(LAS f32x2*)(ct + 80); aq[c] = cmul(*(LAS f32x2*)(ct + 16 + eq * 8), *(LAS f32x2*)(ct + 48 + wr * 8));
        }
        unsigned mx = 0;
#pragma unroll
        for (int q = 0; q < 7; ++q) mx = max(mx, max((unsigned)gq[0][q], (unsigned)gq[1][q]));
        if (pm > 0 && __any(mx == 0xFFFFFFFFu)) {
            unsigned spins = 0;
            do {
                mx = 0;
#pragma unroll
                for (int c = 0; c < 2; ++c)
#pragma unroll
                    for (int q = 0; q < 7; ++q) {
                        gq[c][q] = __hip_atomic_load(carry + (q + pm >= 7 ? q + pm - 7 : 0) * 1024 + CHAN(c, er), RLX_AGENT);
                        mx = max(mx, (unsigned)gq[c][q]);
                    }
            } while (__any(mx == 0xFFFFFFFFu) && ++spins < (1u << 18));
        }
        f32x2 cg[2] = {{0.f, 0.f}, {0.f, 0.f}};
#pragma unroll
        for (int q = 0; q < 7; ++q) {
            const unsigned keep = q + pm >= 7 ? 0xFFFFFFFFu : 0u;
#pragma unroll
            for (int c = 0; c < 2; ++c) cg[c] = cfma(a256[c], cg[c], (f32x2){__uint_as_float((unsigned)gq[c][q] & keep), __uint_as_float((unsigned)(gq[c][q] >> 32) & keep)});
        }
        h[0] = cfma(aq[0], cg[0], h[0]); h[1] = cfma(aq[1], cg[1], h[1]);
        STAMP(0, 5);
        FSTAMP(31);
        const unsigned ls = IMG_S(er, eq);
        const f32x2 b1 = {cs1[0], cs1[1]}, bf = {cs1[2], cs1[3]};
#pragma unroll
        for (int hh = 0; hh < 2; ++hh) {
            float vr[8], vi[8], wr8[8], wi8[8];
#pragma unroll
            for (int e = 0; e < 8; ++e) {
                const int m = 2 * hh + (e >> 2), q = e & 3;
                h[0] = cfma(a1, h[0], (f32x2){acc[m][7][q], acc[m][8][q]});
                h[1] = cfma(b1, h[1], (f32x2){ur[m][q], ui[m][q]});
                const float g2 = bflo(gt[m][q]);
                const f32x2 s0 = cmul(cf, h[0]), s1 = cmul(bf, h[1]);
                vr[e] = s0.x * g2; vi[e] = -s0.y * g2; wr8[e] = s1.x * g2; wi8[e] = -s1.y * g2;
            }
            ST16(ls + 40 * A2S + hh * 512, PK8(vr)); ST16(ls + 40 * A2S + hh * 512 + 128, PK8(wr8));
            ST16(ls + hh * 512, PK8(vi)); ST16(ls + hh * 512 + 128, PK8(wi8));
        }
    }
    STAMP(0, 6);
    int ty = threadIdx.x; asm volatile("" : "+v"(ty) :: "memory");
    const int ln = ty & 63, fr2 = ty & 15, fq2 = (ty >> 4) & 3;
    const int wn = wid & 1, kk = wid >> 1;
    LAS char* rH = lds + kk * 8192 + (ln >> 5) * 4096 + (ln & 31) * 8;
    LAS char* rC = lds + 16384 + (wn * 64 + fr2) * 128 + (((4 * kk + fq2) ^ (fr2 & 6)) * 16);
    LAS char* rWb = lds + 16384 + kk * 8192 + (ln >> 5) * 4096 + wn * 1024 + ((ln >> 2) & 7) * 128 + (ln & 3) * 8;
    const int rsw = (ln >> 3) & 3;
    f32x4 ac2[4][8];
#pragma unroll
    for (int n = 0; n < 4; ++n)
#pragma unroll
        for (int m = 0; m < 8; ++m) { ac2[n][m] = (f32x4){0.f, 0.f, 0.f, 0.f}; asm volatile("" : "+v"(ac2[n][m])); }
    STAMP(0, 7);
    FSTAMP(32);
    FDUMP();
    s16x4 HX[8][2], HY[8][2], WX[4][2], WY[4][2];
    int sl = 0;

#define RD1(RD, i, H, W) do { \
        if ((i) < 8) { \
            if (RD == 1) W[(i) >> 1][(i) & 1] = tr4(rWb + sl + ((((i) >> 1) ^ rsw) * 32) + ((i) & 1) * 2048); \
            else if (((i) & 1) == 0) { const s16x8 v_ = *(LAS s16x8*)(rC + sl + ((i) >> 1) * 2048); \
                W[(i) >> 1][0] = __builtin_shufflevector(v_, v_, 0, 1, 2, 3); W[(i) >> 1][1] = __builtin_shufflevector(v_, v_, 4, 5, 6, 7); } \
        } else H[((i) - 8) >> 1][(i) & 1] = tr4(rH + sl + (((i) - 8) >> 1) * 256 + ((i) & 1) * 2048); \
    } while (0)
#define LGKM0() __builtin_amdgcn_s_waitcnt(0xC07F)
#define MM(i, HC, WC) mfma_acc(ac2[(i) & 3][(i) >> 2], cat8(WC[(i) & 3][0], WC[(i) & 3][1]), cat8(HC[(i) >> 2][0], HC[(i) >> 2][1]))
#define STEP(RD, HC, WC, HN, WN) do { \
        _Pragma("unroll") for (int i = 0; i < 32; ++i) { MM(i, HC, WC); if (i < 24) RD1(RD, i, HN, WN); } \
        sl = sl == 4 * 32768 ? 0 : sl + 32768; LGKM0(); \
    } while (0)

    LGKM0();
    BAR();
    STAMP_TAKE(st9);
#pragma unroll
    for (int i = 0; i < 24; ++i) RD1(1, i, HX, WX);
    sl = 32768; LGKM0();
    const int nf = late ? 15 : 11;
#pragma nounroll
    for (int i = 0; i < nf; ++i) {
        if (i == 3) WAIT_V(0);
        BAR(); STEP(1, HX, WX, HY, WY); BAR(); STEP(1, HY, WY, HX, WX);
    }
    BAR(); STEP(1, HX, WX, HY, WY); BAR(); STEP(0, HY, WY, HX, WX);
#pragma nounroll
    for (int i = 0; i < 7; ++i) { BAR(); STEP(0, HX, WX, HY, WY); BAR(); STEP(0, HY, WY, HX, WX); }
    BAR(); STEP(0, HX, WX, HY, WY);
    {
        const int sw = rs0 + wid;
        LAS char* dmp = lds + (sw >= 5 ? sw - 5 : sw) * 32768 + fq2 * 256;
#define DUMP(i) *(LAS f32x4*)(dmp + (i) * 1024 + ((fr2 ^ (4 * ((i) & 3) + fq2)) * 16)) = ac2[(i) & 3][(i) >> 2]
#pragma unroll
        for (int i = 0; i < 32; ++i) { MM(i, HY, WY); if (i >= 12) DUMP(i - 12); }
        asm volatile("s_nop 15" ::: "memory");
#pragma unroll
        for (int i = 20; i < 32; ++i) DUMP(i);
    }
    WAIT_L0();
    STAMP_PUT(1, 0, st0); STAMP_PUT(1, 1, st9);
    STAMP(1, 2);
    BAR();
    asm volatile(".Lk1_end_b:");
}

extern "C" void kernel_launch(void* const* d_in, const int* in_sizes, int n_in, void* d_out, int out_size, void* d_ws, size_t ws_size,
                              hipStream_t stream) {
    const u16* x = (const u16*)d_in[0];
    const u16* lre = (const u16*)d_in[1];
    const u16* lim = (const u16*)d_in[2];
    const u16* Bre = (const u16*)d_in[3];
    const u16* Bim = (const u16*)d_in[4];
    const u16* Cre = (const u16*)d_in[5];
    const u16* Cim = (const u16*)d_in[6];
    const u16* logdt = (const u16*)d_in[7];
    const u16* dskip = (const u16*)d_in[8];
    const u16* Wg = (const u16*)d_in[10];
    const u16* Wu = (const u16*)d_in[11];
    const u16* Wd = (const u16*)d_in[12];
    const u16* gw = (const u16*)d_in[13];
    char* ws = (char*)d_ws;
    (void)hipFuncSetAttribute((const void*)k1_fused, hipFuncAttributeMaxDynamicSharedMemorySize, K2_LDS);
    k1_fused<<<256, 512, K2_LDS, stream>>>(x, Wg, Wu, Bre, Bim, lre, lim, logdt, gw, Cre, Cim, Wd, dskip, ws, (float*)d_out);
}
```

```cpp
#include <hip/hip_runtime.h>
#include <hip/hip_bf16.h>

typedef unsigned short u16;
typedef __bf16 bf16x8 __attribute__((ext_vector_type(8)));
typedef __bf16 bf16x2 __attribute__((ext_vector_type(2)));
typedef short s16x4 __attribute__((ext_vector_type(4)));
typedef short s16x8 __attribute__((ext_vector_type(8)));
typedef float f32x4 __attribute__((ext_vector_type(4)));
typedef float f32x2 __attribute__((ext_vector_type(2)));
typedef unsigned u32x4 __attribute__((ext_vector_type(4)));
typedef unsigned u32x2 __attribute__((ext_vector_type(2)));
#define LAS __attribute__((address_space(3)))
#define GAS __attribute__((address_space(1)))
typedef GAS unsigned gu32;
typedef GAS unsigned long long gu64;
typedef __attribute__((address_space(4))) const unsigned long long* kptr;
#define RLX_AGENT __ATOMIC_RELAXED, __HIP_MEMORY_SCOPE_AGENT

constexpr int SEQ = 2048, DM = 1024, NP = 1024, FF = 3584;
constexpr int KSTEPS2 = (2 * NP + FF) / 64;

constexpr unsigned WS_CARRY = 0;
constexpr unsigned WS_G = 7 * 8192;
constexpr unsigned WS_RDY = 65536;
constexpr unsigned WS_FLAG2 = WS_RDY + 1024;
constexpr unsigned WS_HQ = WS_FLAG2 + 16384;
constexpr unsigned WS_HA = WS_HQ + 1024;
constexpr unsigned WS_HR = WS_HA + 1024;
constexpr unsigned WS_SYNC_BYTES = WS_HR + 2048;
constexpr size_t WS_A2 = 1 << 20;
constexpr unsigned A2S = 262144 + 128;
constexpr size_t WS_A2_BYTES = (size_t)(KSTEPS2 + 32) * A2S;

__device__ __forceinline__ float bflo(unsigned v) { return __uint_as_float(v << 16); }
__device__ __forceinline__ float bfhi(unsigned v) { return __uint_as_float(v & 0xffff0000u); }
__device__ __forceinline__ float rbf(float f) { return (float)(__bf16)f; }
__device__ __forceinline__ unsigned pk2(float a, float b) {
    bf16x2 v; v.x = (__bf16)a; v.y = (__bf16)b; return __builtin_bit_cast(unsigned, v);
}
__device__ __forceinline__ float relu(float v) { return __int_as_float(max(__float_as_int(v), 0)); }
__device__ __forceinline__ float dot2(unsigned a, unsigned b, float c) {
    return __builtin_amdgcn_fdot2_f32_bf16(__builtin_bit_cast(bf16x2, a), __builtin_bit_cast(bf16x2, b), c, false);
}

constexpr size_t WS_DBG = 200u << 20;
#define STAMP(kid, i)
#define STAMP_TAKE(v)
#define STAMP_PUT(kid, i, v)

#define FSTAMP(i)
#define FDUMP()
#define WAIT_V(n) asm volatile("s_waitcnt vmcnt(" #n ")" ::: "memory")
#define WAIT_L0() asm volatile("s_waitcnt lgkmcnt(0)" ::: "memory")
#define BAR() __builtin_amdgcn_s_barrier()
#define SCHED() __builtin_amdgcn_sched_barrier(0)
#define SRD(p, bytes) __builtin_amdgcn_make_buffer_rsrc((void*)(p), (short)0, (int)(bytes), 0x00020000)

__device__ __forceinline__ void dma16(unsigned m0v, unsigned voff, __amdgpu_buffer_rsrc_t srd, unsigned soff) {
    asm volatile("s_mov_b32 m0, %0\n\ts_nop 0\n\tbuffer_load_dwordx4 %1, %2, %3 offen lds" ::"s"(m0v), "v"(voff), "s"(srd), "s"(soff) : "m0", "memory");
}
__device__ __forceinline__ bf16x8 lds_b128(LAS char* p) { return __builtin_bit_cast(bf16x8, *(LAS s16x8*)p); }
__device__ __forceinline__ bf16x8 lds_tr(LAS char* p0, LAS char* p1) {
    s16x4 a = __builtin_amdgcn_ds_read_tr16_b64_v4i16((LAS s16x4*)p0);
    s16x4 b = __builtin_amdgcn_ds_read_tr16_b64_v4i16((LAS s16x4*)p1);
    return __builtin_bit_cast(bf16x8, __builtin_shufflevector(a, b, 0, 1, 2, 3, 4, 5, 6, 7));
}
#define MFMA(a, b, c) __builtin_amdgcn_mfma_f32_16x16x32_bf16(a, b, c, 0, 0, 0)
__device__ __forceinline__ f32x2 cmul(f32x2 a, f32x2 b) { return (f32x2){__builtin_fmaf(a.x, b.x, -(a.y * b.y)), __builtin_fmaf(a.x, b.y, a.y * b.x)}; }
__device__ __forceinline__ f32x2 cfma(f32x2 a, f32x2 b, f32x2 c) {
    return (f32x2){__builtin_fmaf(a.x, b.x, __builtin_fmaf(-a.y, b.y, c.x)), __builtin_fmaf(a.x, b.y, __builtin_fmaf(a.y, b.x, c.y))};
}
__device__ __forceinline__ f32x2 sel4(int i, f32x2 a, f32x2 b, f32x2 c, f32x2 d) {
    const bool o = i & 1, u = i & 2;
    const float lx = o ? b.x : a.x, ly = o ? b.y : a.y, hx = o ? d.x : c.x, hy = o ? d.y : c.y;
    return (f32x2){u ? hx : lx, u ? hy : ly};
}

#define STPOL 16
#define PF_W0 1
#define PF_D0 4
#define OUTPOL 16

#define K2_SPLIT 40
#define MAP 0
#define ABL_NOLOAD 0
#define ABL_NOREAD 0
#define ABL_NOMFMA 0
#define CODE_T 10
#define ILV 1
#define SHIFT 2
#define GATE2 1
#define LATE_FLIP 0
#define DIST 5
constexpr int K2_LDS = 5 * 32768;

__device__ __forceinline__ void dma16c(unsigned m0v, unsigned voff, __amdgpu_buffer_rsrc_t srd, unsigned soff) {
    asm volatile("s_mov_b32 m0, %0\n\ts_nop 0\n\tbuffer_load_dwordx4 %1, %2, %3 offen sc1 lds" ::"s"(m0v), "v"(voff), "s"(srd), "s"(soff) : "m0", "memory");
}
__device__ __forceinline__ void mfma_acc(f32x4& c, bf16x8 a, bf16x8 b) {
    asm volatile("v_mfma_f32_16x16x32_bf16 %0, %1, %2, %0" : "+v"(c) : "v"(a), "v"(b) : "memory");
}
__device__ __forceinline__ s16x4 tr4(LAS char* p) { return __builtin_amdgcn_ds_read_tr16_b64_v4i16((LAS s16x4*)p); }
__device__ __forceinline__ bf16x8 cat8(s16x4 a, s16x4 b) { return __builtin_bit_cast(bf16x8, __builtin_shufflevector(a, b, 0, 1, 2, 3, 4, 5, 6, 7)); }

constexpr int K1_ASB = 40960;
constexpr int K1_W = 81920;
constexpr int K1_WBUF = 28704, K1_UOFF = 14352;
constexpr int K1_ETAB = K1_W + 2 * K1_WBUF;
constexpr int K1_CTAB = K1_ETAB + 32 * 144;
constexpr int K1_SINK = K1_CTAB + 32 * 96;
constexpr int K1_GTAB = K1_SINK + 1024;
constexpr int K1_HS = K1_GTAB + 1024;

__global__ __launch_bounds__(512, 2) void k1_fused(const u16* __restrict__ x, const u16* __restrict__ Wg, const u16* __restrict__ Wu,
                                                       const u16* __restrict__ Bre, const u16* __restrict__ Bim, const u16* __restrict__ lre,
                                                       const u16* __restrict__ lim, const u16* __restrict__ logdt, const u16* __restrict__ gw,
                                                       const u16* __restrict__ Cre, const u16* __restrict__ Cim, const u16* __restrict__ Wd,
                                                       const u16* __restrict__ dskip, char* __restrict__ ws, float* __restrict__ out) {
    extern __shared__ __attribute__((aligned(16))) char smem[];
    LAS char* lds = (LAS char*)smem;
    const unsigned ldsb = (unsigned)(size_t)lds;
    const int tid = threadIdx.x, lane = tid & 63;
    const int wid = __builtin_amdgcn_readfirstlane(tid >> 6);
    const int wr = wid & 3, wc = wid >> 2;
    const int bid = blockIdx.x, idx = bid >> 3, j = (bid & 7) * 4 + (idx & 3), pm = idx >> 2;
    const int fr = lane & 15, fq = lane >> 4;
#define ROLE(b_, i_) const int xc = (b_) & 7, late = (xc & 1) ^ LATE_FLIP, mb2 = (xc >> 2) * 8 + ((i_) >> 2), nblk = ((xc >> 1) & 1) * 4 + ((i_) & 3), tile = mb2 * 8 + nblk
    STAMP_TAKE(st0);
#define LT(n)
    FSTAMP(0);

    const char* code0; unsigned codeA, codeB;
    asm volatile("s_getpc_b64 %0\n.Lk1_ref:\n\ts_mov_b32 %1, .Lk1_end_a-.Lk1_ref\n\ts_mov_b32 %2, .Lk1_end_b-.Lk1_ref" : "=s"(code0), "=s"(codeA), "=s"(codeB));
    const unsigned codeN = (codeA > codeB ? codeA : codeB) + 192;
    if (wid == 7) {
        const __amdgpu_buffer_rsrc_t sI = SRD(code0, codeN);
        dma16(ldsb + K1_SINK, (unsigned)(lane * 128), sI, 0u); dma16(ldsb + K1_SINK, (unsigned)(lane * 128), sI, 8192u);
    }

    const int ch = 32 * j + 8 * (fq & 1) + 4 * (fr >> 3) + 16 * ((fr >> 2) & 1) + (fr & 3);
    const int grow = pm * 256 + j * 8 + wid;
    u32x4 xa, xb, ga, gb; unsigned plr, pli, pld;
    {
        const u16* xr = x + (size_t)grow * DM;
        const unsigned l16 = lane * 16, c2 = ch * 2;
        asm volatile("s_nop 4\n\tglobal_load_dwordx4 %0, %7, %9\n\tglobal_load_dwordx4 %1, %7, %9 offset:1024\n\t"
                     "global_load_dwordx4 %2, %7, %10\n\tglobal_load_dwordx4 %3, %7, %10 offset:1024\n\t"
                     "global_load_ushort %4, %8, %11\n\tglobal_load_ushort %5, %8, %12\n\tglobal_load_ushort %6, %8, %13"
                     : "=&v"(xa), "=&v"(xb), "=&v"(ga), "=&v"(gb), "=&v"(plr), "=&v"(pli), "=&v"(pld)
                     : "v"(l16), "v"(c2), "s"(xr), "s"(gw), "s"(lre), "s"(lim), "s"(logdt) : "memory");
    }

    const bool pf = wid == 7;
    const int q4 = wid + 28, q5 = wid + 35;
    const __amdgpu_buffer_rsrc_t sX = SRD(!pf || !(bid & 32) ? x : (bid & 16) ? Bim : Bre, !pf || !(bid & 32) ? SEQ * DM * 2 : NP * DM * 2);
    const int pb = (bid >> 3) | ((bid & 7) << 5);
    const __amdgpu_buffer_rsrc_t sG = SRD(!pf ? Wg : (pb & 1) ? Wd : (pb & 2) ? Cim : Cre, (pf && !(pb & 1)) ? DM * NP * 2 : DM * FF * 2);
    const __amdgpu_buffer_rsrc_t sU = SRD((pf && (pb & 1)) ? Wg : Wu, DM * FF * 2);
    const __amdgpu_buffer_rsrc_t s4 = SRD(pf ? (const u16*)ws : q4 < 32 ? x : (q4 & 2) ? Bim : Bre, pf ? WS_SYNC_BYTES : q4 < 32 ? SEQ * DM * 2 : NP * DM * 2);
    const __amdgpu_buffer_rsrc_t s5 = SRD(pf ? (const u16*)code0 : (q5 & 2) ? Bim : Bre, pf ? codeN : q5 < 40 ? NP * DM * 2 : 0);

    const int l3 = lane >> 3;
    const unsigned voffX = pf ? (unsigned)(lane * 2048) : (unsigned)(((l3 >> 2) * 16 + (l3 & 3)) * 2048 + (((lane & 7) ^ (l3 & 6)) * 16));
    unsigned voffW;
    { const int gam = wid * 64 + lane, rho = gam / 14, c = gam - rho * 14, k = (rho & 0x13) | ((rho & 4) << 1) | ((rho & 8) >> 1);
      voffW = pf ? (unsigned)(lane * 128) : (unsigned)(k * (FF * 2) + c * 16); }
#define XROW(q) (pm * 256 + ((q) >> 3) * 64 + (((q) >> 2) & 1) * 8 + ((q) & 1) * 32 + (((q) >> 1) & 1) * 4)
#define BROW(q) (32 * j + 8 * (((q) >> 2) & 1) + 4 * ((q) & 1))
    constexpr unsigned WST = 64 * FF * 2, WSH = 32 * FF * 2, FAR = 0x40000000u;
    const unsigned so0 = pf ? 0u : (unsigned)(XROW(wid) * 2048);
    unsigned px = (unsigned)((bid & ((bid & 32) ? 15 : 31)) * 131072 + (bid >> 6) * 128), pxs = 512;
    const unsigned so1 = pf ? FAR : (unsigned)(XROW(wid + 7) * 2048), so2 = pf ? FAR : (unsigned)(XROW(wid + 14) * 2048), so3 = pf ? FAR : (unsigned)(XROW(wid + 21) * 2048),
                   so4 = pf ? FAR : (unsigned)((q4 < 32 ? XROW(q4) : BROW(q4)) * 2048), so5 = pf ? FAR : (unsigned)(BROW(q5) * 2048);
    const unsigned soW = (unsigned)(j * 224), soW2 = pf ? FAR : soW + WSH;
    const unsigned mSink = ldsb + K1_SINK, mb0 = pf ? mSink : ldsb + wid * 1024, mk = pf ? 0u : ~0u;
#define MZ(off) (mb0 + ((unsigned)(off) & mk))
    const unsigned m50 = q5 < 40 ? MZ(35 * 1024) : mSink, m51 = q5 < 40 ? MZ(35 * 1024 + K1_ASB) : mSink;
    const unsigned ust = pf ? (1u << 20) : WST, gst = !pf ? WST : (pb & 1) ? (1u << 20) : (1u << 19);
    const unsigned su0 = pf ? (unsigned)((pb >> 1) * 8192) + (unsigned)(PF_W0) * ust : soW;
    const unsigned sg0 = pf ? (unsigned)(((pb & 1) ? pb >> 1 : pb >> 2) * 8192) - (unsigned)(PF_D0 + 2) * gst : soW;
#define ST_X(i, ko, b) dma16(MZ((b) * K1_ASB + (i) * 7 * 1024), voffX, sX, ((i) == 0 ? so0 : (i) == 1 ? so1 : (i) == 2 ? so2 : so3) + (ko))
#define ST_S4(ko, b) dma16(MZ((b) * K1_ASB + 28 * 1024), voffX, s4, so4 + (ko))
#define ST_S5(ko, b) dma16((b) ? m51 : m50, voffX, s5, so5 + (ko))
#define ST_G1(t, b) dma16(MZ(K1_W + (b) * K1_WBUF), voffW, sG, sg0 + (t) * gst)
#define ST_G2(t, b) dma16(MZ(K1_W + (b) * K1_WBUF + 7168), voffW, sG, soW2 + (t) * WST)
#define ST_U1(t, b) dma16(MZ(K1_W + (b) * K1_WBUF + K1_UOFF), voffW, sU, su0 + (t) * ust)
#define ST_U2(t, b) dma16(MZ(K1_W + (b) * K1_WBUF + K1_UOFF + 7168), voffW, sU, soW2 + (t) * WST)

    ST_X(0, pf ? px : 0u, 0);
    if (!pf) { ST_X(1, 0, 0); ST_X(2, 0, 0); ST_X(3, 0, 0); ST_S4(0, 0); ST_S5(0, 0); ST_G1(0, 0); ST_G2(0, 0); ST_U2(0, 0); }
    ST_U1(0, 0);
    FSTAMP(1);

    const int ch0 = (fq ^ (fr & 6)) * 16;
    LAS char* rA0 = lds + wr * 8192 + fr * 128 + ch0;
    LAS char* rA1 = lds + wr * 8192 + fr * 128 + (ch0 ^ 64);
    LAS char* rS0 = lds + 32768 + wc * 4096 + fr * 128 + ch0;
    LAS char* rS1 = lds + 32768 + wc * 4096 + fr * 128 + (ch0 ^ 64);
    const int trow = (16 * (fq >> 1) + 4 * (fq & 1) + ((lane & 15) >> 2)) * 224;
    LAS char* rW = lds + K1_W + trow + wc * 96 + (lane & 3) * 8;
    LAS char* rM = lds + K1_W + trow + 192 + wc * 16 + (lane & 1) * 8 + ((lane >> 1) & 1) * K1_UOFF;

    f32x4 acc[4][9];
#pragma unroll
    for (int m = 0; m < 4; ++m)
#pragma unroll
        for (int n = 0; n < 9; ++n) { acc[m][n] = (f32x4){0.f, 0.f, 0.f, 0.f}; asm volatile("" : "+v"(acc[m][n])); }
    bf16x8 Af[4][2], Bf[3][2];

#define RD_A(b) do { _Pragma("unroll") for (int m = 0; m < 4; ++m) { Af[m][0] = lds_b128(rA0 + (b) * K1_ASB + m * 2048); Af[m][1] = lds_b128(rA1 + (b) * K1_ASB + m * 2048); } } while (0)
#define RD_S(slot, ri, b) do { Bf[slot][0] = lds_b128(rS0 + (b) * K1_ASB + (ri) * 2048); Bf[slot][1] = lds_b128(rS1 + (b) * K1_ASB + (ri) * 2048); } while (0)
#define RD_W(slot, base, off) do { Bf[slot][0] = lds_tr((base) + (off), (base) + (off) + 1792); Bf[slot][1] = lds_tr((base) + (off) + 7168, (base) + (off) + 7168 + 1792); } while (0)
#define MMA3(n0, n1, n2) do { __builtin_amdgcn_s_setprio(1); \
        _Pragma("unroll") for (int kk = 0; kk < 2; ++kk) _Pragma("unroll") for (int m = 0; m < 4; ++m) { \
            acc[m][n0] = MFMA(Af[m][kk], Bf[0][kk], acc[m][n0]); acc[m][n1] = MFMA(Af[m][kk], Bf[1][kk], acc[m][n1]); acc[m][n2] = MFMA(Af[m][kk], Bf[2][kk], acc[m][n2]); } \
        __builtin_amdgcn_s_setprio(0); } while (0)
#define RD_AM(m, b) do { Af[m][0] = lds_b128(rA0 + (b) * K1_ASB + (m) * 2048); Af[m][1] = lds_b128(rA1 + (b) * K1_ASB + (m) * 2048); } while (0)
#define SEC1(t, b) do { RD_S(0, 0, b); RD_S(1, 1, b); ST_U1((t) + 1, (b) ^ 1); RD_W(2, rW, (b) * K1_WBUF); RD_AM(0, b); ST_U2((t) + 1, (b) ^ 1); RD_AM(1, b); RD_AM(2, b); RD_AM(3, b); } while (0)
#define SEC2(k0, ko, b) do { ST_X(0, k0, b); RD_W(0, rW, (b) * K1_WBUF + 32); ST_X(1, ko, b); RD_W(1, rW, (b) * K1_WBUF + 64); ST_X(2, ko, b); RD_W(2, rM, (b) * K1_WBUF); ST_X(3, ko, b); } while (0)
#define SEC3(t, ko, b) do { ST_G1((t) + 2, b); RD_W(0, rW, (b) * K1_WBUF + K1_UOFF); ST_G2((t) + 2, b); RD_W(1, rW, (b) * K1_WBUF + K1_UOFF + 32); ST_S4(ko, b); \
        RD_W(2, rW, (b) * K1_WBUF + K1_UOFF + 64); ST_S5(ko, b); } while (0)
#define GO(n0, n1, n2) do { WAIT_L0(); BAR(); SCHED(); MMA3(n0, n1, n2); SCHED(); } while (0)

#define RELANE() int tz = threadIdx.x; asm volatile("" : "+v"(tz)); const int er = tz & 15, eq = (tz >> 4) & 3; \
    const int ech = 32 * j + 8 * wc + 4 * (er >> 3) + 16 * ((er >> 2) & 1) + (er & 3); (void)ech; (void)eq
    asm volatile("" : "+v"(rA0), "+v"(rA1), "+v"(rS0), "+v"(rS1), "+v"(rW), "+v"(rM));
    gu32* gtab = (gu32*)(ws + WS_G);
    WAIT_V(10);
    if (pf) WAIT_V(2);
    asm volatile("" : "+v"(xa), "+v"(xb), "+v"(ga), "+v"(gb), "+v"(plr), "+v"(pli), "+v"(pld) :: "memory");
    FSTAMP(2);
    {
        float s = 0.f, s2 = 0.f;
#pragma unroll
        for (int e = 0; e < 4; ++e) { s = dot2(xa[e], ga[e], s); s2 = dot2(xb[e], gb[e], s2); }
        s += s2;
#define DPP_ADD(ctrl, rmask) s += __int_as_float(__builtin_amdgcn_update_dpp(0, __float_as_int(s), ctrl, rmask, 0xf, true))
        DPP_ADD(0x111, 0xf); DPP_ADD(0x112, 0xf); DPP_ADD(0x114, 0xf); DPP_ADD(0x118, 0xf); DPP_ADD(0x142, 0xa); DPP_ADD(0x143, 0xc);
        const float g = rbf(__builtin_amdgcn_rcpf(1.f + __builtin_amdgcn_exp2f(-1.4426950408889634f * rbf(s))));
        if (lane == 63) __hip_atomic_store(gtab + grow, pk2(g, 1.f - g), RLX_AGENT);
    }
    FSTAMP(3);
    if (!pf) { ST_X(0, 128, 1); ST_X(1, 128, 1); ST_X(2, 128, 1); ST_X(3, 128, 1); ST_S4(128, 1); ST_S5(128, 1); ST_G1(1, 1); ST_G2(1, 1); FSTAMP(4); WAIT_V(11); }
    else {
        dma16c(ldsb + K1_HS + 1024, (unsigned)(lane * 16), s4, WS_HR + (bid >> 7) * 1024);
        dma16c(ldsb + K1_HS, (unsigned)(lane * 16), s4, bid == 0 ? WS_HA : 0x80000000u);
        const float lr = bflo(plr), li = bflo(pli), dt = rbf(expf(bflo(pld)));
        const float e = expf(dt * lr); float sn, cs; sincosf(dt * li, &sn, &cs);
        const f32x2 a1 = {e * cs, e * sn};
        const float nr = a1.x - 1.f, den = lr * lr + li * li;
        f32x2 a16 = cmul(a1, a1); a16 = cmul(a16, a16); a16 = cmul(a16, a16); a16 = cmul(a16, a16);
        const f32x2 a32 = cmul(a16, a16), a48 = cmul(a32, a16), a64 = cmul(a32, a32), a128 = cmul(a64, a64), a192 = cmul(a128, a64), a256 = cmul(a128, a128);
        if (lane < 32) {
            LAS f32x4* ct = (LAS f32x4*)(lds + K1_CTAB + lane * 96);
            ct[0] = (f32x4){a1.x, a1.y, 2.f * ((nr * lr + a1.y * li) / den), 2.f * ((a1.y * lr - nr * li) / den)};
            ct[1] = (f32x4){1.f, 0.f, a16.x, a16.y}; ct[2] = (f32x4){a32.x, a32.y, a48.x, a48.y};
            ct[3] = (f32x4){1.f, 0.f, a64.x, a64.y}; ct[4] = (f32x4){a128.x, a128.y, a192.x, a192.y};
            ct[5] = (f32x4){a256.x, a256.y, 0.f, 0.f};
        }
    }
    FSTAMP(5);
    if (wc == 1) BAR();
    STAMP_TAKE(st1);
    LT(1);
    FSTAMP(6);
#pragma nounroll
    for (int t = 0; t < 16; t += 2) {
        asm volatile("" : "+s"(t));
        const unsigned ko = t == 14 ? 0x80000000u : (unsigned)(t + 2) * 128u;
        const unsigned k0 = pf ? px + pxs : ko, k1 = pf ? px + 2 * pxs : ko + 128;
        px += 2 * pxs;
        BAR(); FSTAMP(8 + t); SEC1(t, 0); if (!pf) WAIT_V(10);
        GO(7, 8, 0);
        BAR(); SEC2(k0, ko, 0);
        GO(1, 2, 6);
        BAR(); SEC3(t, ko, 0); if (!pf) WAIT_V(10);
        GO(3, 4, 5);
        BAR(); FSTAMP(9 + t); SEC1(t + 1, 1); if (!pf) WAIT_V(10);
        GO(7, 8, 0);
        BAR(); SEC2(k1, ko + 128, 1);
        GO(1, 2, 6);
        BAR(); SEC3(t + 1, ko + 128, 1);
        if (!pf) WAIT_V(10);
        else {
            unsigned l128 = voffW; int hb = bid; kptr kh = (kptr)__builtin_amdgcn_kernarg_segment_ptr();
            asm volatile("" : "+v"(l128)); asm volatile("" : "+s"(hb), "+s"(kh));
            const unsigned l16 = l128 >> 3, OUT = 0x80000000u;
            const int hi_ = hb >> 3, hj = (hb & 7) * 4 + (hi_ & 3), hp = hi_ >> 2;
            LAS unsigned* hst = (LAS unsigned*)(lds + K1_HS + 2048);
#define RFL(v) ((unsigned)__builtin_amdgcn_readfirstlane(v))
#define HDMA(m, vo, srd, so) asm volatile("s_nop 4\n\ts_mov_b32 m0, %0\n\ts_nop 0\n\tbuffer_load_dwordx4 %1, %2, %3 offen sc1 lds" ::"s"((unsigned)(m)), "v"(vo), "s"(srd), "s"((unsigned)(so)) : "m0", "memory")
#define HLD(op, v, vo) asm volatile("s_nop 4\n\t" op " %0, %1, %2, 0 offen sc1\n\ts_waitcnt vmcnt(0)" : "=v"(v) : "v"(vo), "s"(s4) : "memory")
#define HST32(off, v) __builtin_amdgcn_raw_buffer_store_b32((unsigned)(v), s4, off, 0, 16)
#define HST64(off, lo_, hi2_) do { __builtin_amdgcn_raw_buffer_store_b64((u32x2){(unsigned)(lo_), (unsigned)(hi2_)}, s4, off, 0, 16); asm volatile("" ::: "memory"); } while (0)
#define REPLY(q_, tag_) do { _Pragma("unroll") for (int i = 0; i < 4; ++i) HST64(WS_HR + l16 * 2 + i * 8, (q_)[i], tag_); } while (0)
#define LANE0(off) (l16 == 0 ? (unsigned)(off) : OUT)
#define AT(v) int t##v = t; asm volatile("" : "+s"(t##v)); if (t##v == v)
            if ((t & 2) == 0) {
                WAIT_V(20);
                AT(0) {
                    const unsigned ask = *(LAS unsigned*)(lds + K1_HS + 1024 + (hb & 127) * 8) + 1u;
                    const u32x4 old = *(LAS u32x4*)(lds + K1_HS + l16);
                    HST32(LANE0(WS_HQ + hb * 4), ask);
                    unsigned tag = RFL(old[0]) + 1u;
                    while (__any(old[0] == tag || old[1] == tag || old[2] == tag || old[3] == tag)) ++tag;
                    hst[0] = ask; hst[1] = tag; hst[2] = 0u;
                }
                AT(4) { if (hb == 0) { const u32x4 q = *(LAS u32x4*)(lds + K1_HS + l16); const unsigned tag = hst[1]; REPLY(q, tag); } }
                AT(8) {
                    const u32x2 r = *(LAS u32x2*)(lds + K1_HS + 1024 + (hb & 127) * 8);
                    const bool hit = r[0] == hst[0];
                    HST32(hit ? LANE0(WS_HA + hb * 4) : OUT, r[1]);
                    hst[hit ? 1 : 3] = r[1]; hst[2] = hit;
                }
                AT(12) {
                    u32x4 ans = *(LAS u32x4*)(lds + K1_HS + l16);
                    unsigned tag = hst[1], done = hst[2];
                    if (!__all(done && ans[0] == tag && ans[1] == tag && ans[2] == tag && ans[3] == tag)) {
                        const unsigned ask = RFL(hst[0]); unsigned spins = 0;
                        tag = RFL(tag); done = RFL(done);
#pragma nounroll
                        do {
                            if (hb == 0) { u32x4 q; HLD("buffer_load_dwordx4", q, WS_HQ + l16); REPLY(q, tag); }
                            u32x2 r; HLD("buffer_load_dwordx2", r, WS_HR + hb * 8);
                            if (!done && RFL(r[0]) == ask) { tag = RFL(r[1]); done = 1u; HST32(LANE0(WS_HA + hb * 4), tag); }
                            HLD("buffer_load_dwordx4", ans, WS_HA + l16);
                        } while (!__all(done && ans[0] == tag && ans[1] == tag && ans[2] == tag && ans[3] == tag) && ++spins < (1u << 18));
                    }
                    HDMA(ldsb + K1_GTAB, l16, s4, WS_G + hp * 1024);
                }
            }
            int tq = t; asm volatile("" : "+s"(tq));
            if (tq & 2) {
                AT(2) {
                    ROLE(hb, hi_);
                    HST64((hp < 7 ? WS_CARRY + hp * 8192 + hj * 256 : OUT) + (l16 < 32 * 16 ? l16 >> 1 : OUT >> 1), ~0u, ~0u);
                    HST32(l16 == 4 * 16 ? WS_RDY + (hp * 32 + hj) * 4 : (late ? OUT : WS_FLAG2 + tile * 128) + (l16 < 4 * 16 ? l16 * 2 : OUT >> 1), ~0u);
                    HDMA(ldsb + K1_HS, l16, s4, hb == 0 ? WS_HQ : OUT);
                    px = OUT; pxs = 0;
                }
                AT(6) {
                    const __amdgpu_buffer_rsrc_t sA = SRD((char*)kh[13] + WS_A2, WS_A2_BYTES), sO = SRD(kh[14], SEQ * DM * 4);
                    const unsigned vo = (l128 >> 7) * A2S, vo2 = l128 << 10;
                    HDMA(ldsb + K1_SINK, vo, sA, A2S - 128); HDMA(ldsb + K1_SINK, vo, sA, 65 * A2S - 128); HDMA(ldsb + K1_SINK, vo2, sO, 0u);
                    HDMA(ldsb + K1_HS + 1024, l16, s4, WS_HR + (hb >> 7) * 1024);
                }
                AT(10) {
#pragma unroll
                    for (int i = 0; i < 3; ++i) HDMA(ldsb + K1_SINK, l128, s5, 8192 + i * 8192);
                    HDMA(ldsb + K1_HS, l16, s4, WS_HA);
                }
                AT(14) WAIT_V(20);
            }
        }
        GO(3, 4, 5);
    }
    if (wc == 0) BAR();
    RELANE();
    STAMP_PUT(0, 0, st0); STAMP_PUT(0, 1, st1);
    STAMP(0, 2);
    LT(2);
    FSTAMP(24);
    kptr ka = (kptr)__builtin_amdgcn_kernarg_segment_ptr(); asm volatile("" : "+s"(ka));
#define KARG(T, i) ((T)ka[i])
    char* const ws2 = KARG(char*, 13);

    LAS char* ctab = lds + K1_CTAB + (wc * 16 + er) * 96;
    LAS char* etab = lds + K1_ETAB + (wc * 16 + er) * 144;
    const f32x4 cst = *(LAS f32x4*)ctab;
    const f32x2 a1 = {cst[0], cst[1]}, cf = {cst[2], cst[3]};
    u32x4 gt[4];
#pragma unroll
    for (int m = 0; m < 4; ++m) gt[m] = *(LAS u32x4*)(lds + K1_GTAB + wr * 256 + eq * 64 + m * 16);
    FSTAMP(25);
    const __amdgpu_buffer_rsrc_t sA2 = SRD(ws2 + WS_A2, WS_A2_BYTES);
#define ST16(voff, v) __builtin_amdgcn_raw_buffer_store_b128(v, sA2, voff, 0, STPOL)
#define PK8(v) (u32x4){pk2(v[0], v[1]), pk2(v[2], v[3]), pk2(v[4], v[5]), pk2(v[6], v[7])}
    const int mblk = pm * 2 + (wr >> 1);
#define IMG_Q(r, q) (unsigned)(((q) >> 1) * 256 + ((q) & 1) * 16 + ((r) & 3) * 32 + mblk * 16384 + (wr & 1) * 1024)
#define IMG_T(r, q) (IMG_Q(r, q) + (unsigned)((((r) >> 2) & 1) * 2048 + ((r) >> 3) * 128 + (j >> 2) * A2S + (j & 3) * 4096))
#define IMG_M(r, q) (IMG_Q(r, q) + (unsigned)((((r) >> 2) & 1) * 2048 + ((r) >> 3) * 512 + (24 + (j >> 2)) * A2S + (j & 3) * 4096))
#define IMG_S(r, q) (IMG_Q(r, q) + (unsigned)((((r) >> 2) & 1) * 4096 + ((r) >> 3) * 2048 + (32 + (j >> 1)) * A2S + (j & 1) * 8192))
    const unsigned lo = IMG_T(er, eq);
#define GLU_HALF(n, hh, o) do { float v[8]; \
        _Pragma("unroll") for (int e = 0; e < 8; ++e) { const int m = 2 * (hh) + (e >> 2), q = e & 3; v[e] = relu(acc[m][n][q]) * (acc[m][3 + (n)][q] * bfhi(gt[m][q])); } \
        o = PK8(v); } while (0)
#define GLU_TILE(n) do { u32x4 o_; GLU_HALF(n, 0, o_); ST16(lo + (wc * 48 + 8 * (n)) * A2S, o_); GLU_HALF(n, 1, o_); ST16(lo + (wc * 48 + 8 * (n)) * A2S + 512, o_); } while (0)
    GLU_TILE(0);
    GLU_TILE(1);
    asm volatile("" ::: "memory");
    {
        f32x2 h = {acc[0][7][0], acc[0][8][0]};
#pragma unroll
        for (int e = 1; e < 16; ++e) h = cfma(a1, h, (f32x2){acc[e >> 2][7][e & 3], acc[e >> 2][8][e & 3]});
        *(LAS f32x2*)(etab + (wr * 4 + eq) * 8) = h;
    }
    WAIT_V(0); WAIT_L0();
    FSTAMP(26);
    BAR(); asm volatile("" ::: "memory");
    gu32* rdy = (gu32*)(ws2 + WS_RDY);
    if (tz == 192) __hip_atomic_store(rdy + pm * 32 + j, 1u, RLX_AGENT);
    gu64* carry = (gu64*)(ws2 + WS_CARRY);
#define CHAN(c, r) (32 * j + 8 * (c) + 4 * ((r) >> 3) + 16 * (((r) >> 2) & 1) + ((r) & 3))
    if (wid < 2 && pm < 7) {
        const f32x2 a16 = *(LAS f32x2*)(lds + K1_CTAB + (wid * 16 + er) * 96 + 24);
        f32x4 E[8];
#pragma unroll
        for (int i = 0; i < 8; ++i) E[i] = *(LAS f32x4*)(lds + K1_ETAB + (wid * 16 + er) * 144 + i * 16);
        f32x2 run = {E[0][0], E[0][1]};
#pragma unroll
        for (int i = 1; i < 16; ++i) run = cfma(a16, run, (f32x2){E[i >> 1][(i & 1) * 2], E[i >> 1][(i & 1) * 2 + 1]});
        unsigned ux = __float_as_uint(run.x), uy = __float_as_uint(run.y);
        if (ux == 0xFFFFFFFFu) ux = 0x7FC00000u;
        if (eq == 0) __hip_atomic_store(carry + pm * 1024 + CHAN(wid, er), ((unsigned long long)uy << 32) | ux, RLX_AGENT);
    }
    STAMP(0, 3);
    FSTAMP(27);
    GLU_TILE(2);
    {
        float v[8];
        const bool lowh = er < 8;
#pragma unroll
        for (int e = 0; e < 8; ++e) {
            const int m = e >> 2, q = e & 3;
            const float v0 = lowh ? relu(acc[m][6][q]) : acc[m][6][q] * bfhi(gt[m][q]);
            const float v1 = lowh ? relu(acc[2 + m][6][q]) : acc[2 + m][6][q] * bfhi(gt[2 + m][q]);
            const float rcv = __int_as_float(__builtin_amdgcn_mov_dpp(__float_as_int(lowh ? v1 : v0), 0x128, 0xf, 0xf, true));
            v[e] = (lowh ? v0 : v1) * rcv;
        }
        ST16(IMG_M(er, eq) + wc * 128, PK8(v));
    }
    LAS f32x4* park = (LAS f32x4*)(lds + 3 * 32768 + wr * 8192 + (tz & 63) * 16);
    if (wc == 1) {
#pragma unroll
        for (int m = 0; m < 4; ++m) { park[m * 128] = acc[m][7]; park[m * 128 + 64] = acc[m][8]; }
    }
    STAMP(0, 4);
    WAIT_L0();
    FSTAMP(28);
    BAR(); asm volatile("" ::: "memory");
    FSTAMP(29);

    ROLE(bid, idx);
    const int n0 = nblk * 128;
    const int rs0 = late ? (48 + SHIFT) % 5 : (40 - SHIFT) % 5;

    if (wc == 1) {
        int ty = threadIdx.x; asm volatile("" : "+v"(ty) :: "memory");
        const int ln = ty & 63, e3 = ln >> 3;
#define LEVEL(lvl) do { if (ty == 256) __hip_atomic_store(rdy + pm * 32 + j, (unsigned)(lvl), RLX_AGENT); } while (0)
        const __amdgpu_buffer_rsrc_t sC = SRD(late ? KARG(const u16*, 10) : KARG(const u16*, 9), DM * NP * 2), sW = SRD(KARG(const u16*, 11), FF * DM * 2);
        const unsigned voffA = (unsigned)(ln * 16);
        const unsigned voffC = (unsigned)(e3 * 2048 + (((ln & 7) ^ (e3 & 6)) * 16));
        const unsigned voffD = (unsigned)(((e3 >> 2) * 8 + (e3 & 3)) * 2048 + ((((ln >> 1) & 3) ^ (e3 >> 1)) * 32) + (ln & 1) * 16);
        const unsigned soA = (unsigned)((late ? 0 : 48) * A2S + mb2 * 16384 + wr * 4096);
        const unsigned soC = (unsigned)((n0 + 32 * wr) * 2048);
        const unsigned soD = (unsigned)(((late ? 0 : 48) + 112 * wr) * 2048 + n0 * 2);
        const unsigned mB = ldsb + wr * 4096;
#define DMA_A(i, k, sl) dma16c(mB + (sl) + (i) * 1024, voffA, sA2, soA + (k) * A2S + (i) * 1024)
#define DMA_C(srd, i, p, sl) dma16(mB + (sl) + 16384 + (i) * 1024, voffC, srd, soC + (p) * 128 + (i) * (8 * 2048))
#define DMA_D(i, row, sl) dma16(mB + (sl) + 16384 + (i) * 1024, voffD, sW, soD + (row) * 2048 + ((i) >> 1) * (4 * 2048) + ((i) & 1) * 128)
#define POLL(v, p) asm volatile("global_load_dword %0, %1, off sc1\n\ts_waitcnt vmcnt(0)" : "=v"(v) : "v"(p) : "memory")
#define ASK(v, p) asm volatile("global_load_dword %0, %1, off sc1" : "=v"(v) : "v"(p) : "memory")
#define NEED(v, p, lvl) do { asm volatile("" : "+v"(v) :: "memory"); if (!__all((int)(v) >= (lvl))) { unsigned spins_ = 0, w_; \
            do { __builtin_amdgcn_s_sleep(1); POLL(w_, p); } while (!__all((int)w_ >= (lvl)) && ++spins_ < (1u << 18)); } } while (0)
        gu32* rl = rdy + (mb2 >> 1) * 32 + (ln & 31);
#define XP(k_) (((((k_) & 3) << 1) | (((k_) >> 2) & 1)) ^ (xc & 1))
#pragma unroll
        for (int p = 0; p < 3; ++p)
#pragma unroll
            for (int i = 0; i < 4; ++i) DMA_D(i, 448 * XP(p), p * 32768);
        unsigned fv;
        {
            const int other = ((ln >> 2) ^ xc) & 1;
            unsigned spins = 0;
            do POLL(fv, rl); while (!__all((int)fv >= 1 || (GATE2 && other)) && ++spins < (1u << 18));
        }
        FSTAMP(31);
#pragma unroll
        for (int p = 0; p < 3; ++p)
#pragma unroll
            for (int i = 0; i < 4; ++i) DMA_A(i, XP(p), p * 32768);
        WAIT_V(8); FSTAMP(32); BAR(); FSTAMP(33); LT(3);
        int k = 3, sl = 3 * 32768;
#define STAGE_W() do { const int q_ = k < 16 ? (k & 3) | ((k & 4) << 1) | ((k & 8) >> 1) : k, x_ = XP(q_), row_ = 448 * x_ + ((q_ >> 3) == 3 ? 96 : 16 * (q_ >> 3)); \
            _Pragma("unroll") for (int i = 0; i < 4; ++i) { DMA_A(i, (q_ & ~7) + x_, sl); DMA_D(i, row_, sl); } ++k; sl = sl == 4 * 32768 ? 0 : sl + 32768; } while (0)
#define LSYNC() do { WAIT_V(16); BAR(); } while (0)
        STAGE_W(); WAIT_V(12); BAR();
        STAGE_W(); LSYNC(); STAGE_W(); LSYNC();
        ASK(fv, rl);
        STAGE_W(); LSYNC(); STAGE_W(); LSYNC();
        NEED(fv, rl, 1);
        STAGE_W(); LSYNC(); STAGE_W(); LSYNC();
        LEVEL(2);
#pragma nounroll
        while (k < 13) { STAGE_W(); LSYNC(); }
        ASK(fv, rl);
        STAGE_W(); LSYNC(); STAGE_W(); LSYNC(); STAGE_W(); LSYNC();
        NEED(fv, rl, 2);
        const int nw = late ? 32 : 24;
#pragma nounroll
        while (k < nw) { STAGE_W(); LSYNC(); }
        const int np = late ? 16 : 16 - SHIFT;
#pragma nounroll
        for (int p = 0; p < np; ++p) {
#pragma unroll
            for (int i = 0; i < 4; ++i) { DMA_A(i, k, sl); DMA_C(sC, i, p, sl); }
            ++k; sl = sl == 4 * 32768 ? 0 : sl + 32768;
            LSYNC();
        }
        if (SHIFT && late) {
            const __amdgpu_buffer_rsrc_t sR = SRD(KARG(const u16*, 9), DM * NP * 2);
#pragma nounroll
            for (int p = 16 - SHIFT; p < 16; ++p) {
#pragma unroll
                for (int i = 0; i < 4; ++i) { DMA_A(i, 72 + p, sl); DMA_C(sR, i, p, sl); }
                sl = sl == 4 * 32768 ? 0 : sl + 32768;
                LSYNC();
            }
        }
        const int tb = mb2 * 128 + (wr >> 1) * 64 + (wr & 1) * 8 + (ln >> 5) * 16, dcol = n0 + (ln & 31) * 4;
#define ROW(i) (tb + ((i) >> 3) * 32 + ((i) & 7))
        LT(4);
        FSTAMP(40);
        gu32* flag = (gu32*)(ws2 + WS_FLAG2 + tile * 128 + wr * 32);
        const __amdgpu_buffer_rsrc_t sP = SRD(ws2 + WS_A2 + (size_t)(KSTEPS2 + (tile >> 2)) * A2S + (tile & 3) * 65536, 65536);
        const int wn1 = (ln >> 4) & 1, s0 = rs0 + wn1, s1 = rs0 + 2 + wn1;
        const int inp = wr * 8192 + ((ln >> 2) & 3) * 1024 + (ln & 3) * 256, sw = (ln & 15) ^ ((ln >> 5) * 8);
        LAS char* q0 = lds + (s0 >= 5 ? s0 - 5 : s0) * 32768 + inp;
        LAS char* q1 = lds + (s1 >= 5 ? s1 - 5 : s1) * 32768 + inp;
#define SUMS(i) (*(LAS f32x4*)(q0 + ((i) >> 3) * 4096 + ((sw ^ ((i) & 7)) * 16)) + *(LAS f32x4*)(q1 + ((i) >> 3) * 4096 + ((sw ^ ((i) & 7)) * 16)))
        f32x4 ex[16];
        if (late) {
            const __amdgpu_buffer_rsrc_t sO = SRD(KARG(float*, 14), SEQ * DM * 4);
            WAIT_V(8); BAR();
            FSTAMP(41);
            ASK(fv, flag);
            WAIT_V(0); BAR();
            FSTAMP(42);
            NEED(fv, flag, 1);
            FSTAMP(43);
#pragma unroll
            for (int i = 0; i < 16; ++i) ex[i] = __builtin_bit_cast(f32x4, __builtin_amdgcn_raw_buffer_load_b128(sP, (wr * 16 + i) * 1024 + ln * 16, 0, 16));
            FSTAMP(44);
            BAR(); asm volatile("" ::: "memory");
            FSTAMP(45);
            STAMP(1, 3);
#pragma unroll
            for (int i = 0; i < 16; ++i) __builtin_amdgcn_raw_buffer_store_b128(__builtin_bit_cast(u32x4, ex[i] + SUMS(i)), sO, (ROW(i) * DM + dcol) * 4, 0, OUTPOL);
        } else {
            const __amdgpu_buffer_rsrc_t sXk = SRD(KARG(const u16*, 0), SEQ * DM * 2), sDk = SRD(KARG(const u16*, 12), DM * 2), sGk = SRD(ws2 + WS_G, 8192);
            WAIT_V(8); BAR();
            FSTAMP(41);
            u32x2 dk = __builtin_amdgcn_raw_buffer_load_b64(sDk, dcol * 2, 0, 0), xv[16]; u32x4 gwd[4];
#pragma unroll
            for (int i = 0; i < 4; ++i) gwd[i] = __builtin_amdgcn_raw_buffer_load_b128(sGk, ROW(4 * i) * 4, 0, 16);
#pragma unroll
            for (int i = 0; i < 16; ++i) xv[i] = __builtin_amdgcn_raw_buffer_load_b64(sXk, (tb * DM + dcol) * 2 + (i & 1) * 2048, (((i) >> 3) * 32 + ((i) & 6)) * 2048, 0);
            WAIT_V(21); BAR();
            FSTAMP(42);
            asm volatile("" : "+v"(dk), "+v"(gwd[0]), "+v"(gwd[1]), "+v"(gwd[2]), "+v"(gwd[3]));
#pragma unroll
            for (int i = 0; i < 16; ++i) {
                const float g = bflo(gwd[i >> 2][i & 3]);
                ex[i] = (f32x4){g * rbf(bflo(xv[i][0]) * bflo(dk[0])), g * rbf(bfhi(xv[i][0]) * bfhi(dk[0])),
                                g * rbf(bflo(xv[i][1]) * bflo(dk[1])), g * rbf(bfhi(xv[i][1]) * bfhi(dk[1]))};
                asm volatile("" : "+v"(ex[i]));
            }
            FSTAMP(44);
            BAR(); asm volatile("" ::: "memory");
            FSTAMP(45);
            STAMP(1, 3);
#pragma unroll
            for (int i = 0; i < 16; ++i) __builtin_amdgcn_raw_buffer_store_b128(__builtin_bit_cast(u32x4, ex[i] + SUMS(i)), sP, (wr * 16 + i) * 1024 + ln * 16, 0, 16);
            FSTAMP(46);
            WAIT_V(0);
            if (ln == 0) __hip_atomic_store(flag, 1u, RLX_AGENT);
        }
        STAMP(1, 6);
        STAMP(1, 7);
        FSTAMP(47);
        FSTAMP(48);
        FDUMP();
        asm volatile(".Lk1_end_a:");
        return;
    }

    {
        unsigned long long gq[2][7];
#pragma unroll
        for (int c = 0; c < 2; ++c)
#pragma unroll
            for (int q = 0; q < 7; ++q) gq[c][q] = __hip_atomic_load(carry + (q + pm >= 7 ? q + pm - 7 : 0) * 1024 + CHAN(c, er), RLX_AGENT);
        f32x2 h[2];
        {
            f32x2 a16[2], run[2]; f32x4 E[2][6], F0[2]; f32x2 F1[2];
#pragma unroll
            for (int c = 0; c < 2; ++c) {
                a16[c] = *(LAS f32x2*)(ctab + c * (16 * 96) + 24);
#pragma unroll
                for (int i = 0; i < 6; ++i) E[c][i] = *(LAS f32x4*)(etab + c * (16 * 144) + i * 16);
                F0[c] = *(LAS f32x4*)(etab + c * (16 * 144) + wr * 32); F1[c] = *(LAS f32x2*)(etab + c * (16 * 144) + wr * 32 + 16);
                run[c] = (f32x2){0.f, 0.f};
            }
#pragma unroll
            for (int w = 0; w < 3; ++w)
                if (wr > w) {
#pragma unroll
                    for (int i = 0; i < 4; ++i)
#pragma unroll
                        for (int c = 0; c < 2; ++c) run[c] = cfma(a16[c], run[c], (f32x2){E[c][2 * w + (i >> 1)][(i & 1) * 2], E[c][2 * w + (i >> 1)][(i & 1) * 2 + 1]});
                }
#pragma unroll
            for (int c = 0; c < 2; ++c) {
                const f32x2 r1 = cfma(a16[c], run[c], (f32x2){F0[c][0], F0[c][1]}), r2 = cfma(a16[c], r1, (f32x2){F0[c][2], F0[c][3]}), r3 = cfma(a16[c], r2, F1[c]);
                h[c] = sel4(eq, run[c], r1, r2, r3);
            }
        }
        f32x4 ur[4], ui[4];
#pragma unroll
        for (int m = 0; m < 4; ++m) { ur[m] = park[m * 128]; ui[m] = park[m * 128 + 64]; }
        const f32x4 cs1 = *(LAS f32x4*)(ctab + 16 * 96);
        FSTAMP(30);
        f32x2 a256[2], aq[2];
#pragma unroll
        for (int c = 0; c < 2; ++c) {
            LAS char* ct = ctab + c * (16 * 96);
            a256[c] = *(LAS f32x2*)(ct + 80); aq[c] = cmul(*(LAS f32x2*)(ct + 16 + eq * 8), *(LAS f32x2*)(ct + 48 + wr * 8));
        }
        unsigned mx = 0;
#pragma unroll
        for (int q = 0; q < 7; ++q) mx = max(mx, max((unsigned)gq[0][q], (unsigned)gq[1][q]));
        if (pm > 0 && __any(mx == 0xFFFFFFFFu)) {
            unsigned spins = 0;
            do {
                mx = 0;
#pragma unroll
                for (int c = 0; c < 2; ++c)
#pragma unroll
                    for (int q = 0; q < 7; ++q) {
                        gq[c][q] = __hip_atomic_load(carry + (q + pm >= 7 ? q + pm - 7 : 0) * 1024 + CHAN(c, er), RLX_AGENT);
                        mx = max(mx, (unsigned)gq[c][q]);
                    }
            } while (__any(mx == 0xFFFFFFFFu) && ++spins < (1u << 18));
        }
        f32x2 cg[2] = {{0.f, 0.f}, {0.f, 0.f}};
#pragma unroll
        for (int q = 0; q < 7; ++q) {
            const unsigned keep = q + pm >= 7 ? 0xFFFFFFFFu : 0u;
#pragma unroll
            for (int c = 0; c < 2; ++c) cg[c] = cfma(a256[c], cg[c], (f32x2){__uint_as_float((unsigned)gq[c][q] & keep), __uint_as_float((unsigned)(gq[c][q] >> 32) & keep)});
        }
        h[0] = cfma(aq[0], cg[0], h[0]); h[1] = cfma(aq[1], cg[1], h[1]);
        STAMP(0, 5);
        FSTAMP(31);
        const unsigned ls = IMG_S(er, eq);
        const f32x2 b1 = {cs1[0], cs1[1]}, bf = {cs1[2], cs1[3]};
#pragma unroll
        for (int hh = 0; hh < 2; ++hh) {
            float vr[8], vi[8], wr8[8], wi8[8];
#pragma unroll
            for (int e = 0; e < 8; ++e) {
                const int m = 2 * hh + (e >> 2), q = e & 3;
                h[0] = cfma(a1, h[0], (f32x2){acc[m][7][q], acc[m][8][q]});
                h[1] = cfma(b1, h[1], (f32x2){ur[m][q], ui[m][q]});
                const float g2 = bflo(gt[m][q]);
                const f32x2 s0 = cmul(cf, h[0]), s1 = cmul(bf, h[1]);
                vr[e] = s0.x * g2; vi[e] = -s0.y * g2; wr8[e] = s1.x * g2; wi8[e] = -s1.y * g2;
            }
            ST16(ls + 40 * A2S + hh * 512, PK8(vr)); ST16(ls + 40 * A2S + hh * 512 + 128, PK8(wr8));
            ST16(ls + hh * 512, PK8(vi)); ST16(ls + hh * 512 + 128, PK8(wi8));
        }
    }
    STAMP(0, 6);
    int ty = threadIdx.x; asm volatile("" : "+v"(ty) :: "memory");
    const int ln = ty & 63, fr2 = ty & 15, fq2 = (ty >> 4) & 3;
    const int wn = wid & 1, kk = wid >> 1;
    LAS char* rH = lds + kk * 8192 + (ln >> 5) * 4096 + (ln & 31) * 8;
    LAS char* rC = lds + 16384 + (wn * 64 + fr2) * 128 + (((4 * kk + fq2) ^ (fr2 & 6)) * 16);
    LAS char* rWb = lds + 16384 + kk * 8192 + (ln >> 5) * 4096 + wn * 1024 + ((ln >> 2) & 7) * 128 + (ln & 3) * 8;
    const int rsw = (ln >> 3) & 3;
    f32x4 ac2[4][8];
#pragma unroll
    for (int n = 0; n < 4; ++n)
#pragma unroll
        for (int m = 0; m < 8; ++m) { ac2[n][m] = (f32x4){0.f, 0.f, 0.f, 0.f}; asm volatile("" : "+v"(ac2[n][m])); }
    STAMP(0, 7);
    FSTAMP(32);
    FDUMP();
    s16x4 HX[8][2], HY[8][2], WX[4][2], WY[4][2];
    int sl = 0;

#define RD1(RD, i, H, W) do { \
        if ((i) < 8) { \
            if (RD == 1) W[(i) >> 1][(i) & 1] = tr4(rWb + sl + ((((i) >> 1) ^ rsw) * 32) + ((i) & 1) * 2048); \
            else if (((i) & 1) == 0) { const s16x8 v_ = *(LAS s16x8*)(rC + sl + ((i) >> 1) * 2048); \
                W[(i) >> 1][0] = __builtin_shufflevector(v_, v_, 0, 1, 2, 3); W[(i) >> 1][1] = __builtin_shufflevector(v_, v_, 4, 5, 6, 7); } \
        } else H[((i) - 8) >> 1][(i) & 1] = tr4(rH + sl + (((i) - 8) >> 1) * 256 + ((i) & 1) * 2048); \
    } while (0)
#define LGKM0() __builtin_amdgcn_s_waitcnt(0xC07F)
#define MM(i, HC, WC) mfma_acc(ac2[(i) & 3][(i) >> 2], cat8(WC[(i) & 3][0], WC[(i) & 3][1]), cat8(HC[(i) >> 2][0], HC[(i) >> 2][1]))
#define STEP(RD, HC, WC, HN, WN) do { \
        _Pragma("unroll") for (int i = 0; i < 32; ++i) { MM(i, HC, WC); if (i < 24) RD1(RD, i, HN, WN); } \
        sl = sl == 4 * 32768 ? 0 : sl + 32768; LGKM0(); \
    } while (0)

    LGKM0();
    BAR();
    STAMP_TAKE(st9);
#pragma unroll
    for (int i = 0; i < 24; ++i) RD1(1, i, HX, WX);
    sl = 32768; LGKM0();
    const int nf = late ? 15 : 11;
#pragma nounroll
    for (int i = 0; i < nf; ++i) {
        if (i == 3) WAIT_V(0);
        BAR(); STEP(1, HX, WX, HY, WY); BAR(); STEP(1, HY, WY, HX, WX);
    }
    BAR(); STEP(1, HX, WX, HY, WY); BAR(); STEP(0, HY, WY, HX, WX);
#pragma nounroll
    for (int i = late ? -SHIFT / 2 : SHIFT / 2; i < 7; ++i) { BAR(); STEP(0, HX, WX, HY, WY); BAR(); STEP(0, HY, WY, HX, WX); }
    BAR(); STEP(0, HX, WX, HY, WY);
    {
        const int sw = rs0 + wid;
        LAS char* dmp = lds + (sw >= 5 ? sw - 5 : sw) * 32768 + fq2 * 256;
#define DUMP(i) *(LAS f32x4*)(dmp + (i) * 1024 + ((fr2 ^ (4 * ((i) & 3) + fq2)) * 16)) = ac2[(i) & 3][(i) >> 2]
#pragma unroll
        for (int i = 0; i < 32; ++i) { MM(i, HY, WY); if (i >= 12) DUMP(i - 12); }
        asm volatile("s_nop 15" ::: "memory");
#pragma unroll
        for (int i = 20; i < 32; ++i) DUMP(i);
    }
    WAIT_L0();
    STAMP_PUT(1, 0, st0); STAMP_PUT(1, 1, st9);
    STAMP(1, 2);
    BAR();
    asm volatile(".Lk1_end_b:");
}

extern "C" void kernel_launch(void* const* d_in, const int* in_sizes, int n_in, void* d_out, int out_size, void* d_ws, size_t ws_size,
                              hipStream_t stream) {
    const u16* x = (const u16*)d_in[0];
    const u16* lre = (const u16*)d_in[1];
    const u16* lim = (const u16*)d_in[2];
    const u16* Bre = (const u16*)d_in[3];
    const u16* Bim = (const u16*)d_in[4];
    const u16* Cre = (const u16*)d_in[5];
    const u16* Cim = (const u16*)d_in[6];
    const u16* logdt = (const u16*)d_in[7];
    const u16* dskip = (const u16*)d_in[8];
    const u16* Wg = (const u16*)d_in[10];
    const u16* Wu = (const u16*)d_in[11];
    const u16* Wd = (const u16*)d_in[12];
    const u16* gw = (const u16*)d_in[13];
    char* ws = (char*)d_ws;
    (void)hipFuncSetAttribute((const void*)k1_fused, hipFuncAttributeMaxDynamicSharedMemorySize, K2_LDS);
    k1_fused<<<256, 512, K2_LDS, stream>>>(x, Wg, Wu, Bre, Bim, lre, lim, logdt, gw, Cre, Cim, Wd, dskip, ws, (float*)d_out);
}
```

```cpp
#include <hip/hip_runtime.h>
#include <hip/hip_bf16.h>

typedef unsigned short u16;
typedef __bf16 bf16x8 __attribute__((ext_vector_type(8)));
typedef __bf16 bf16x2 __attribute__((ext_vector_type(2)));
typedef short s16x4 __attribute__((ext_vector_type(4)));
typedef short s16x8 __attribute__((ext_vector_type(8)));
typedef float f32x4 __attribute__((ext_vector_type(4)));
typedef float f32x2 __attribute__((ext_vector_type(2)));
typedef unsigned u32x4 __attribute__((ext_vector_type(4)));
typedef unsigned u32x2 __attribute__((ext_vector_type(2)));
#define LAS __attribute__((address_space(3)))
#define GAS __attribute__((address_space(1)))
typedef GAS unsigned gu32;
typedef GAS unsigned long long gu64;
typedef __attribute__((address_space(4))) const unsigned long long* kptr;
#define RLX_AGENT __ATOMIC_RELAXED, __HIP_MEMORY_SCOPE_AGENT

constexpr int SEQ = 2048, DM = 1024, NP = 1024, FF = 3584;
constexpr int KSTEPS2 = (2 * NP + FF) / 64;

constexpr unsigned WS_CARRY = 0;
constexpr unsigned WS_G = 7 * 8192;
constexpr unsigned WS_RDY = 65536;
constexpr unsigned WS_FLAG2 = WS_RDY + 1024;
constexpr unsigned WS_HQ = WS_FLAG2 + 16384;
constexpr unsigned WS_HA = WS_HQ + 1024;
constexpr unsigned WS_HR = WS_HA + 1024;
constexpr unsigned WS_SYNC_BYTES = WS_HR + 2048;
constexpr size_t WS_A2 = 1 << 20;
constexpr unsigned A2S = 262144 + 128;
constexpr size_t WS_A2_BYTES = (size_t)(KSTEPS2 + 32) * A2S;

__device__ __forceinline__ float bflo(unsigned v) { return __uint_as_float(v << 16); }
__device__ __forceinline__ float bfhi(unsigned v) { return __uint_as_float(v & 0xffff0000u); }
__device__ __forceinline__ float rbf(float f) { return (float)(__bf16)f; }
__device__ __forceinline__ unsigned pk2(float a, float b) {
    bf16x2 v; v.x = (__bf16)a; v.y = (__bf16)b; return __builtin_bit_cast(unsigned, v);
}
__device__ __forceinline__ float relu(float v) { return __int_as_float(max(__float_as_int(v), 0)); }
__device__ __forceinline__ float dot2(unsigned a, unsigned b, float c) {
    return __builtin_amdgcn_fdot2_f32_bf16(__builtin_bit_cast(bf16x2, a), __builtin_bit_cast(bf16x2, b), c, false);
}

constexpr size_t WS_DBG = 200u << 20;
#define STAMP(kid, i)
#define STAMP_TAKE(v)
#define STAMP_PUT(kid, i, v)

#define FSTAMP(i)
#define FDUMP()
#define WAIT_V(n) asm volatile("s_waitcnt vmcnt(" #n ")" ::: "memory")
#define WAIT_L0() asm volatile("s_waitcnt lgkmcnt(0)" ::: "memory")
#define BAR() __builtin_amdgcn_s_barrier()
#define SCHED() __builtin_amdgcn_sched_barrier(0)
#define SRD(p, bytes) __builtin_amdgcn_make_buffer_rsrc((void*)(p), (short)0, (int)(bytes), 0x00020000)

__device__ __forceinline__ void dma16(unsigned m0v, unsigned voff, __amdgpu_buffer_rsrc_t srd, unsigned soff) {
    asm volatile("s_mov_b32 m0, %0\n\ts_nop 0\n\tbuffer_load_dwordx4 %1, %2, %3 offen lds" ::"s"(m0v), "v"(voff), "s"(srd), "s"(soff) : "m0", "memory");
}
__device__ __forceinline__ bf16x8 lds_b128(LAS char* p) { return __builtin_bit_cast(bf16x8, *(LAS s16x8*)p); }
__device__ __forceinline__ bf16x8 lds_tr(LAS char* p0, LAS char* p1) {
    s16x4 a = __builtin_amdgcn_ds_read_tr16_b64_v4i16((LAS s16x4*)p0);
    s16x4 b = __builtin_amdgcn_ds_read_tr16_b64_v4i16((LAS s16x4*)p1);
    return __builtin_bit_cast(bf16x8, __builtin_shufflevector(a, b, 0, 1, 2, 3, 4, 5, 6, 7));
}
#define MFMA(a, b, c) __builtin_amdgcn_mfma_f32_16x16x32_bf16(a, b, c, 0, 0, 0)
__device__ __forceinline__ f32x2 cmul(f32x2 a, f32x2 b) { return (f32x2){__builtin_fmaf(a.x, b.x, -(a.y * b.y)), __builtin_fmaf(a.x, b.y, a.y * b.x)}; }
__device__ __forceinline__ f32x2 cfma(f32x2 a, f32x2 b, f32x2 c) {
    return (f32x2){__builtin_fmaf(a.x, b.x, __builtin_fmaf(-a.y, b.y, c.x)), __builtin_fmaf(a.x, b.y, __builtin_fmaf(a.y, b.x, c.y))};
}
__device__ __forceinline__ f32x2 sel4(int i, f32x2 a, f32x2 b, f32x2 c, f32x2 d) {
    const bool o = i & 1, u = i & 2;
    const float lx = o ? b.x : a.x, ly = o ? b.y : a.y, hx = o ? d.x : c.x, hy = o ? d.y : c.y;
    return (f32x2){u ? hx : lx, u ? hy : ly};
}

#define STPOL 16
#define PF_W0 1
#define PF_D0 4
#define OUTPOL 16

#define K2_SPLIT 40
#define MAP 0
#define ABL_NOLOAD 0
#define ABL_NOREAD 0
#define ABL_NOMFMA 0
#define CODE_T 10
#define ILV 1
#define SHIFT 2
#define GATE2 1
#define PRO 0
#define MORD 3
#define DEEP 0
#define THIN 1
#define M04 1
#define LATE_FLIP 0
#define DIST 5
constexpr int K2_LDS = 5 * 32768;

__device__ __forceinline__ void dma16c(unsigned m0v, unsigned voff, __amdgpu_buffer_rsrc_t srd, unsigned soff) {
    asm volatile("s_mov_b32 m0, %0\n\ts_nop 0\n\tbuffer_load_dwordx4 %1, %2, %3 offen sc1 lds" ::"s"(m0v), "v"(voff), "s"(srd), "s"(soff) : "m0", "memory");
}
#define BPOL ""
#define APOL " sc1"
__device__ __forceinline__ void dma4c(unsigned m0v, unsigned voff, __amdgpu_buffer_rsrc_t srd, unsigned soff) {
    asm volatile("s_mov_b32 m0, %0\n\ts_nop 0\n\tbuffer_load_dwordx4 %1, %2, %3 offen" APOL " lds\n\tbuffer_load_dwordx4 %1, %2, %3 offen offset:1024" APOL " lds\n\t"
                 "buffer_load_dwordx4 %1, %2, %3 offen offset:2048" APOL " lds\n\tbuffer_load_dwordx4 %1, %2, %3 offen offset:3072" APOL " lds" ::"s"(m0v), "v"(voff), "s"(srd), "s"(soff) : "m0", "memory");
}
__device__ __forceinline__ void dma4(unsigned m0v, unsigned voff, __amdgpu_buffer_rsrc_t srd, unsigned s0, unsigned s1, unsigned s2, unsigned s3) {
    asm volatile("s_mov_b32 m0, %0\n\ts_nop 0\n\tbuffer_load_dwordx4 %1, %2, %3 offen lds\n\tbuffer_load_dwordx4 %1, %2, %4 offen offset:1024 lds\n\t"
                 "buffer_load_dwordx4 %1, %2, %5 offen offset:2048 lds\n\tbuffer_load_dwordx4 %1, %2, %6 offen offset:3072 lds" ::"s"(m0v), "v"(voff), "s"(srd), "s"(s0), "s"(s1), "s"(s2), "s"(s3) : "m0", "memory");
}
__device__ __forceinline__ void mfma_acc(f32x4& c, bf16x8 a, bf16x8 b) {
    asm volatile("v_mfma_f32_16x16x32_bf16 %0, %1, %2, %0" : "+v"(c) : "v"(a), "v"(b) : "memory");
}
__device__ __forceinline__ s16x4 tr4(LAS char* p) { return __builtin_amdgcn_ds_read_tr16_b64_v4i16((LAS s16x4*)p); }
__device__ __forceinline__ bf16x8 cat8(s16x4 a, s16x4 b) { return __builtin_bit_cast(bf16x8, __builtin_shufflevector(a, b, 0, 1, 2, 3, 4, 5, 6, 7)); }

constexpr int K1_ASB = 40960;
constexpr int K1_W = 81920;
constexpr int K1_WBUF = 28704, K1_UOFF = 14352;
constexpr int K1_ETAB = K1_W + 2 * K1_WBUF;
constexpr int K1_CTAB = K1_ETAB + 32 * 144;
constexpr int K1_SINK = K1_CTAB + 32 * 96;
constexpr int K1_GTAB = K1_SINK + 1024;
constexpr int K1_HS = K1_GTAB + 1024;

__global__ __launch_bounds__(512, 2) void k1_fused(const u16* __restrict__ x, const u16* __restrict__ Wg, const u16* __restrict__ Wu,
                                                       const u16* __restrict__ Bre, const u16* __restrict__ Bim, const u16* __restrict__ lre,
                                                       const u16* __restrict__ lim, const u16* __restrict__ logdt, const u16* __restrict__ gw,
                                                       const u16* __restrict__ Cre, const u16* __restrict__ Cim, const u16* __restrict__ Wd,
                                                       const u16* __restrict__ dskip, char* __restrict__ ws, float* __restrict__ out) {
    extern __shared__ __attribute__((aligned(16))) char smem[];
    LAS char* lds = (LAS char*)smem;
    const unsigned ldsb = (unsigned)(size_t)lds;
    const int tid = threadIdx.x, lane = tid & 63;
    const int wid = __builtin_amdgcn_readfirstlane(tid >> 6);
    const int wr = wid & 3, wc = wid >> 2;
    const int bid = blockIdx.x, idx = bid >> 3, j = (bid & 7) * 4 + (idx & 3), pm = idx >> 2;
    const int fr = lane & 15, fq = lane >> 4;
#define ROLE(b_, i_) const int xc = (b_) & 7, late = (xc & 1) ^ LATE_FLIP, mb2 = (xc >> 2) * 8 + ((i_) >> 2), nblk = ((xc >> 1) & 1) * 4 + ((i_) & 3), tile = mb2 * 8 + nblk
    STAMP_TAKE(st0);
#define LT(n)
    FSTAMP(0);

    const char* code0; unsigned codeA, codeB;
    asm volatile("s_getpc_b64 %0\n.Lk1_ref:\n\ts_mov_b32 %1, .Lk1_end_a-.Lk1_ref\n\ts_mov_b32 %2, .Lk1_end_b-.Lk1_ref" : "=s"(code0), "=s"(codeA), "=s"(codeB));
    const unsigned codeN = (codeA > codeB ? codeA : codeB) + 192;
    if (wid == 7) {
        const __amdgpu_buffer_rsrc_t sI = SRD(code0, codeN);
        dma16(ldsb + K1_SINK, (unsigned)(lane * 4096) | (unsigned)((3 - lane) & 0x80000000), sI, (unsigned)(idx * 128));
    }

    const int ch = 32 * j + 8 * (fq & 1) + 4 * (fr >> 3) + 16 * ((fr >> 2) & 1) + (fr & 3);
    const int grow = pm * 256 + j * 8 + wid;
    u32x4 xa, xb, ga, gb; unsigned plr, pli, pld;
    {
        const u16* xr = x + (size_t)grow * DM;
        const unsigned l16 = lane * 16, c2 = ch * 2;
        asm volatile("s_nop 4\n\tglobal_load_dwordx4 %0, %7, %9\n\tglobal_load_dwordx4 %1, %7, %9 offset:1024\n\t"
                     "global_load_dwordx4 %2, %7, %10\n\tglobal_load_dwordx4 %3, %7, %10 offset:1024\n\t"
                     "global_load_ushort %4, %8, %11\n\tglobal_load_ushort %5, %8, %12\n\tglobal_load_ushort %6, %8, %13"
                     : "=&v"(xa), "=&v"(xb), "=&v"(ga), "=&v"(gb), "=&v"(plr), "=&v"(pli), "=&v"(pld)
                     : "v"(l16), "v"(c2), "s"(xr), "s"(gw), "s"(lre), "s"(lim), "s"(logdt) : "memory");
    }

    const bool pf = wid == 7;
    const int q4 = wid + 28, q5 = wid + 35;
    const __amdgpu_buffer_rsrc_t sX = SRD(!pf || !(bid & 32) ? x : (bid & 16) ? Bim : Bre, !pf || !(bid & 32) ? SEQ * DM * 2 : NP * DM * 2);
    const int pb = (bid >> 3) | ((bid & 7) << 5);
    const __amdgpu_buffer_rsrc_t sG = SRD(!pf ? Wg : (pb & 1) ? Wd : (pb & 2) ? Cim : Cre, (pf && !(pb & 1)) ? DM * NP * 2 : DM * FF * 2);
    const __amdgpu_buffer_rsrc_t sU = SRD((pf && (pb & 1)) ? Wg : Wu, DM * FF * 2);
    const __amdgpu_buffer_rsrc_t s4 = SRD(pf ? (const u16*)ws : q4 < 32 ? x : (q4 & 2) ? Bim : Bre, pf ? WS_SYNC_BYTES : q4 < 32 ? SEQ * DM * 2 : NP * DM * 2);
    const __amdgpu_buffer_rsrc_t s5 = SRD(pf ? (const u16*)code0 : (q5 & 2) ? Bim : Bre, pf ? codeN : q5 < 40 ? NP * DM * 2 : 0);

    const int l3 = lane >> 3;
    const unsigned voffX = pf ? (unsigned)(lane * 2048) : (unsigned)(((l3 >> 2) * 16 + (l3 & 3)) * 2048 + (((lane & 7) ^ (l3 & 6)) * 16));
    unsigned voffW;
    { const int gam = wid * 64 + lane, rho = gam / 14, c = gam - rho * 14, k = (rho & 0x13) | ((rho & 4) << 1) | ((rho & 8) >> 1);
      voffW = pf ? (unsigned)(lane * 128) : (unsigned)(k * (FF * 2) + c * 16); }
#define XROW(q) (pm * 256 + ((q) >> 3) * 64 + (((q) >> 2) & 1) * 8 + ((q) & 1) * 32 + (((q) >> 1) & 1) * 4)
#define BROW(q) (32 * j + 8 * (((q) >> 2) & 1) + 4 * ((q) & 1))
    constexpr unsigned WST = 64 * FF * 2, WSH = 32 * FF * 2, FAR = 0x40000000u;
    const unsigned so0 = pf ? 0u : (unsigned)(XROW(wid) * 2048);
    unsigned px = (unsigned)((bid & ((bid & 32) ? 15 : 31)) * 131072 + (bid >> 6) * 128), pxs = 512;
    px = 0x80000000u; pxs = 0;
    const unsigned so1 = pf ? FAR : (unsigned)(XROW(wid + 7) * 2048), so2 = pf ? FAR : (unsigned)(XROW(wid + 14) * 2048), so3 = pf ? FAR : (unsigned)(XROW(wid + 21) * 2048),
                   so4 = pf ? FAR : (unsigned)((q4 < 32 ? XROW(q4) : BROW(q4)) * 2048), so5 = pf ? FAR : (unsigned)(BROW(q5) * 2048);
    const unsigned soW = (unsigned)(j * 224), soW2 = pf ? FAR : soW + WSH;
    const unsigned mSink = ldsb + K1_SINK, mb0 = pf ? mSink : ldsb + wid * 1024, mk = pf ? 0u : ~0u;
#define MZ(off) (mb0 + ((unsigned)(off) & mk))
    const unsigned m50 = q5 < 40 ? MZ(35 * 1024) : mSink, m51 = q5 < 40 ? MZ(35 * 1024 + K1_ASB) : mSink;
    const unsigned ust = pf ? (1u << 20) : WST, gst = !pf ? WST : (pb & 1) ? (1u << 20) : (1u << 19);
    const unsigned su0 = pf ? (unsigned)((pb >> 1) * 8192) + (unsigned)(PF_W0) * ust : soW;
    const unsigned sg0 = pf ? (unsigned)(((pb & 1) ? pb >> 1 : pb >> 2) * 8192) - (unsigned)(PF_D0 + 2) * gst : soW;
#define ST_X(i, ko, b) dma16(MZ((b) * K1_ASB + (i) * 7 * 1024), voffX, sX, ((i) == 0 ? so0 : (i) == 1 ? so1 : (i) == 2 ? so2 : so3) + (ko))
#define ST_S4(ko, b) dma16(MZ((b) * K1_ASB + 28 * 1024), voffX, s4, so4 + (ko))
#define ST_S5(ko, b) dma16((b) ? m51 : m50, voffX, s5, so5 + (ko))
#define ST_G1(t, b) dma16(MZ(K1_W + (b) * K1_WBUF), voffW, sG, sg0 + (t) * gst)
#define ST_G2(t, b) dma16(MZ(K1_W + (b) * K1_WBUF + 7168), voffW, sG, soW2 + (t) * WST)
#define ST_U1(t, b) dma16(MZ(K1_W + (b) * K1_WBUF + K1_UOFF), voffW, sU, su0 + (t) * ust)
#define ST_U2(t, b) dma16(MZ(K1_W + (b) * K1_WBUF + K1_UOFF + 7168), voffW, sU, soW2 + (t) * WST)

    ST_X(0, pf ? px : 0u, 0);
    if (!pf) { ST_X(1, 0, 0); ST_X(2, 0, 0); ST_X(3, 0, 0); ST_S4(0, 0); ST_S5(0, 0); ST_G1(0, 0); ST_G2(0, 0); ST_U2(0, 0); }
    ST_U1(0, 0);
    FSTAMP(1);

    const int ch0 = (fq ^ (fr & 6)) * 16;
    LAS char* rA0 = lds + wr * 8192 + fr * 128 + ch0;
    LAS char* rA1 = lds + wr * 8192 + fr * 128 + (ch0 ^ 64);
    LAS char* rS0 = lds + 32768 + wc * 4096 + fr * 128 + ch0;
    LAS char* rS1 = lds + 32768 + wc * 4096 + fr * 128 + (ch0 ^ 64);
    const int trow = (16 * (fq >> 1) + 4 * (fq & 1) + ((lane & 15) >> 2)) * 224;
    LAS char* rW = lds + K1_W + trow + wc * 96 + (lane & 3) * 8;
    LAS char* rM = lds + K1_W + trow + 192 + wc * 16 + (lane & 1) * 8 + ((lane >> 1) & 1) * K1_UOFF;

    f32x4 acc[4][9];
#pragma unroll
    for (int m = 0; m < 4; ++m)
#pragma unroll
        for (int n = 0; n < 9; ++n) { acc[m][n] = (f32x4){0.f, 0.f, 0.f, 0.f}; asm volatile("" : "+v"(acc[m][n])); }
    bf16x8 Af[4][2], Bf[3][2];

#define RD_A(b) do { _Pragma("unroll") for (int m = 0; m < 4; ++m) { Af[m][0] = lds_b128(rA0 + (b) * K1_ASB + m * 2048); Af[m][1] = lds_b128(rA1 + (b) * K1_ASB + m * 2048); } } while (0)
#define RD_S(slot, ri, b) do { Bf[slot][0] = lds_b128(rS0 + (b) * K1_ASB + (ri) * 2048); Bf[slot][1] = lds_b128(rS1 + (b) * K1_ASB + (ri) * 2048); } while (0)
#define RD_W(slot, base, off) do { Bf[slot][0] = lds_tr((base) + (off), (base) + (off) + 1792); Bf[slot][1] = lds_tr((base) + (off) + 7168, (base) + (off) + 7168 + 1792); } while (0)
#define MMA3(n0, n1, n2) do { __builtin_amdgcn_s_setprio(1); \
        _Pragma("unroll") for (int m = 0; m < 4; ++m) { \
            mfma_acc(acc[m][n0], Af[m][0], Bf[0][0]); mfma_acc(acc[m][n0], Af[m][1], Bf[0][1]); mfma_acc(acc[m][n1], Af[m][0], Bf[1][0]); mfma_acc(acc[m][n1], Af[m][1], Bf[1][1]); \
            mfma_acc(acc[m][n2], Af[m][0], Bf[2][0]); mfma_acc(acc[m][n2], Af[m][1], Bf[2][1]); } \
        __builtin_amdgcn_s_setprio(0); } while (0)
#define RD_AM(m, b) do { Af[m][0] = lds_b128(rA0 + (b) * K1_ASB + (m) * 2048); Af[m][1] = lds_b128(rA1 + (b) * K1_ASB + (m) * 2048); } while (0)
#define SEC1(t, b) do { RD_S(0, 0, b); RD_S(1, 1, b); ST_U1((t) + 1, (b) ^ 1); RD_W(2, rW, (b) * K1_WBUF); RD_AM(0, b); ST_U2((t) + 1, (b) ^ 1); RD_AM(1, b); RD_AM(2, b); RD_AM(3, b); } while (0)
#define SEC2(k0, ko, b) do { ST_X(0, k0, b); RD_W(0, rW, (b) * K1_WBUF + 32); ST_X(1, ko, b); RD_W(1, rW, (b) * K1_WBUF + 64); ST_X(2, ko, b); RD_W(2, rM, (b) * K1_WBUF); ST_X(3, ko, b); } while (0)
#define SEC3(t, ko, b) do { ST_G1((t) + 2, b); RD_W(0, rW, (b) * K1_WBUF + K1_UOFF); ST_G2((t) + 2, b); RD_W(1, rW, (b) * K1_WBUF + K1_UOFF + 32); ST_S4(ko, b); \
        RD_W(2, rW, (b) * K1_WBUF + K1_UOFF + 64); ST_S5(ko, b); } while (0)
#define GO(n0, n1, n2) do { WAIT_L0(); BAR(); SCHED(); MMA3(n0, n1, n2); SCHED(); } while (0)

#define RELANE() int tz = threadIdx.x; asm volatile("" : "+v"(tz)); const int er = tz & 15, eq = (tz >> 4) & 3; \
    const int ech = 32 * j + 8 * wc + 4 * (er >> 3) + 16 * ((er >> 2) & 1) + (er & 3); (void)ech; (void)eq
    asm volatile("" : "+v"(rA0), "+v"(rA1), "+v"(rS0), "+v"(rS1), "+v"(rW), "+v"(rM));
    gu32* gtab = (gu32*)(ws + WS_G);
    WAIT_V(10);
    if (pf) WAIT_V(2);
    asm volatile("" : "+v"(xa), "+v"(xb), "+v"(ga), "+v"(gb), "+v"(plr), "+v"(pli), "+v"(pld) :: "memory");
    FSTAMP(2);
    {
        float s = 0.f, s2 = 0.f;
#pragma unroll
        for (int e = 0; e < 4; ++e) { s = dot2(xa[e], ga[e], s); s2 = dot2(xb[e], gb[e], s2); }
        s += s2;
#define DPP_ADD(ctrl, rmask) s += __int_as_float(__builtin_amdgcn_update_dpp(0, __float_as_int(s), ctrl, rmask, 0xf, true))
        DPP_ADD(0x111, 0xf); DPP_ADD(0x112, 0xf); DPP_ADD(0x114, 0xf); DPP_ADD(0x118, 0xf); DPP_ADD(0x142, 0xa); DPP_ADD(0x143, 0xc);
        const float g = rbf(__builtin_amdgcn_rcpf(1.f + __builtin_amdgcn_exp2f(-1.4426950408889634f * rbf(s))));
        if (lane == 63) __hip_atomic_store(gtab + grow, pk2(g, 1.f - g), RLX_AGENT);
    }
    FSTAMP(3);
    if (!pf) { ST_X(0, 128, 1); ST_X(1, 128, 1); ST_X(2, 128, 1); ST_X(3, 128, 1); ST_S4(128, 1); ST_S5(128, 1); ST_G1(1, 1); ST_G2(1, 1); FSTAMP(4); WAIT_V(11); }
    else {
        dma16c(ldsb + K1_HS + 1024, (unsigned)(lane * 16), s4, WS_HR + (bid >> 7) * 1024);
        dma16c(ldsb + K1_HS, (unsigned)(lane * 16), s4, bid == 0 ? WS_HA : 0x80000000u);
        const float lr = bflo(plr), li = bflo(pli), dt = rbf(expf(bflo(pld)));
        const float e = expf(dt * lr); float sn, cs; sincosf(dt * li, &sn, &cs);
        const f32x2 a1 = {e * cs, e * sn};
        const float nr = a1.x - 1.f, den = lr * lr + li * li;
        f32x2 a16 = cmul(a1, a1); a16 = cmul(a16, a16); a16 = cmul(a16, a16); a16 = cmul(a16, a16);
        const f32x2 a32 = cmul(a16, a16), a48 = cmul(a32, a16), a64 = cmul(a32, a32), a128 = cmul(a64, a64), a192 = cmul(a128, a64), a256 = cmul(a128, a128);
        if (lane < 32) {
            LAS f32x4* ct = (LAS f32x4*)(lds + K1_CTAB + lane * 96);
            ct[0] = (f32x4){a1.x, a1.y, 2.f * ((nr * lr + a1.y * li) / den), 2.f * ((a1.y * lr - nr * li) / den)};
            ct[1] = (f32x4){1.f, 0.f, a16.x, a16.y}; ct[2] = (f32x4){a32.x, a32.y, a48.x, a48.y};
            ct[3] = (f32x4){1.f, 0.f, a64.x, a64.y}; ct[4] = (f32x4){a128.x, a128.y, a192.x, a192.y};
            ct[5] = (f32x4){a256.x, a256.y, 0.f, 0.f};
        }
    }
    FSTAMP(5);
    if (wc == 1) BAR();
    STAMP_TAKE(st1);
    LT(1);
    FSTAMP(6);
#pragma nounroll
    for (int t = 0; t < 16; t += 2) {
        asm volatile("" : "+s"(t));
        const unsigned ko = t == 14 ? 0x80000000u : (unsigned)(t + 2) * 128u;
        const unsigned k0 = pf ? px + pxs : ko, k1 = pf ? px + 2 * pxs : ko + 128;
        px += 2 * pxs;
        BAR(); FSTAMP(8 + t); SEC1(t, 0); if (!pf) WAIT_V(10);
        GO(7, 8, 0);
        BAR(); SEC2(k0, ko, 0);
        GO(1, 2, 6);
        BAR(); SEC3(t, ko, 0); if (!pf) WAIT_V(10);
        GO(3, 4, 5);
        BAR(); FSTAMP(9 + t); SEC1(t + 1, 1); if (!pf) WAIT_V(10);
        GO(7, 8, 0);
        BAR(); SEC2(k1, ko + 128, 1);
        GO(1, 2, 6);
        BAR(); SEC3(t + 1, ko + 128, 1);
        if (!pf) WAIT_V(10);
        else {
            unsigned l128 = voffW; int hb = bid; kptr kh = (kptr)__builtin_amdgcn_kernarg_segment_ptr();
            asm volatile("" : "+v"(l128)); asm volatile("" : "+s"(hb), "+s"(kh));
            const unsigned l16 = l128 >> 3, OUT = 0x80000000u;
            const int hi_ = hb >> 3, hj = (hb & 7) * 4 + (hi_ & 3), hp = hi_ >> 2;
            LAS unsigned* hst = (LAS unsigned*)(lds + K1_HS + 2048);
#define RFL(v) ((unsigned)__builtin_amdgcn_readfirstlane(v))
#define HDMA(m, vo, srd, so) asm volatile("s_nop 4\n\ts_mov_b32 m0, %0\n\ts_nop 0\n\tbuffer_load_dwordx4 %1, %2, %3 offen sc1 lds" ::"s"((unsigned)(m)), "v"(vo), "s"(srd), "s"((unsigned)(so)) : "m0", "memory")
#define HLD(op, v, vo) asm volatile("s_nop 4\n\t" op " %0, %1, %2, 0 offen sc1\n\ts_waitcnt vmcnt(0)" : "=v"(v) : "v"(vo), "s"(s4) : "memory")
#define HST32(off, v) __builtin_amdgcn_raw_buffer_store_b32((unsigned)(v), s4, off, 0, 16)
#define HST64(off, lo_, hi2_) do { __builtin_amdgcn_raw_buffer_store_b64((u32x2){(unsigned)(lo_), (unsigned)(hi2_)}, s4, off, 0, 16); asm volatile("" ::: "memory"); } while (0)
#define REPLY(q_, tag_) do { _Pragma("unroll") for (int i = 0; i < 4; ++i) HST64(WS_HR + l16 * 2 + i * 8, (q_)[i], tag_); } while (0)
#define LANE0(off) (l16 == 0 ? (unsigned)(off) : OUT)
#define AT(v) int t##v = t; asm volatile("" : "+s"(t##v)); if (t##v == v)
            if ((t & 2) == 0) {
                WAIT_V(20);
                AT(0) {
                    const unsigned ask = *(LAS unsigned*)(lds + K1_HS + 1024 + (hb & 127) * 8) + 1u;
                    const u32x4 old = *(LAS u32x4*)(lds + K1_HS + l16);
                    HST32(LANE0(WS_HQ + hb * 4), ask);
                    unsigned tag = RFL(old[0]) + 1u;
                    while (__any(old[0] == tag || old[1] == tag || old[2] == tag || old[3] == tag)) ++tag;
                    hst[0] = ask; hst[1] = tag; hst[2] = 0u;
                }
                AT(4) { if (hb == 0) { const u32x4 q = *(LAS u32x4*)(lds + K1_HS + l16); const unsigned tag = hst[1]; REPLY(q, tag); } }
                AT(8) {
                    const u32x2 r = *(LAS u32x2*)(lds + K1_HS + 1024 + (hb & 127) * 8);
                    const bool hit = r[0] == hst[0];
                    HST32(hit ? LANE0(WS_HA + hb * 4) : OUT, r[1]);
                    hst[hit ? 1 : 3] = r[1]; hst[2] = hit;
                }
                AT(12) {
                    u32x4 ans = *(LAS u32x4*)(lds + K1_HS + l16);
                    unsigned tag = hst[1], done = hst[2];
                    if (!__all(done && ans[0] == tag && ans[1] == tag && ans[2] == tag && ans[3] == tag)) {
                        const unsigned ask = RFL(hst[0]); unsigned spins = 0;
                        tag = RFL(tag); done = RFL(done);
#pragma nounroll
                        do {
                            if (hb == 0) { u32x4 q; HLD("buffer_load_dwordx4", q, WS_HQ + l16); REPLY(q, tag); }
                            u32x2 r; HLD("buffer_load_dwordx2", r, WS_HR + hb * 8);
                            if (!done && RFL(r[0]) == ask) { tag = RFL(r[1]); done = 1u; HST32(LANE0(WS_HA + hb * 4), tag); }
                            HLD("buffer_load_dwordx4", ans, WS_HA + l16);
                        } while (!__all(done && ans[0] == tag && ans[1] == tag && ans[2] == tag && ans[3] == tag) && ++spins < (1u << 18));
                    }
                    HDMA(ldsb + K1_GTAB, l16, s4, WS_G + hp * 1024);
                }
            }
            int tq = t; asm volatile("" : "+s"(tq));
            if (tq & 2) {
                AT(2) {
                    ROLE(hb, hi_);
                    HST64((hp < 7 ? WS_CARRY + hp * 8192 + hj * 256 : OUT) + (l16 < 32 * 16 ? l16 >> 1 : OUT >> 1), ~0u, ~0u);
                    HST32(l16 == 4 * 16 ? WS_RDY + (hp * 32 + hj) * 4 : (late ? OUT : WS_FLAG2 + tile * 128) + (l16 < 4 * 16 ? l16 * 2 : OUT >> 1), ~0u);
                    HDMA(ldsb + K1_HS, l16, s4, hb == 0 ? WS_HQ : OUT);
                    px = OUT; pxs = 0;
                }
                AT(6) {
                    const __amdgpu_buffer_rsrc_t sA = SRD((char*)kh[13] + WS_A2, WS_A2_BYTES), sO = SRD(kh[14], SEQ * DM * 4);
                    const unsigned vo = (l128 >> 7) * A2S, vo2 = l128 << 10;
                    HDMA(ldsb + K1_SINK, vo * 32, sA, (hi_ + 1) * A2S - 128); HDMA(ldsb + K1_SINK, vo2 * 32, sO, hi_ * 131072);
                    HDMA(ldsb + K1_HS + 1024, l16, s4, WS_HR + (hb >> 7) * 1024);
                }
                AT(10) {
                    HDMA(ldsb + K1_SINK, l128 * 32, s5, 8192 + hi_ * 128);
                    HDMA(ldsb + K1_HS, l16, s4, WS_HA);
                }
                AT(14) WAIT_V(20);
            }
        }
        GO(3, 4, 5);
    }
    asm volatile("s_nop 15" ::: "memory");
    if (wc == 0) BAR();
    RELANE();
    STAMP_PUT(0, 0, st0); STAMP_PUT(0, 1, st1);
    STAMP(0, 2);
    LT(2);
    FSTAMP(24);
    kptr ka = (kptr)__builtin_amdgcn_kernarg_segment_ptr(); asm volatile("" : "+s"(ka));
#define KARG(T, i) ((T)ka[i])
    char* const ws2 = KARG(char*, 13);

    LAS char* ctab = lds + K1_CTAB + (wc * 16 + er) * 96;
    LAS char* etab = lds + K1_ETAB + (wc * 16 + er) * 144;
    const f32x4 cst = *(LAS f32x4*)ctab;
    const f32x2 a1 = {cst[0], cst[1]}, cf = {cst[2], cst[3]};
    u32x4 gt[4];
#pragma unroll
    for (int m = 0; m < 4; ++m) gt[m] = *(LAS u32x4*)(lds + K1_GTAB + wr * 256 + eq * 64 + m * 16);
    FSTAMP(25);
    const __amdgpu_buffer_rsrc_t sA2 = SRD(ws2 + WS_A2, WS_A2_BYTES);
#define ST16(voff, v) __builtin_amdgcn_raw_buffer_store_b128(v, sA2, voff, 0, STPOL)
#define PK8(v) (u32x4){pk2(v[0], v[1]), pk2(v[2], v[3]), pk2(v[4], v[5]), pk2(v[6], v[7])}
    const int mblk = pm * 2 + (wr >> 1);
#define IMG_Q(r, q) (unsigned)(((q) >> 1) * 256 + ((q) & 1) * 16 + ((r) & 3) * 32 + mblk * 16384 + (wr & 1) * 1024)
#define IMG_T(r, q) (IMG_Q(r, q) + (unsigned)((((r) >> 2) & 1) * 2048 + ((r) >> 3) * 128 + (j >> 2) * A2S + (j & 3) * 4096))
#define IMG_M(r, q) (IMG_Q(r, q) + (unsigned)((((r) >> 2) & 1) * 2048 + ((r) >> 3) * 512 + (24 + (j >> 2)) * A2S + (j & 3) * 4096))
#define IMG_S(r, q) (IMG_Q(r, q) + (unsigned)((((r) >> 2) & 1) * 4096 + ((r) >> 3) * 2048 + (32 + (j >> 1)) * A2S + (j & 1) * 8192))
    const unsigned lo = IMG_T(er, eq);
#define GLU_HALF(n, hh, o) do { float v[8]; \
        _Pragma("unroll") for (int e = 0; e < 8; ++e) { const int m = 2 * (hh) + (e >> 2), q = e & 3; v[e] = relu(acc[m][n][q]) * (acc[m][3 + (n)][q] * bfhi(gt[m][q])); } \
        o = PK8(v); } while (0)
#define GLU_TILE(n) do { u32x4 o_; GLU_HALF(n, 0, o_); ST16(lo + (wc * 48 + 8 * (n)) * A2S, o_); GLU_HALF(n, 1, o_); ST16(lo + (wc * 48 + 8 * (n)) * A2S + 512, o_); } while (0)
    GLU_TILE(0);
    GLU_TILE(1);
    asm volatile("" ::: "memory");
    {
        f32x2 h = {acc[0][7][0], acc[0][8][0]};
#pragma unroll
        for (int e = 1; e < 16; ++e) h = cfma(a1, h, (f32x2){acc[e >> 2][7][e & 3], acc[e >> 2][8][e & 3]});
        *(LAS f32x2*)(etab + (wr * 4 + eq) * 8) = h;
    }
    WAIT_V(0); WAIT_L0();
    FSTAMP(26);
    BAR(); asm volatile("" ::: "memory");
    gu32* rdy = (gu32*)(ws2 + WS_RDY);
    if (tz == 192) __hip_atomic_store(rdy + pm * 32 + j, 1u, RLX_AGENT);
    gu64* carry = (gu64*)(ws2 + WS_CARRY);
#define CHAN(c, r) (32 * j + 8 * (c) + 4 * ((r) >> 3) + 16 * (((r) >> 2) & 1) + ((r) & 3))
    if (wid < 2 && pm < 7) {
        const f32x2 a16 = *(LAS f32x2*)(lds + K1_CTAB + (wid * 16 + er) * 96 + 24);
        f32x4 E[8];
#pragma unroll
        for (int i = 0; i < 8; ++i) E[i] = *(LAS f32x4*)(lds + K1_ETAB + (wid * 16 + er) * 144 + i * 16);
        f32x2 run = {E[0][0], E[0][1]};
#pragma unroll
        for (int i = 1; i < 16; ++i) run = cfma(a16, run, (f32x2){E[i >> 1][(i & 1) * 2], E[i >> 1][(i & 1) * 2 + 1]});
        unsigned ux = __float_as_uint(run.x), uy = __float_as_uint(run.y);
        if (ux == 0xFFFFFFFFu) ux = 0x7FC00000u;
        if (eq == 0) __hip_atomic_store(carry + pm * 1024 + CHAN(wid, er), ((unsigned long long)uy << 32) | ux, RLX_AGENT);
    }
    STAMP(0, 3);
    FSTAMP(27);
    GLU_TILE(2);
    {
        float v[8];
        const bool lowh = er < 8;
#pragma unroll
        for (int e = 0; e < 8; ++e) {
            const int m = e >> 2, q = e & 3;
            const float v0 = lowh ? relu(acc[m][6][q]) : acc[m][6][q] * bfhi(gt[m][q]);
            const float v1 = lowh ? relu(acc[2 + m][6][q]) : acc[2 + m][6][q] * bfhi(gt[2 + m][q]);
            const float rcv = __int_as_float(__builtin_amdgcn_mov_dpp(__float_as_int(lowh ? v1 : v0), 0x128, 0xf, 0xf, true));
            v[e] = (lowh ? v0 : v1) * rcv;
        }
        ST16(IMG_M(er, eq) + wc * 128, PK8(v));
    }
    LAS f32x4* park = (LAS f32x4*)(lds + 3 * 32768 + wr * 8192 + (tz & 63) * 16);
    if (wc == 1) {
#pragma unroll
        for (int m = 0; m < 4; ++m) { park[m * 128] = acc[m][7]; park[m * 128 + 64] = acc[m][8]; }
    }
    STAMP(0, 4);
    WAIT_L0();
    FSTAMP(28);
    BAR(); asm volatile("" ::: "memory");
    FSTAMP(29);

    ROLE(bid, idx);
    const int n0 = nblk * 128;
    const int rs0 = late ? (48 + SHIFT) % 5 : (40 - SHIFT) % 5;

    if (wc == 1) {
        int ty = threadIdx.x; asm volatile("" : "+v"(ty) :: "memory");
        const int ln = ty & 63, e3 = ln >> 3;
#define LEVEL(lvl) do { if (ty == 256) __hip_atomic_store(rdy + pm * 32 + j, (unsigned)(lvl), RLX_AGENT); } while (0)
        const __amdgpu_buffer_rsrc_t sC = SRD(late ? KARG(const u16*, 10) : KARG(const u16*, 9), DM * NP * 2), sW = SRD(KARG(const u16*, 11), FF * DM * 2);
        const unsigned voffA = (unsigned)(ln * 16);
        const unsigned voffC = (unsigned)(e3 * 2048 + (((ln & 7) ^ (e3 & 6)) * 16));
        const unsigned voffD = (unsigned)(((e3 >> 2) * 8 + (e3 & 3)) * 2048 + ((((ln >> 1) & 3) ^ (e3 >> 1)) * 32) + (ln & 1) * 16);
        const unsigned soA = (unsigned)((late ? 0 : 48) * A2S + mb2 * 16384 + wr * 4096);
        const unsigned soC = (unsigned)((n0 + 32 * wr) * 2048);
        const unsigned soD = (unsigned)(((late ? 0 : 48) + 112 * wr) * 2048 + n0 * 2);
        const unsigned mB = ldsb + wr * 4096;
#define DMA_A(i, k, sl) dma16c(mB + (sl) + (i) * 1024, voffA, sA2, soA + (k) * A2S + (i) * 1024)
#define DMA_C(srd, i, p, sl) dma16(mB + (sl) + 16384 + (i) * 1024, voffC, srd, soC + (p) * 128 + (i) * (8 * 2048))
#define DMA_D(i, row, sl) dma16(mB + (sl) + 16384 + (i) * 1024, voffD, sW, soD + (row) * 2048 + ((i) >> 1) * (4 * 2048) + ((i) & 1) * 128)
#define POLL(v, p) asm volatile("global_load_dword %0, %1, off sc1\n\ts_waitcnt vmcnt(0)" : "=v"(v) : "v"(p) : "memory")
#define ASK(v, p) asm volatile("global_load_dword %0, %1, off sc1" : "=v"(v) : "v"(p) : "memory")
#define NEED(v, p, lvl) do { asm volatile("" : "+v"(v) :: "memory"); if (!__all((int)(v) >= (lvl))) { unsigned spins_ = 0, w_; \
            do { __builtin_amdgcn_s_sleep(1); POLL(w_, p); } while (!__all((int)w_ >= (lvl)) && ++spins_ < (1u << 18)); } } while (0)
        gu32* rl = rdy + (mb2 >> 1) * 32 + (ln & 31);
#define XP(k_) (((((k_) & 3) << 1) | (((k_) >> 2) & 1)) ^ (xc & 1))
#pragma unroll
        for (int p = 0; p < 3; ++p)
#pragma unroll
            for (int i = 0; i < 4; ++i) DMA_D(i, 448 * XP(p), p * 32768);
        unsigned fv;
        {
            const int other = ((ln >> 2) ^ xc) & 1;
            unsigned spins = 0;
            do POLL(fv, rl); while (!__all((int)fv >= 1 || (GATE2 && other)) && ++spins < (1u << 18));
        }
        FSTAMP(31);
#pragma unroll
        for (int p = 0; p < 3; ++p)
#pragma unroll
            for (int i = 0; i < 4; ++i) DMA_A(i, XP(p), p * 32768);
        WAIT_V(8); FSTAMP(32); BAR(); FSTAMP(33); LT(3);
        int k = 3, sl = 3 * 32768;
#define A4(img, sl) dma4c(mB + (sl), voffA, sA2, soA + (img) * A2S)
#define D4(row, sl) do { const unsigned b_ = soD + (row) * 2048; \
            asm volatile("s_mov_b32 m0, %0\n\ts_nop 0\n\tbuffer_load_dwordx4 %1, %2, %3 offen" BPOL " lds\n\tbuffer_load_dwordx4 %1, %2, %4 offen offset:2048" BPOL " lds\n\t" \
                         "buffer_load_dwordx4 %1, %2, %5 offen offset:3072" BPOL " lds\n\ts_mov_b32 m0, %6\n\ts_nop 0\n\tbuffer_load_dwordx4 %1, %2, %3 offen offset:128" BPOL " lds" \
                         ::"s"(mB + (sl) + 16384), "v"(voffD), "s"(sW), "s"(b_), "s"(b_ + 8192 - 2048), "s"(b_ + 8192 + 128 - 3072), "s"(mB + (sl) + 16384 + 1024 - 128) : "m0", "memory"); } while (0)
#define C4(srd, p, sl) do { const unsigned b_ = soC + (p) * 128; dma4(mB + (sl) + 16384, voffC, srd, b_, b_ + 16384 - 1024, b_ + 32768 - 2048, b_ + 49152 - 3072); } while (0)
#define STAGE_W() do { const int q_ = k < 16 ? (k & 3) | ((k & 4) << 1) | ((k & 8) >> 1) : k, x_ = XP(q_), row_ = 448 * x_ + ((q_ >> 3) == 3 ? 96 : 16 * (q_ >> 3)); \
            A4((q_ & ~7) + x_, sl); D4(row_, sl); ++k; sl = sl == 4 * 32768 ? 0 : sl + 32768; } while (0)
#define LSYNC() do { WAIT_V(16); BAR(); } while (0)
        STAGE_W(); WAIT_V(12); BAR();
        STAGE_W(); LSYNC(); STAGE_W(); LSYNC();
        ASK(fv, rl);
        STAGE_W(); LSYNC(); STAGE_W(); LSYNC();
        NEED(fv, rl, 1);
        STAGE_W(); LSYNC(); STAGE_W(); LSYNC();
        LEVEL(2);
#pragma nounroll
        while (k < 13) { STAGE_W(); LSYNC(); }
        ASK(fv, rl);
        STAGE_W(); LSYNC(); STAGE_W(); LSYNC(); STAGE_W(); LSYNC();
        NEED(fv, rl, 2);
        const int nw = late ? 32 : 24;
#pragma nounroll
        while (k < nw) { STAGE_W(); LSYNC(); }
        const int np = late ? 16 : 16 - SHIFT;
#pragma nounroll
        for (int p = 0; p < np; ++p) {
            A4(k, sl); C4(sC, p, sl);
            ++k; sl = sl == 4 * 32768 ? 0 : sl + 32768;
            LSYNC();
        }
        if (SHIFT && late) {
            const __amdgpu_buffer_rsrc_t sR = SRD(KARG(const u16*, 9), DM * NP * 2);
#pragma nounroll
            for (int p = 16 - SHIFT; p < 16; ++p) {
                A4(72 + p, sl); C4(sR, p, sl);
                sl = sl == 4 * 32768 ? 0 : sl + 32768;
                LSYNC();
            }
        }
        const int tb = mb2 * 128 + (wr >> 1) * 64 + (wr & 1) * 8 + (ln >> 5) * 16, dcol = n0 + (ln & 31) * 4;
#define ROW(i) (tb + ((i) >> 3) * 32 + ((i) & 7))
        LT(4);
        FSTAMP(40);
        gu32* flag = (gu32*)(ws2 + WS_FLAG2 + tile * 128 + wr * 32);
        const __amdgpu_buffer_rsrc_t sP = SRD(KARG(float*, 14), SEQ * DM * 4);
#define POFF(i) ((ROW(i) * DM + dcol) * 4)
        const int wn1 = (ln >> 4) & 1, s0 = rs0 + wn1, s1 = rs0 + 2 + wn1;
        const int inp = wr * 8192 + ((ln >> 2) & 3) * 1024 + (ln & 3) * 256, sw = (ln & 15) ^ ((ln >> 5) * 8);
        LAS char* q0 = lds + (s0 >= 5 ? s0 - 5 : s0) * 32768 + inp;
        LAS char* q1 = lds + (s1 >= 5 ? s1 - 5 : s1) * 32768 + inp;
#define SUMS(i) (*(LAS f32x4*)(q0 + ((i) >> 3) * 4096 + ((sw ^ ((i) & 7)) * 16)) + *(LAS f32x4*)(q1 + ((i) >> 3) * 4096 + ((sw ^ ((i) & 7)) * 16)))
        f32x4 ex[16];
        if (late) {
            const __amdgpu_buffer_rsrc_t sO = SRD(KARG(float*, 14), SEQ * DM * 4);
            WAIT_V(8); BAR();
            FSTAMP(41);
            ASK(fv, flag);
            WAIT_V(0); BAR();
            FSTAMP(42);
            NEED(fv, flag, 1);
            FSTAMP(43);
#pragma unroll
            for (int i = 0; i < 16; ++i) ex[i] = __builtin_bit_cast(f32x4, __builtin_amdgcn_raw_buffer_load_b128(sP, POFF(i), 0, 16));
            FSTAMP(44);
            BAR(); asm volatile("" ::: "memory");
            FSTAMP(45);
            STAMP(1, 3);
#pragma unroll
            for (int i = 0; i < 16; ++i) __builtin_amdgcn_raw_buffer_store_b128(__builtin_bit_cast(u32x4, ex[i] + SUMS(i)), sO, (ROW(i) * DM + dcol) * 4, 0, OUTPOL);
        } else {
            const __amdgpu_buffer_rsrc_t sXk = SRD(KARG(const u16*, 0), SEQ * DM * 2), sDk = SRD(KARG(const u16*, 12), DM * 2), sGk = SRD(ws2 + WS_G, 8192);
            WAIT_V(8); BAR();
            FSTAMP(41);
            u32x2 dk = __builtin_amdgcn_raw_buffer_load_b64(sDk, dcol * 2, 0, 0), xv[16]; u32x4 gwd[4];
#pragma unroll
            for (int i = 0; i < 4; ++i) gwd[i] = __builtin_amdgcn_raw_buffer_load_b128(sGk, ROW(4 * i) * 4, 0, 16);
#pragma unroll
            for (int i = 0; i < 16; ++i) xv[i] = __builtin_amdgcn_raw_buffer_load_b64(sXk, (tb * DM + dcol) * 2 + (i & 1) * 2048, (((i) >> 3) * 32 + ((i) & 6)) * 2048, 0);
            WAIT_V(21); BAR();
            FSTAMP(42);
            asm volatile("" : "+v"(dk), "+v"(gwd[0]), "+v"(gwd[1]), "+v"(gwd[2]), "+v"(gwd[3]));
#pragma unroll
            for (int i = 0; i < 16; ++i) {
                const float g = bflo(gwd[i >> 2][i & 3]);
                ex[i] = (f32x4){g * rbf(bflo(xv[i][0]) * bflo(dk[0])), g * rbf(bfhi(xv[i][0]) * bfhi(dk[0])),
                                g * rbf(bflo(xv[i][1]) * bflo(dk[1])), g * rbf(bfhi(xv[i][1]) * bfhi(dk[1]))};
                asm volatile("" : "+v"(ex[i]));
            }
            FSTAMP(44);
            BAR(); asm volatile("" ::: "memory");
            FSTAMP(45);
            STAMP(1, 3);
#pragma unroll
            for (int i = 0; i < 16; ++i) __builtin_amdgcn_raw_buffer_store_b128(__builtin_bit_cast(u32x4, ex[i] + SUMS(i)), sP, POFF(i), 0, 16);
            FSTAMP(46);
            WAIT_V(0);
            if (ln == 0) __hip_atomic_store(flag, 1u, RLX_AGENT);
        }
        STAMP(1, 6);
        STAMP(1, 7);
        FSTAMP(47);
        FSTAMP(48);
        FDUMP();
        asm volatile(".Lk1_end_a:");
        return;
    }

    {
        unsigned long long gq[2][7];
#pragma unroll
        for (int c = 0; c < 2; ++c)
#pragma unroll
            for (int q = 0; q < 7; ++q) gq[c][q] = __hip_atomic_load(carry + (q + pm >= 7 ? q + pm - 7 : 0) * 1024 + CHAN(c, er), RLX_AGENT);
        f32x2 h[2];
        {
            f32x2 a16[2], run[2]; f32x4 E[2][6], F0[2]; f32x2 F1[2];
#pragma unroll
            for (int c = 0; c < 2; ++c) {
                a16[c] = *(LAS f32x2*)(ctab + c * (16 * 96) + 24);
#pragma unroll
                for (int i = 0; i < 6; ++i) E[c][i] = *(LAS f32x4*)(etab + c * (16 * 144) + i * 16);
                F0[c] = *(LAS f32x4*)(etab + c * (16 * 144) + wr * 32); F1[c] = *(LAS f32x2*)(etab + c * (16 * 144) + wr * 32 + 16);
                run[c] = (f32x2){0.f, 0.f};
            }
#pragma unroll
            for (int w = 0; w < 3; ++w)
                if (wr > w) {
#pragma unroll
                    for (int i = 0; i < 4; ++i)
#pragma unroll
                        for (int c = 0; c < 2; ++c) run[c] = cfma(a16[c], run[c], (f32x2){E[c][2 * w + (i >> 1)][(i & 1) * 2], E[c][2 * w + (i >> 1)][(i & 1) * 2 + 1]});
                }
#pragma unroll
            for (int c = 0; c < 2; ++c) {
                const f32x2 r1 = cfma(a16[c], run[c], (f32x2){F0[c][0], F0[c][1]}), r2 = cfma(a16[c], r1, (f32x2){F0[c][2], F0[c][3]}), r3 = cfma(a16[c], r2, F1[c]);
                h[c] = sel4(eq, run[c], r1, r2, r3);
            }
        }
        f32x4 ur[4], ui[4];
#pragma unroll
        for (int m = 0; m < 4; ++m) { ur[m] = park[m * 128]; ui[m] = park[m * 128 + 64]; }
        const f32x4 cs1 = *(LAS f32x4*)(ctab + 16 * 96);
        FSTAMP(30);
        f32x2 a256[2], aq[2];
#pragma unroll
        for (int c = 0; c < 2; ++c) {
            LAS char* ct = ctab + c * (16 * 96);
            a256[c] = *(LAS f32x2*)(ct + 80); aq[c] = cmul(*(LAS f32x2*)(ct + 16 + eq * 8), *(LAS f32x2*)(ct + 48 + wr * 8));
        }
        unsigned mx = 0;
#pragma unroll
        for (int q = 0; q < 7; ++q) mx = max(mx, max((unsigned)gq[0][q], (unsigned)gq[1][q]));
        if (pm > 0 && __any(mx == 0xFFFFFFFFu)) {
            unsigned spins = 0;
            do {
                mx = 0;
#pragma unroll
                for (int c = 0; c < 2; ++c)
#pragma unroll
                    for (int q = 0; q < 7; ++q) {
                        gq[c][q] = __hip_atomic_load(carry + (q + pm >= 7 ? q + pm - 7 : 0) * 1024 + CHAN(c, er), RLX_AGENT);
                        mx = max(mx, (unsigned)gq[c][q]);
                    }
            } while (__any(mx == 0xFFFFFFFFu) && ++spins < (1u << 18));
        }
        f32x2 cg[2] = {{0.f, 0.f}, {0.f, 0.f}};
#pragma unroll
        for (int q = 0; q < 7; ++q) {
            const unsigned keep = q + pm >= 7 ? 0xFFFFFFFFu : 0u;
#pragma unroll
            for (int c = 0; c < 2; ++c) cg[c] = cfma(a256[c], cg[c], (f32x2){__uint_as_float((unsigned)gq[c][q] & keep), __uint_as_float((unsigned)(gq[c][q] >> 32) & keep)});
        }
        h[0] = cfma(aq[0], cg[0], h[0]); h[1] = cfma(aq[1], cg[1], h[1]);
        STAMP(0, 5);
        FSTAMP(31);
        const unsigned ls = IMG_S(er, eq);
        const f32x2 b1 = {cs1[0], cs1[1]}, bf = {cs1[2], cs1[3]};
#pragma unroll
        for (int hh = 0; hh < 2; ++hh) {
            float vr[8], vi[8], wr8[8], wi8[8];
#pragma unroll
            for (int e = 0; e < 8; ++e) {
                const int m = 2 * hh + (e >> 2), q = e & 3;
                h[0] = cfma(a1, h[0], (f32x2){acc[m][7][q], acc[m][8][q]});
                h[1] = cfma(b1, h[1], (f32x2){ur[m][q], ui[m][q]});
                const float g2 = bflo(gt[m][q]);
                const f32x2 s0 = cmul(cf, h[0]), s1 = cmul(bf, h[1]);
                vr[e] = s0.x * g2; vi[e] = -s0.y * g2; wr8[e] = s1.x * g2; wi8[e] = -s1.y * g2;
            }
            ST16(ls + 40 * A2S + hh * 512, PK8(vr)); ST16(ls + 40 * A2S + hh * 512 + 128, PK8(wr8));
            ST16(ls + hh * 512, PK8(vi)); ST16(ls + hh * 512 + 128, PK8(wi8));
        }
    }
    STAMP(0, 6);
    int ty = threadIdx.x; asm volatile("" : "+v"(ty) :: "memory");
    const int ln = ty & 63, fr2 = ty & 15, fq2 = (ty >> 4) & 3;
    const int wn = wid & 1, kk = wid >> 1;
    LAS char* rH = lds + kk * 8192 + (ln >> 5) * 4096 + (ln & 31) * 8;
    LAS char* rC = lds + 16384 + (wn * 64 + fr2) * 128 + (((4 * kk + fq2) ^ (fr2 & 6)) * 16);
    LAS char* rWb = lds + 16384 + kk * 8192 + (ln >> 5) * 4096 + wn * 1024 + ((ln >> 2) & 7) * 128 + (ln & 3) * 8;
    const int rsw = (ln >> 3) & 3;
    f32x4 ac2[4][8];
#pragma unroll
    for (int n = 0; n < 4; ++n)
#pragma unroll
        for (int m = 0; m < 8; ++m) { ac2[n][m] = (f32x4){0.f, 0.f, 0.f, 0.f}; asm volatile("" : "+v"(ac2[n][m])); }
    STAMP(0, 7);
    FSTAMP(32);
    FDUMP();
    s16x4 HX[8][2], HY[8][2], WX[4][2], WY[4][2];
    int sl = 0;

#define RD1(RD, i, H, W) do { \
        if ((i) < 8) { \
            if (RD == 1) W[(i) >> 1][(i) & 1] = tr4(rWb + sl + ((((i) >> 1) ^ rsw) * 32) + ((i) & 1) * 2048); \
            else if (((i) & 1) == 0) { const s16x8 v_ = *(LAS s16x8*)(rC + sl + ((i) >> 1) * 2048); \
                W[(i) >> 1][0] = __builtin_shufflevector(v_, v_, 0, 1, 2, 3); W[(i) >> 1][1] = __builtin_shufflevector(v_, v_, 4, 5, 6, 7); } \
        } else H[((i) - 8) >> 1][(i) & 1] = tr4(rH + sl + (((i) - 8) >> 1) * 256 + ((i) & 1) * 2048); \
    } while (0)
#define LGKM0() __builtin_amdgcn_s_waitcnt(0xC07F)
#define MM(i, HC, WC) mfma_acc(ac2[(i) & 3][(i) >> 2], cat8(WC[(i) & 3][0], WC[(i) & 3][1]), cat8(HC[(i) >> 2][0], HC[(i) >> 2][1]))
#define STEP(RD, HC, WC, HN, WN) do { \
        _Pragma("unroll") for (int i = 0; i < 32; ++i) { MM(i, HC, WC); if (i < 24) RD1(RD, i, HN, WN); } \
        sl = sl == 4 * 32768 ? 0 : sl + 32768; LGKM0(); \
    } while (0)

    LGKM0();
    BAR();
    STAMP_TAKE(st9);
#pragma unroll
    for (int i = 0; i < 24; ++i) RD1(1, i, HX, WX);
    sl = 32768; LGKM0();
    const int nf = late ? 15 : 11;
#pragma nounroll
    for (int i = 0; i < nf; ++i) {
        if (i == 3) WAIT_V(0);
        BAR(); STEP(1, HX, WX, HY, WY); BAR(); STEP(1, HY, WY, HX, WX);
    }
    BAR(); STEP(1, HX, WX, HY, WY); BAR(); STEP(0, HY, WY, HX, WX);
#pragma nounroll
    for (int i = late ? -SHIFT / 2 : SHIFT / 2; i < 7; ++i) { BAR(); STEP(0, HX, WX, HY, WY); BAR(); STEP(0, HY, WY, HX, WX); }
    BAR(); STEP(0, HX, WX, HY, WY);
    {
        const int sw = rs0 + wid;
        LAS char* dmp = lds + (sw >= 5 ? sw - 5 : sw) * 32768 + fq2 * 256;
#define DUMP(i) *(LAS f32x4*)(dmp + (i) * 1024 + ((fr2 ^ (4 * ((i) & 3) + fq2)) * 16)) = ac2[(i) & 3][(i) >> 2]
#pragma unroll
        for (int i = 0; i < 32; ++i) { MM(i, HY, WY); if (i >= 12) DUMP(i - 12); }
        asm volatile("s_nop 15" ::: "memory");
#pragma unroll
        for (int i = 20; i < 32; ++i) DUMP(i);
    }
    WAIT_L0();
    STAMP_PUT(1, 0, st0); STAMP_PUT(1, 1, st9);
    STAMP(1, 2);
    BAR();
    asm volatile(".Lk1_end_b:");
}

extern "C" void kernel_launch(void* const* d_in, const int* in_sizes, int n_in, void* d_out, int out_size, void* d_ws, size_t ws_size,
                              hipStream_t stream) {
    const u16* x = (const u16*)d_in[0];
    const u16* lre = (const u16*)d_in[1];
    const u16* lim = (const u16*)d_in[2];
    const u16* Bre = (const u16*)d_in[3];
    const u16* Bim = (const u16*)d_in[4];
    const u16* Cre = (const u16*)d_in[5];
    const u16* Cim = (const u16*)d_in[6];
    const u16* logdt = (const u16*)d_in[7];
    const u16* dskip = (const u16*)d_in[8];
    const u16* Wg = (const u16*)d_in[10];
    const u16* Wu = (const u16*)d_in[11];
    const u16* Wd = (const u16*)d_in[12];
    const u16* gw = (const u16*)d_in[13];
    char* ws = (char*)d_ws;
    (void)hipFuncSetAttribute((const void*)k1_fused, hipFuncAttributeMaxDynamicSharedMemorySize, K2_LDS);
    k1_fused<<<256, 512, K2_LDS, stream>>>(x, Wg, Wu, Bre, Bim, lre, lim, logdt, gw, Cre, Cim, Wd, dskip, ws, (float*)d_out);
}
```

```cpp
#include <hip/hip_runtime.h>
#include <hip/hip_bf16.h>

typedef unsigned short u16;
typedef __bf16 bf16x8 __attribute__((ext_vector_type(8)));
typedef __bf16 bf16x2 __attribute__((ext_vector_type(2)));
typedef short s16x4 __attribute__((ext_vector_type(4)));
typedef short s16x8 __attribute__((ext_vector_type(8)));
typedef float f32x4 __attribute__((ext_vector_type(4)));
typedef float f32x2 __attribute__((ext_vector_type(2)));
typedef unsigned u32x4 __attribute__((ext_vector_type(4)));
typedef unsigned u32x2 __attribute__((ext_vector_type(2)));
#define LAS __attribute__((address_space(3)))
#define GAS __attribute__((address_space(1)))
typedef GAS unsigned gu32;
typedef GAS unsigned long long gu64;
typedef __attribute__((address_space(4))) const unsigned long long* kptr;
#define RLX_AGENT __ATOMIC_RELAXED, __HIP_MEMORY_SCOPE_AGENT

constexpr int SEQ = 2048, DM = 1024, NP = 1024, FF = 3584;
constexpr int KSTEPS2 = (2 * NP + FF) / 64;

constexpr unsigned WS_CARRY = 0;
constexpr unsigned WS_G = 7 * 8192;
constexpr unsigned WS_RDY = 65536;
constexpr unsigned WS_FLAG2 = WS_RDY + 1024;
constexpr unsigned WS_HQ = WS_FLAG2 + 16384;
constexpr unsigned WS_HA = WS_HQ + 1024;
constexpr unsigned WS_HR = WS_HA + 1024;
constexpr unsigned WS_SYNC_BYTES = WS_HR + 2048;
constexpr size_t WS_A2 = 1 << 20;
constexpr unsigned A2S = 262144 + 128;
constexpr size_t WS_A2_BYTES = (size_t)(KSTEPS2 + 32) * A2S;

__device__ __forceinline__ float bflo(unsigned v) { return __uint_as_float(v << 16); }
__device__ __forceinline__ float bfhi(unsigned v) { return __uint_as_float(v & 0xffff0000u); }
__device__ __forceinline__ float rbf(float f) { return (float)(__bf16)f; }
__device__ __forceinline__ unsigned pk2(float a, float b) {
    bf16x2 v; v.x = (__bf16)a; v.y = (__bf16)b; return __builtin_bit_cast(unsigned, v);
}
__device__ __forceinline__ float relu(float v) { return __int_as_float(max(__float_as_int(v), 0)); }
__device__ __forceinline__ float dot2(unsigned a, unsigned b, float c) {
    return __builtin_amdgcn_fdot2_f32_bf16(__builtin_bit_cast(bf16x2, a), __builtin_bit_cast(bf16x2, b), c, false);
}

constexpr size_t WS_DBG = 200u << 20;
#define STAMP(kid, i)
#define STAMP_TAKE(v)
#define STAMP_PUT(kid, i, v)

#define FSTAMP(i)
#define FDUMP()
#define WAIT_V(n) asm volatile("s_waitcnt vmcnt(" #n ")" ::: "memory")
#define WAIT_L0() asm volatile("s_waitcnt lgkmcnt(0)" ::: "memory")
#define BAR() __builtin_amdgcn_s_barrier()
#define SCHED() __builtin_amdgcn_sched_barrier(0)
#define SRD(p, bytes) __builtin_amdgcn_make_buffer_rsrc((void*)(p), (short)0, (int)(bytes), 0x00020000)

__device__ __forceinline__ void dma16(unsigned m0v, unsigned voff, __amdgpu_buffer_rsrc_t srd, unsigned soff) {
    asm volatile("s_mov_b32 m0, %0\n\ts_nop 0\n\tbuffer_load_dwordx4 %1, %2, %3 offen lds" ::"s"(m0v), "v"(voff), "s"(srd), "s"(soff) : "m0", "memory");
}
__device__ __forceinline__ bf16x8 lds_b128(LAS char* p) { return __builtin_bit_cast(bf16x8, *(LAS s16x8*)p); }
__device__ __forceinline__ bf16x8 lds_tr(LAS char* p0, LAS char* p1) {
    s16x4 a = __builtin_amdgcn_ds_read_tr16_b64_v4i16((LAS s16x4*)p0);
    s16x4 b = __builtin_amdgcn_ds_read_tr16_b64_v4i16((LAS s16x4*)p1);
    return __builtin_bit_cast(bf16x8, __builtin_shufflevector(a, b, 0, 1, 2, 3, 4, 5, 6, 7));
}
#define MFMA(a, b, c) __builtin_amdgcn_mfma_f32_16x16x32_bf16(a, b, c, 0, 0, 0)
__device__ __forceinline__ f32x2 cmul(f32x2 a, f32x2 b) { return (f32x2){__builtin_fmaf(a.x, b.x, -(a.y * b.y)), __builtin_fmaf(a.x, b.y, a.y * b.x)}; }
__device__ __forceinline__ f32x2 cfma(f32x2 a, f32x2 b, f32x2 c) {
    return (f32x2){__builtin_fmaf(a.x, b.x, __builtin_fmaf(-a.y, b.y, c.x)), __builtin_fmaf(a.x, b.y, __builtin_fmaf(a.y, b.x, c.y))};
}
__device__ __forceinline__ f32x2 sel4(int i, f32x2 a, f32x2 b, f32x2 c, f32x2 d) {
    const bool o = i & 1, u = i & 2;
    const float lx = o ? b.x : a.x, ly = o ? b.y : a.y, hx = o ? d.x : c.x, hy = o ? d.y : c.y;
    return (f32x2){u ? hx : lx, u ? hy : ly};
}

#define STPOL 16
#define PF_W0 1
#define PF_D0 4
#define OUTPOL 16

#define K2_SPLIT 40
#define MAP 0
#define ABL_NOLOAD 0
#define ABL_NOREAD 0
#define ABL_NOMFMA 0
#define CODE_T 10
#define ILV 1
#define SHIFT 2
#define GATE2 1
#define PRO 0
#define MORD 3
#define DEEP 0
#define THIN 0
#define M04 1
#define LATE_FLIP 0
#define DIST 5
constexpr int K2_LDS = 5 * 32768;

__device__ __forceinline__ void dma16c(unsigned m0v, unsigned voff, __amdgpu_buffer_rsrc_t srd, unsigned soff) {
    asm volatile("s_mov_b32 m0, %0\n\ts_nop 0\n\tbuffer_load_dwordx4 %1, %2, %3 offen sc1 lds" ::"s"(m0v), "v"(voff), "s"(srd), "s"(soff) : "m0", "memory");
}
#define BPOL ""
#define APOL " sc1"
__device__ __forceinline__ void dma4c(unsigned m0v, unsigned voff, __amdgpu_buffer_rsrc_t srd, unsigned soff) {
    asm volatile("s_mov_b32 m0, %0\n\ts_nop 0\n\tbuffer_load_dwordx4 %1, %2, %3 offen" APOL " lds\n\tbuffer_load_dwordx4 %1, %2, %3 offen offset:1024" APOL " lds\n\t"
                 "buffer_load_dwordx4 %1, %2, %3 offen offset:2048" APOL " lds\n\tbuffer_load_dwordx4 %1, %2, %3 offen offset:3072" APOL " lds" ::"s"(m0v), "v"(voff), "s"(srd), "s"(soff) : "m0", "memory");
}
__device__ __forceinline__ void dma4(unsigned m0v, unsigned voff, __amdgpu_buffer_rsrc_t srd, unsigned s0, unsigned s1, unsigned s2, unsigned s3) {
    asm volatile("s_mov_b32 m0, %0\n\ts_nop 0\n\tbuffer_load_dwordx4 %1, %2, %3 offen lds\n\tbuffer_load_dwordx4 %1, %2, %4 offen offset:1024 lds\n\t"
                 "buffer_load_dwordx4 %1, %2, %5 offen offset:2048 lds\n\tbuffer_load_dwordx4 %1, %2, %6 offen offset:3072 lds" ::"s"(m0v), "v"(voff), "s"(srd), "s"(s0), "s"(s1), "s"(s2), "s"(s3) : "m0", "memory");
}
__device__ __forceinline__ void mfma_acc(f32x4& c, bf16x8 a, bf16x8 b) {
    asm volatile("v_mfma_f32_16x16x32_bf16 %0, %1, %2, %0" : "+v"(c) : "v"(a), "v"(b) : "memory");
}
__device__ __forceinline__ s16x4 tr4(LAS char* p) { return __builtin_amdgcn_ds_read_tr16_b64_v4i16((LAS s16x4*)p); }
__device__ __forceinline__ bf16x8 cat8(s16x4 a, s16x4 b) { return __builtin_bit_cast(bf16x8, __builtin_shufflevector(a, b, 0, 1, 2, 3, 4, 5, 6, 7)); }

constexpr int K1_ASB = 40960;
constexpr int K1_W = 81920;
constexpr int K1_WBUF = 28704, K1_UOFF = 14352;
constexpr int K1_ETAB = K1_W + 2 * K1_WBUF;
constexpr int K1_CTAB = K1_ETAB + 32 * 144;
constexpr int K1_SINK = K1_CTAB + 32 * 96;
constexpr int K1_GTAB = K1_SINK + 1024;
constexpr int K1_HS = K1_GTAB + 1024;

__global__ __launch_bounds__(512, 2) void k1_fused(const u16* __restrict__ x, const u16* __restrict__ Wg, const u16* __restrict__ Wu,
                                                       const u16* __restrict__ Bre, const u16* __restrict__ Bim, const u16* __restrict__ lre,
                                                       const u16* __restrict__ lim, const u16* __restrict__ logdt, const u16* __restrict__ gw,
                                                       const u16* __restrict__ Cre, const u16* __restrict__ Cim, const u16* __restrict__ Wd,
                                                       const u16* __restrict__ dskip, char* __restrict__ ws, float* __restrict__ out) {
    extern __shared__ __attribute__((aligned(16))) char smem[];
    LAS char* lds = (LAS char*)smem;
    const unsigned ldsb = (unsigned)(size_t)lds;
    const int tid = threadIdx.x, lane = tid & 63;
    const int wid = __builtin_amdgcn_readfirstlane(tid >> 6);
    const int wr = wid & 3, wc = wid >> 2;
    const int bid = blockIdx.x, idx = bid >> 3, j = (bid & 7) * 4 + (idx & 3), pm = idx >> 2;
    const int fr = lane & 15, fq = lane >> 4;
#define ROLE(b_, i_) const int xc = (b_) & 7, late = (xc & 1) ^ LATE_FLIP, mb2 = (xc >> 2) * 8 + ((i_) >> 2), nblk = ((xc >> 1) & 1) * 4 + ((i_) & 3), tile = mb2 * 8 + nblk
    STAMP_TAKE(st0);
#define LT(n)
    FSTAMP(0);

    const char* code0; unsigned codeA, codeB;
    asm volatile("s_getpc_b64 %0\n.Lk1_ref:\n\ts_mov_b32 %1, .Lk1_end_a-.Lk1_ref\n\ts_mov_b32 %2, .Lk1_end_b-.Lk1_ref" : "=s"(code0), "=s"(codeA), "=s"(codeB));
    const unsigned codeN = (codeA > codeB ? codeA : codeB) + 192;
    if (wid == 7) {
        const __amdgpu_buffer_rsrc_t sI = SRD(code0, codeN);
        dma16(ldsb + K1_SINK, (unsigned)(lane * 128), sI, 0u); dma16(ldsb + K1_SINK, (unsigned)(lane * 128), sI, 8192u);
    }

    const int ch = 32 * j + 8 * (fq & 1) + 4 * (fr >> 3) + 16 * ((fr >> 2) & 1) + (fr & 3);
    const int grow = pm * 256 + j * 8 + wid;
    u32x4 xa, xb, ga, gb; unsigned plr, pli, pld;
    {
        const u16* xr = x + (size_t)grow * DM;
        const unsigned l16 = lane * 16, c2 = ch * 2;
        asm volatile("s_nop 4\n\tglobal_load_dwordx4 %0, %7, %9\n\tglobal_load_dwordx4 %1, %7, %9 offset:1024\n\t"
                     "global_load_dwordx4 %2, %7, %10\n\tglobal_load_dwordx4 %3, %7, %10 offset:1024\n\t"
                     "global_load_ushort %4, %8, %11\n\tglobal_load_ushort %5, %8, %12\n\tglobal_load_ushort %6, %8, %13"
                     : "=&v"(xa), "=&v"(xb), "=&v"(ga), "=&v"(gb), "=&v"(plr), "=&v"(pli), "=&v"(pld)
                     : "v"(l16), "v"(c2), "s"(xr), "s"(gw), "s"(lre), "s"(lim), "s"(logdt) : "memory");
    }

    const bool pf = wid == 7;
    const int q4 = wid + 28, q5 = wid + 35;
    const __amdgpu_buffer_rsrc_t sX = SRD(!pf || !(bid & 32) ? x : (bid & 16) ? Bim : Bre, !pf || !(bid & 32) ? SEQ * DM * 2 : NP * DM * 2);
    const int pb = (bid >> 3) | ((bid & 7) << 5);
    const __amdgpu_buffer_rsrc_t sG = SRD(!pf ? Wg : (pb & 1) ? Wd : (pb & 2) ? Cim : Cre, (pf && !(pb & 1)) ? DM * NP * 2 : DM * FF * 2);
    const __amdgpu_buffer_rsrc_t sU = SRD((pf && (pb & 1)) ? Wg : Wu, DM * FF * 2);
    const __amdgpu_buffer_rsrc_t s4 = SRD(pf ? (const u16*)ws : q4 < 32 ? x : (q4 & 2) ? Bim : Bre, pf ? WS_SYNC_BYTES : q4 < 32 ? SEQ * DM * 2 : NP * DM * 2);
    const __amdgpu_buffer_rsrc_t s5 = SRD(pf ? (const u16*)code0 : (q5 & 2) ? Bim : Bre, pf ? codeN : q5 < 40 ? NP * DM * 2 : 0);

    const int l3 = lane >> 3;
    const unsigned voffX = pf ? (unsigned)(lane * 2048) : (unsigned)(((l3 >> 2) * 16 + (l3 & 3)) * 2048 + (((lane & 7) ^ (l3 & 6)) * 16));
    unsigned voffW;
    { const int gam = wid * 64 + lane, rho = gam / 14, c = gam - rho * 14, k = (rho & 0x13) | ((rho & 4) << 1) | ((rho & 8) >> 1);
      voffW = pf ? (unsigned)(lane * 128) : (unsigned)(k * (FF * 2) + c * 16); }
#define XROW(q) (pm * 256 + ((q) >> 3) * 64 + (((q) >> 2) & 1) * 8 + ((q) & 1) * 32 + (((q) >> 1) & 1) * 4)
#define BROW(q) (32 * j + 8 * (((q) >> 2) & 1) + 4 * ((q) & 1))
    constexpr unsigned WST = 64 * FF * 2, WSH = 32 * FF * 2, FAR = 0x40000000u;
    const unsigned so0 = pf ? 0u : (unsigned)(XROW(wid) * 2048);
    unsigned px = (unsigned)((bid & ((bid & 32) ? 15 : 31)) * 131072 + (bid >> 6) * 128), pxs = 512;
    px = 0x80000000u; pxs = 0;
    const unsigned so1 = pf ? FAR : (unsigned)(XROW(wid + 7) * 2048), so2 = pf ? FAR : (unsigned)(XROW(wid + 14) * 2048), so3 = pf ? FAR : (unsigned)(XROW(wid + 21) * 2048),
                   so4 = pf ? FAR : (unsigned)((q4 < 32 ? XROW(q4) : BROW(q4)) * 2048), so5 = pf ? FAR : (unsigned)(BROW(q5) * 2048);
    const unsigned soW = (unsigned)(j * 224), soW2 = pf ? FAR : soW + WSH;
    const unsigned mSink = ldsb + K1_SINK, mb0 = pf ? mSink : ldsb + wid * 1024, mk = pf ? 0u : ~0u;
#define MZ(off) (mb0 + ((unsigned)(off) & mk))
    const unsigned m50 = q5 < 40 ? MZ(35 * 1024) : mSink, m51 = q5 < 40 ? MZ(35 * 1024 + K1_ASB) : mSink;
    const unsigned ust = pf ? (1u << 20) : WST, gst = !pf ? WST : (pb & 1) ? (1u << 20) : (1u << 19);
    const unsigned su0 = pf ? (unsigned)((pb >> 1) * 8192) + (unsigned)(PF_W0) * ust : soW;
    const unsigned sg0 = pf ? (unsigned)(((pb & 1) ? pb >> 1 : pb >> 2) * 8192) - (unsigned)(PF_D0 + 2) * gst : soW;
#define ST_X(i, ko, b) dma16(MZ((b) * K1_ASB + (i) * 7 * 1024), voffX, sX, ((i) == 0 ? so0 : (i) == 1 ? so1 : (i) == 2 ? so2 : so3) + (ko))
#define ST_S4(ko, b) dma16(MZ((b) * K1_ASB + 28 * 1024), voffX, s4, so4 + (ko))
#define ST_S5(ko, b) dma16((b) ? m51 : m50, voffX, s5, so5 + (ko))
#define ST_G1(t, b) dma16(MZ(K1_W + (b) * K1_WBUF), voffW, sG, sg0 + (t) * gst)
#define ST_G2(t, b) dma16(MZ(K1_W + (b) * K1_WBUF + 7168), voffW, sG, soW2 + (t) * WST)
#define ST_U1(t, b) dma16(MZ(K1_W + (b) * K1_WBUF + K1_UOFF), voffW, sU, su0 + (t) * ust)
#define ST_U2(t, b) dma16(MZ(K1_W + (b) * K1_WBUF + K1_UOFF + 7168), voffW, sU, soW2 + (t) * WST)

    ST_X(0, pf ? px : 0u, 0);
    if (!pf) { ST_X(1, 0, 0); ST_X(2, 0, 0); ST_X(3, 0, 0); ST_S4(0, 0); ST_S5(0, 0); ST_G1(0, 0); ST_G2(0, 0); ST_U2(0, 0); }
    ST_U1(0, 0);
    FSTAMP(1);

    const int ch0 = (fq ^ (fr & 6)) * 16;
    LAS char* rA0 = lds + wr * 8192 + fr * 128 + ch0;
    LAS char* rA1 = lds + wr * 8192 + fr * 128 + (ch0 ^ 64);
    LAS char* rS0 = lds + 32768 + wc * 4096 + fr * 128 + ch0;
    LAS char* rS1 = lds + 32768 + wc * 4096 + fr * 128 + (ch0 ^ 64);
    const int trow = (16 * (fq >> 1) + 4 * (fq & 1) + ((lane & 15) >> 2)) * 224;
    LAS char* rW = lds + K1_W + trow + wc * 96 + (lane & 3) * 8;
    LAS char* rM = lds + K1_W + trow + 192 + wc * 16 + (lane & 1) * 8 + ((lane >> 1) & 1) * K1_UOFF;

    f32x4 acc[4][9];
#pragma unroll
    for (int m = 0; m < 4; ++m)
#pragma unroll
        for (int n = 0; n < 9; ++n) { acc[m][n] = (f32x4){0.f, 0.f, 0.f, 0.f}; asm volatile("" : "+v"(acc[m][n])); }
    bf16x8 Af[4][2], Bf[3][2];

#define RD_A(b) do { _Pragma("unroll") for (int m = 0; m < 4; ++m) { Af[m][0] = lds_b128(rA0 + (b) * K1_ASB + m * 2048); Af[m][1] = lds_b128(rA1 + (b) * K1_ASB + m * 2048); } } while (0)
#define RD_S(slot, ri, b) do { Bf[slot][0] = lds_b128(rS0 + (b) * K1_ASB + (ri) * 2048); Bf[slot][1] = lds_b128(rS1 + (b) * K1_ASB + (ri) * 2048); } while (0)
#define RD_W(slot, base, off) do { Bf[slot][0] = lds_tr((base) + (off), (base) + (off) + 1792); Bf[slot][1] = lds_tr((base) + (off) + 7168, (base) + (off) + 7168 + 1792); } while (0)
#define MMA3(n0, n1, n2) do { __builtin_amdgcn_s_setprio(1); \
        _Pragma("unroll") for (int m = 0; m < 4; ++m) { \
            mfma_acc(acc[m][n0], Af[m][0], Bf[0][0]); mfma_acc(acc[m][n0], Af[m][1], Bf[0][1]); mfma_acc(acc[m][n1], Af[m][0], Bf[1][0]); mfma_acc(acc[m][n1], Af[m][1], Bf[1][1]); \
            mfma_acc(acc[m][n2], Af[m][0], Bf[2][0]); mfma_acc(acc[m][n2], Af[m][1], Bf[2][1]); } \
        __builtin_amdgcn_s_setprio(0); } while (0)
#define RD_AM(m, b) do { Af[m][0] = lds_b128(rA0 + (b) * K1_ASB + (m) * 2048); Af[m][1] = lds_b128(rA1 + (b) * K1_ASB + (m) * 2048); } while (0)
#define SEC1(t, b) do { RD_S(0, 0, b); RD_S(1, 1, b); ST_U1((t) + 1, (b) ^ 1); RD_W(2, rW, (b) * K1_WBUF); RD_AM(0, b); ST_U2((t) + 1, (b) ^ 1); RD_AM(1, b); RD_AM(2, b); RD_AM(3, b); } while (0)
#define SEC2(k0, ko, b) do { ST_X(0, k0, b); RD_W(0, rW, (b) * K1_WBUF + 32); ST_X(1, ko, b); RD_W(1, rW, (b) * K1_WBUF + 64); ST_X(2, ko, b); RD_W(2, rM, (b) * K1_WBUF); ST_X(3, ko, b); } while (0)
#define SEC3(t, ko, b) do { ST_G1((t) + 2, b); RD_W(0, rW, (b) * K1_WBUF + K1_UOFF); ST_G2((t) + 2, b); RD_W(1, rW, (b) * K1_WBUF + K1_UOFF + 32); ST_S4(ko, b); \
        RD_W(2, rW, (b) * K1_WBUF + K1_UOFF + 64); ST_S5(ko, b); } while (0)
#define GO(n0, n1, n2) do { WAIT_L0(); BAR(); SCHED(); MMA3(n0, n1, n2); SCHED(); } while (0)

#define RELANE() int tz = threadIdx.x; asm volatile("" : "+v"(tz)); const int er = tz & 15, eq = (tz >> 4) & 3; \
    const int ech = 32 * j + 8 * wc + 4 * (er >> 3) + 16 * ((er >> 2) & 1) + (er & 3); (void)ech; (void)eq
    asm volatile("" : "+v"(rA0), "+v"(rA1), "+v"(rS0), "+v"(rS1), "+v"(rW), "+v"(rM));
    gu32* gtab = (gu32*)(ws + WS_G);
    WAIT_V(10);
    if (pf) WAIT_V(2);
    asm volatile("" : "+v"(xa), "+v"(xb), "+v"(ga), "+v"(gb), "+v"(plr), "+v"(pli), "+v"(pld) :: "memory");
    FSTAMP(2);
    {
        float s = 0.f, s2 = 0.f;
#pragma unroll
        for (int e = 0; e < 4; ++e) { s = dot2(xa[e], ga[e], s); s2 = dot2(xb[e], gb[e], s2); }
        s += s2;
#define DPP_ADD(ctrl, rmask) s += __int_as_float(__builtin_amdgcn_update_dpp(0, __float_as_int(s), ctrl, rmask, 0xf, true))
        DPP_ADD(0x111, 0xf); DPP_ADD(0x112, 0xf); DPP_ADD(0x114, 0xf); DPP_ADD(0x118, 0xf); DPP_ADD(0x142, 0xa); DPP_ADD(0x143, 0xc);
        const float g = rbf(__builtin_amdgcn_rcpf(1.f + __builtin_amdgcn_exp2f(-1.4426950408889634f * rbf(s))));
        if (lane == 63) __hip_atomic_store(gtab + grow, pk2(g, 1.f - g), RLX_AGENT);
    }
    FSTAMP(3);
    if (!pf) { ST_X(0, 128, 1); ST_X(1, 128, 1); ST_X(2, 128, 1); ST_X(3, 128, 1); ST_S4(128, 1); ST_S5(128, 1); ST_G1(1, 1); ST_G2(1, 1); FSTAMP(4); WAIT_V(11); }
    else {
        dma16c(ldsb + K1_HS + 1024, (unsigned)(lane * 16), s4, WS_HR + (bid >> 7) * 1024);
        dma16c(ldsb + K1_HS, (unsigned)(lane * 16), s4, bid == 0 ? WS_HA : 0x80000000u);
        const float lr = bflo(plr), li = bflo(pli), dt = rbf(expf(bflo(pld)));
        const float e = expf(dt * lr); float sn, cs; sincosf(dt * li, &sn, &cs);
        const f32x2 a1 = {e * cs, e * sn};
        const float nr = a1.x - 1.f, den = lr * lr + li * li;
        f32x2 a16 = cmul(a1, a1); a16 = cmul(a16, a16); a16 = cmul(a16, a16); a16 = cmul(a16, a16);
        const f32x2 a32 = cmul(a16, a16), a48 = cmul(a32, a16), a64 = cmul(a32, a32), a128 = cmul(a64, a64), a192 = cmul(a128, a64), a256 = cmul(a128, a128);
        if (lane < 32) {
            LAS f32x4* ct = (LAS f32x4*)(lds + K1_CTAB + lane * 96);
            ct[0] = (f32x4){a1.x, a1.y, 2.f * ((nr * lr + a1.y * li) / den), 2.f * ((a1.y * lr - nr * li) / den)};
            ct[1] = (f32x4){1.f, 0.f, a16.x, a16.y}; ct[2] = (f32x4){a32.x, a32.y, a48.x, a48.y};
            ct[3] = (f32x4){1.f, 0.f, a64.x, a64.y}; ct[4] = (f32x4){a128.x, a128.y, a192.x, a192.y};
            ct[5] = (f32x4){a256.x, a256.y, 0.f, 0.f};
        }
    }
    FSTAMP(5);
    if (wc == 1) BAR();
    STAMP_TAKE(st1);
    LT(1);
    FSTAMP(6);
#pragma nounroll
    for (int t = 0; t < 16; t += 2) {
        asm volatile("" : "+s"(t));
        const unsigned ko = t == 14 ? 0x80000000u : (unsigned)(t + 2) * 128u;
        const unsigned k0 = pf ? px + pxs : ko, k1 = pf ? px + 2 * pxs : ko + 128;
        px += 2 * pxs;
        BAR(); FSTAMP(8 + t); SEC1(t, 0); if (!pf) WAIT_V(10);
        GO(7, 8, 0);
        BAR(); SEC2(k0, ko, 0);
        GO(1, 2, 6);
        BAR(); SEC3(t, ko, 0); if (!pf) WAIT_V(10);
        GO(3, 4, 5);
        BAR(); FSTAMP(9 + t); SEC1(t + 1, 1); if (!pf) WAIT_V(10);
        GO(7, 8, 0);
        BAR(); SEC2(k1, ko + 128, 1);
        GO(1, 2, 6);
        BAR(); SEC3(t + 1, ko + 128, 1);
        if (!pf) WAIT_V(10);
        else {
            unsigned l128 = voffW; int hb = bid; kptr kh = (kptr)__builtin_amdgcn_kernarg_segment_ptr();
            asm volatile("" : "+v"(l128)); asm volatile("" : "+s"(hb), "+s"(kh));
            const unsigned l16 = l128 >> 3, OUT = 0x80000000u;
            const int hi_ = hb >> 3, hj = (hb & 7) * 4 + (hi_ & 3), hp = hi_ >> 2;
            LAS unsigned* hst = (LAS unsigned*)(lds + K1_HS + 2048);
#define RFL(v) ((unsigned)__builtin_amdgcn_readfirstlane(v))
#define HDMA(m, vo, srd, so) asm volatile("s_nop 4\n\ts_mov_b32 m0, %0\n\ts_nop 0\n\tbuffer_load_dwordx4 %1, %2, %3 offen sc1 lds" ::"s"((unsigned)(m)), "v"(vo), "s"(srd), "s"((unsigned)(so)) : "m0", "memory")
#define HLD(op, v, vo) asm volatile("s_nop 4\n\t" op " %0, %1, %2, 0 offen sc1\n\ts_waitcnt vmcnt(0)" : "=v"(v) : "v"(vo), "s"(s4) : "memory")
#define HST32(off, v) __builtin_amdgcn_raw_buffer_store_b32((unsigned)(v), s4, off, 0, 16)
#define HST64(off, lo_, hi2_) do { __builtin_amdgcn_raw_buffer_store_b64((u32x2){(unsigned)(lo_), (unsigned)(hi2_)}, s4, off, 0, 16); asm volatile("" ::: "memory"); } while (0)
#define REPLY(q_, tag_) do { _Pragma("unroll") for (int i = 0; i < 4; ++i) HST64(WS_HR + l16 * 2 + i * 8, (q_)[i], tag_); } while (0)
#define LANE0(off) (l16 == 0 ? (unsigned)(off) : OUT)
#define AT(v) int t##v = t; asm volatile("" : "+s"(t##v)); if (t##v == v)
            if ((t & 2) == 0) {
                WAIT_V(20);
                AT(0) {
                    const unsigned ask = *(LAS unsigned*)(lds + K1_HS + 1024 + (hb & 127) * 8) + 1u;
                    const u32x4 old = *(LAS u32x4*)(lds + K1_HS + l16);
                    HST32(LANE0(WS_HQ + hb * 4), ask);
                    unsigned tag = RFL(old[0]) + 1u;
                    while (__any(old[0] == tag || old[1] == tag || old[2] == tag || old[3] == tag)) ++tag;
                    hst[0] = ask; hst[1] = tag; hst[2] = 0u;
                }
                AT(4) { if (hb == 0) { const u32x4 q = *(LAS u32x4*)(lds + K1_HS + l16); const unsigned tag = hst[1]; REPLY(q, tag); } }
                AT(8) {
                    const u32x2 r = *(LAS u32x2*)(lds + K1_HS + 1024 + (hb & 127) * 8);
                    const bool hit = r[0] == hst[0];
                    HST32(hit ? LANE0(WS_HA + hb * 4) : OUT, r[1]);
                    hst[hit ? 1 : 3] = r[1]; hst[2] = hit;
                }
                AT(12) {
                    u32x4 ans = *(LAS u32x4*)(lds + K1_HS + l16);
                    unsigned tag = hst[1], done = hst[2];
                    if (!__all(done && ans[0] == tag && ans[1] == tag && ans[2] == tag && ans[3] == tag)) {
                        const unsigned ask = RFL(hst[0]); unsigned spins = 0;
                        tag = RFL(tag); done = RFL(done);
#pragma nounroll
                        do {
                            if (hb == 0) { u32x4 q; HLD("buffer_load_dwordx4", q, WS_HQ + l16); REPLY(q, tag); }
                            u32x2 r; HLD("buffer_load_dwordx2", r, WS_HR + hb * 8);
                            if (!done && RFL(r[0]) == ask) { tag = RFL(r[1]); done = 1u; HST32(LANE0(WS_HA + hb * 4), tag); }
                            HLD("buffer_load_dwordx4", ans, WS_HA + l16);
                        } while (!__all(done && ans[0] == tag && ans[1] == tag && ans[2] == tag && ans[3] == tag) && ++spins < (1u << 18));
                    }
                    HDMA(ldsb + K1_GTAB, l16, s4, WS_G + hp * 1024);
                }
            }
            int tq = t; asm volatile("" : "+s"(tq));
            if (tq & 2) {
                AT(2) {
                    ROLE(hb, hi_);
                    HST64((hp < 7 ? WS_CARRY + hp * 8192 + hj * 256 : OUT) + (l16 < 32 * 16 ? l16 >> 1 : OUT >> 1), ~0u, ~0u);
                    HST32(l16 == 4 * 16 ? WS_RDY + (hp * 32 + hj) * 4 : (late ? OUT : WS_FLAG2 + tile * 128) + (l16 < 4 * 16 ? l16 * 2 : OUT >> 1), ~0u);
                    HDMA(ldsb + K1_HS, l16, s4, hb == 0 ? WS_HQ : OUT);
                    px = OUT; pxs = 0;
                }
                AT(6) {
                    const __amdgpu_buffer_rsrc_t sA = SRD((char*)kh[13] + WS_A2, WS_A2_BYTES), sO = SRD(kh[14], SEQ * DM * 4);
                    const unsigned vo = (l128 >> 7) * A2S, vo2 = l128 << 10;
                    HDMA(ldsb + K1_SINK, vo, sA, A2S - 128); HDMA(ldsb + K1_SINK, vo, sA, 65 * A2S - 128); HDMA(ldsb + K1_SINK, vo2, sO, 0u);
                    HDMA(ldsb + K1_HS + 1024, l16, s4, WS_HR + (hb >> 7) * 1024);
                }
                AT(10) {
#pragma unroll
                    for (int i = 0; i < 3; ++i) HDMA(ldsb + K1_SINK, l128, s5, 8192 + i * 8192);
                    HDMA(ldsb + K1_HS, l16, s4, WS_HA);
                }
                AT(14) WAIT_V(20);
            }
        }
        GO(3, 4, 5);
    }
    asm volatile("s_nop 15" ::: "memory");
    if (wc == 0) BAR();
    RELANE();
    STAMP_PUT(0, 0, st0); STAMP_PUT(0, 1, st1);
    STAMP(0, 2);
    LT(2);
    FSTAMP(24);
    kptr ka = (kptr)__builtin_amdgcn_kernarg_segment_ptr(); asm volatile("" : "+s"(ka));
#define KARG(T, i) ((T)ka[i])
    char* const ws2 = KARG(char*, 13);

    LAS char* ctab = lds + K1_CTAB + (wc * 16 + er) * 96;
    LAS char* etab = lds + K1_ETAB + (wc * 16 + er) * 144;
    const f32x4 cst = *(LAS f32x4*)ctab;
    const f32x2 a1 = {cst[0], cst[1]}, cf = {cst[2], cst[3]};
    u32x4 gt[4];
#pragma unroll
    for (int m = 0; m < 4; ++m) gt[m] = *(LAS u32x4*)(lds + K1_GTAB + wr * 256 + eq * 64 + m * 16);
    FSTAMP(25);
    const __amdgpu_buffer_rsrc_t sA2 = SRD(ws2 + WS_A2, WS_A2_BYTES);
#define ST16(voff, v) __builtin_amdgcn_raw_buffer_store_b128(v, sA2, voff, 0, STPOL)
#define PK8(v) (u32x4){pk2(v[0], v[1]), pk2(v[2], v[3]), pk2(v[4], v[5]), pk2(v[6], v[7])}
    const int mblk = pm * 2 + (wr >> 1);
#define IMG_Q(r, q) (unsigned)(((q) >> 1) * 256 + ((q) & 1) * 16 + ((r) & 3) * 32 + mblk * 16384 + (wr & 1) * 1024)
#define IMG_T(r, q) (IMG_Q(r, q) + (unsigned)((((r) >> 2) & 1) * 2048 + ((r) >> 3) * 128 + (j >> 2) * A2S + (j & 3) * 4096))
#define IMG_M(r, q) (IMG_Q(r, q) + (unsigned)((((r) >> 2) & 1) * 2048 + ((r) >> 3) * 512 + (24 + (j >> 2)) * A2S + (j & 3) * 4096))
#define IMG_S(r, q) (IMG_Q(r, q) + (unsigned)((((r) >> 2) & 1) * 4096 + ((r) >> 3) * 2048 + (32 + (j >> 1)) * A2S + (j & 1) * 8192))
    const unsigned lo = IMG_T(er, eq);
#define GLU_HALF(n, hh, o) do { float v[8]; \
        _Pragma("unroll") for (int e = 0; e < 8; ++e) { const int m = 2 * (hh) + (e >> 2), q = e & 3; v[e] = relu(acc[m][n][q]) * (acc[m][3 + (n)][q] * bfhi(gt[m][q])); } \
        o = PK8(v); } while (0)
#define GLU_TILE(n) do { u32x4 o_; GLU_HALF(n, 0, o_); ST16(lo + (wc * 48 + 8 * (n)) * A2S, o_); GLU_HALF(n, 1, o_); ST16(lo + (wc * 48 + 8 * (n)) * A2S + 512, o_); } while (0)
    GLU_TILE(0);
    GLU_TILE(1);
    asm volatile("" ::: "memory");
    {
        f32x2 h = {acc[0][7][0], acc[0][8][0]};
#pragma unroll
        for (int e = 1; e < 16; ++e) h = cfma(a1, h, (f32x2){acc[e >> 2][7][e & 3], acc[e >> 2][8][e & 3]});
        *(LAS f32x2*)(etab + (wr * 4 + eq) * 8) = h;
    }
    WAIT_V(0); WAIT_L0();
    FSTAMP(26);
    BAR(); asm volatile("" ::: "memory");
    gu32* rdy = (gu32*)(ws2 + WS_RDY);
    if (tz == 192) __hip_atomic_store(rdy + pm * 32 + j, 1u, RLX_AGENT);
    gu64* carry = (gu64*)(ws2 + WS_CARRY);
#define CHAN(c, r) (32 * j + 8 * (c) + 4 * ((r) >> 3) + 16 * (((r) >> 2) & 1) + ((r) & 3))
    if (wid < 2 && pm < 7) {
        const f32x2 a16 = *(LAS f32x2*)(lds + K1_CTAB + (wid * 16 + er) * 96 + 24);
        f32x4 E[8];
#pragma unroll
        for (int i = 0; i < 8; ++i) E[i] = *(LAS f32x4*)(lds + K1_ETAB + (wid * 16 + er) * 144 + i * 16);
        f32x2 run = {E[0][0], E[0][1]};
#pragma unroll
        for (int i = 1; i < 16; ++i) run = cfma(a16, run, (f32x2){E[i >> 1][(i & 1) * 2], E[i >> 1][(i & 1) * 2 + 1]});
        unsigned ux = __float_as_uint(run.x), uy = __float_as_uint(run.y);
        if (ux == 0xFFFFFFFFu) ux = 0x7FC00000u;
        if (eq == 0) __hip_atomic_store(carry + pm * 1024 + CHAN(wid, er), ((unsigned long long)uy << 32) | ux, RLX_AGENT);
    }
    STAMP(0, 3);
    FSTAMP(27);
    GLU_TILE(2);
    {
        float v[8];
        const bool lowh = er < 8;
#pragma unroll
        for (int e = 0; e < 8; ++e) {
            const int m = e >> 2, q = e & 3;
            const float v0 = lowh ? relu(acc[m][6][q]) : acc[m][6][q] * bfhi(gt[m][q]);
            const float v1 = lowh ? relu(acc[2 + m][6][q]) : acc[2 + m][6][q] * bfhi(gt[2 + m][q]);
            const float rcv = __int_as_float(__builtin_amdgcn_mov_dpp(__float_as_int(lowh ? v1 : v0), 0x128, 0xf, 0xf, true));
            v[e] = (lowh ? v0 : v1) * rcv;
        }
        ST16(IMG_M(er, eq) + wc * 128, PK8(v));
    }
    LAS f32x4* park = (LAS f32x4*)(lds + 3 * 32768 + wr * 8192 + (tz & 63) * 16);
    if (wc == 1) {
#pragma unroll
        for (int m = 0; m < 4; ++m) { park[m * 128] = acc[m][7]; park[m * 128 + 64] = acc[m][8]; }
    }
    STAMP(0, 4);
    WAIT_L0();
    FSTAMP(28);
    BAR(); asm volatile("" ::: "memory");
    FSTAMP(29);

    ROLE(bid, idx);
    const int n0 = nblk * 128;
    const int rs0 = late ? (48 + SHIFT) % 5 : (40 - SHIFT) % 5;

    if (wc == 1) {
        int ty = threadIdx.x; asm volatile("" : "+v"(ty) :: "memory");
        const int ln = ty & 63, e3 = ln >> 3;
#define LEVEL(lvl) do { if (ty == 256) __hip_atomic_store(rdy + pm * 32 + j, (unsigned)(lvl), RLX_AGENT); } while (0)
        const __amdgpu_buffer_rsrc_t sC = SRD(late ? KARG(const u16*, 10) : KARG(const u16*, 9), DM * NP * 2), sW = SRD(KARG(const u16*, 11), FF * DM * 2);
        const unsigned voffA = (unsigned)(ln * 16);
        const unsigned voffC = (unsigned)(e3 * 2048 + (((ln & 7) ^ (e3 & 6)) * 16));
        const unsigned voffD = (unsigned)(((e3 >> 2) * 8 + (e3 & 3)) * 2048 + ((((ln >> 1) & 3) ^ (e3 >> 1)) * 32) + (ln & 1) * 16);
        const unsigned soA = (unsigned)((late ? 0 : 48) * A2S + mb2 * 16384 + wr * 4096);
        const unsigned soC = (unsigned)((n0 + 32 * wr) * 2048);
        const unsigned soD = (unsigned)(((late ? 0 : 48) + 112 * wr) * 2048 + n0 * 2);
        const unsigned mB = ldsb + wr * 4096;
#define DMA_A(i, k, sl) dma16c(mB + (sl) + (i) * 1024, voffA, sA2, soA + (k) * A2S + (i) * 1024)
#define DMA_C(srd, i, p, sl) dma16(mB + (sl) + 16384 + (i) * 1024, voffC, srd, soC + (p) * 128 + (i) * (8 * 2048))
#define DMA_D(i, row, sl) dma16(mB + (sl) + 16384 + (i) * 1024, voffD, sW, soD + (row) * 2048 + ((i) >> 1) * (4 * 2048) + ((i) & 1) * 128)
#define POLL(v, p) asm volatile("global_load_dword %0, %1, off sc1\n\ts_waitcnt vmcnt(0)" : "=v"(v) : "v"(p) : "memory")
#define ASK(v, p) asm volatile("global_load_dword %0, %1, off sc1" : "=v"(v) : "v"(p) : "memory")
#define NEED(v, p, lvl) do { asm volatile("" : "+v"(v) :: "memory"); if (!__all((int)(v) >= (lvl))) { unsigned spins_ = 0, w_; \
            do { __builtin_amdgcn_s_sleep(1); POLL(w_, p); } while (!__all((int)w_ >= (lvl)) && ++spins_ < (1u << 18)); } } while (0)
        gu32* rl = rdy + (mb2 >> 1) * 32 + (ln & 31);
#define XP(k_) (((((k_) & 3) << 1) | (((k_) >> 2) & 1)) ^ (xc & 1))
#pragma unroll
        for (int p = 0; p < 3; ++p)
#pragma unroll
            for (int i = 0; i < 4; ++i) DMA_D(i, 448 * XP(p), p * 32768);
        unsigned fv;
        {
            const int other = ((ln >> 2) ^ xc) & 1;
            unsigned spins = 0;
            do POLL(fv, rl); while (!__all((int)fv >= 1 || (GATE2 && other)) && ++spins < (1u << 18));
        }
        FSTAMP(31);
#pragma unroll
        for (int p = 0; p < 3; ++p)
#pragma unroll
            for (int i = 0; i < 4; ++i) DMA_A(i, XP(p), p * 32768);
        WAIT_V(8); FSTAMP(32); BAR(); FSTAMP(33); LT(3);
        int k = 3, sl = 3 * 32768;
#define A4(img, sl) dma4c(mB + (sl), voffA, sA2, soA + (img) * A2S)
#define D4(row, sl) do { const unsigned b_ = soD + (row) * 2048; \
            asm volatile("s_mov_b32 m0, %0\n\ts_nop 0\n\tbuffer_load_dwordx4 %1, %2, %3 offen" BPOL " lds\n\tbuffer_load_dwordx4 %1, %2, %4 offen offset:2048" BPOL " lds\n\t" \
                         "buffer_load_dwordx4 %1, %2, %5 offen offset:3072" BPOL " lds\n\ts_mov_b32 m0, %6\n\ts_nop 0\n\tbuffer_load_dwordx4 %1, %2, %3 offen offset:128" BPOL " lds" \
                         ::"s"(mB + (sl) + 16384), "v"(voffD), "s"(sW), "s"(b_), "s"(b_ + 8192 - 2048), "s"(b_ + 8192 + 128 - 3072), "s"(mB + (sl) + 16384 + 1024 - 128) : "m0", "memory"); } while (0)
#define C4(srd, p, sl) do { const unsigned b_ = soC + (p) * 128; dma4(mB + (sl) + 16384, voffC, srd, b_, b_ + 16384 - 1024, b_ + 32768 - 2048, b_ + 49152 - 3072); } while (0)
#define STAGE_W() do { const int q_ = k < 16 ? (k & 3) | ((k & 4) << 1) | ((k & 8) >> 1) : k, x_ = XP(q_), row_ = 448 * x_ + ((q_ >> 3) == 3 ? 96 : 16 * (q_ >> 3)); \
            A4((q_ & ~7) + x_, sl); D4(row_, sl); ++k; sl = sl == 4 * 32768 ? 0 : sl + 32768; } while (0)
#define LSYNC() do { WAIT_V(16); BAR(); } while (0)
        STAGE_W(); WAIT_V(12); BAR();
        STAGE_W(); LSYNC(); STAGE_W(); LSYNC();
        ASK(fv, rl);
        STAGE_W(); LSYNC(); STAGE_W(); LSYNC();
        NEED(fv, rl, 1);
        STAGE_W(); LSYNC(); STAGE_W(); LSYNC();
        LEVEL(2);
#pragma nounroll
        while (k < 13) { STAGE_W(); LSYNC(); }
        ASK(fv, rl);
        STAGE_W(); LSYNC(); STAGE_W(); LSYNC(); STAGE_W(); LSYNC();
        NEED(fv, rl, 2);
        const int nw = late ? 32 : 24;
#pragma nounroll
        while (k < nw) { STAGE_W(); LSYNC(); }
        const int np = late ? 16 : 16 - SHIFT;
#pragma nounroll
        for (int p = 0; p < np; ++p) {
            A4(k, sl); C4(sC, p, sl);
            ++k; sl = sl == 4 * 32768 ? 0 : sl + 32768;
            LSYNC();
        }
        if (SHIFT && late) {
            const __amdgpu_buffer_rsrc_t sR = SRD(KARG(const u16*, 9), DM * NP * 2);
#pragma nounroll
            for (int p = 16 - SHIFT; p < 16; ++p) {
                A4(72 + p, sl); C4(sR, p, sl);
                sl = sl == 4 * 32768 ? 0 : sl + 32768;
                LSYNC();
            }
        }
        const int tb = mb2 * 128 + (wr >> 1) * 64 + (wr & 1) * 8 + (ln >> 5) * 16, dcol = n0 + (ln & 31) * 4;
#define ROW(i) (tb + ((i) >> 3) * 32 + ((i) & 7))
        LT(4);
        FSTAMP(40);
        gu32* flag = (gu32*)(ws2 + WS_FLAG2 + tile * 128 + wr * 32);
        const __amdgpu_buffer_rsrc_t sP = SRD(KARG(float*, 14), SEQ * DM * 4);
#define POFF(i) ((ROW(i) * DM + dcol) * 4)
        const int wn1 = (ln >> 4) & 1, s0 = rs0 + wn1, s1 = rs0 + 2 + wn1;
        const int inp = wr * 8192 + ((ln >> 2) & 3) * 1024 + (ln & 3) * 256, sw = (ln & 15) ^ ((ln >> 5) * 8);
        LAS char* q0 = lds + (s0 >= 5 ? s0 - 5 : s0) * 32768 + inp;
        LAS char* q1 = lds + (s1 >= 5 ? s1 - 5 : s1) * 32768 + inp;
#define SUMS(i) (*(LAS f32x4*)(q0 + ((i) >> 3) * 4096 + ((sw ^ ((i) & 7)) * 16)) + *(LAS f32x4*)(q1 + ((i) >> 3) * 4096 + ((sw ^ ((i) & 7)) * 16)))
        f32x4 ex[16];
        if (late) {
            const __amdgpu_buffer_rsrc_t sO = SRD(KARG(float*, 14), SEQ * DM * 4);
            WAIT_V(8); BAR();
            FSTAMP(41);
            ASK(fv, flag);
            WAIT_V(0); BAR();
            FSTAMP(42);
            NEED(fv, flag, 1);
            FSTAMP(43);
#pragma unroll
            for (int i = 0; i < 16; ++i) ex[i] = __builtin_bit_cast(f32x4, __builtin_amdgcn_raw_buffer_load_b128(sP, POFF(i), 0, 16));
            FSTAMP(44);
            BAR(); asm volatile("" ::: "memory");
            FSTAMP(45);
            STAMP(1, 3);
#pragma unroll
            for (int i = 0; i < 16; ++i) __builtin_amdgcn_raw_buffer_store_b128(__builtin_bit_cast(u32x4, ex[i] + SUMS(i)), sO, (ROW(i) * DM + dcol) * 4, 0, OUTPOL);
        } else {
            const __amdgpu_buffer_rsrc_t sXk = SRD(KARG(const u16*, 0), SEQ * DM * 2), sDk = SRD(KARG(const u16*, 12), DM * 2), sGk = SRD(ws2 + WS_G, 8192);
            WAIT_V(8); BAR();
            FSTAMP(41);
            u32x2 dk = __builtin_amdgcn_raw_buffer_load_b64(sDk, dcol * 2, 0, 0), xv[16]; u32x4 gwd[4];
#pragma unroll
            for (int i = 0; i < 4; ++i) gwd[i] = __builtin_amdgcn_raw_buffer_load_b128(sGk, ROW(4 * i) * 4, 0, 16);
#pragma unroll
            for (int i = 0; i < 16; ++i) xv[i] = __builtin_amdgcn_raw_buffer_load_b64(sXk, (tb * DM + dcol) * 2 + (i & 1) * 2048, (((i) >> 3) * 32 + ((i) & 6)) * 2048, 0);
            WAIT_V(21); BAR();
            FSTAMP(42);
            asm volatile("" : "+v"(dk), "+v"(gwd[0]), "+v"(gwd[1]), "+v"(gwd[2]), "+v"(gwd[3]));
#pragma unroll
            for (int i = 0; i < 16; ++i) {
                const float g = bflo(gwd[i >> 2][i & 3]);
                ex[i] = (f32x4){g * rbf(bflo(xv[i][0]) * bflo(dk[0])), g * rbf(bfhi(xv[i][0]) * bfhi(dk[0])),
                                g * rbf(bflo(xv[i][1]) * bflo(dk[1])), g * rbf(bfhi(xv[i][1]) * bfhi(dk[1]))};
                asm volatile("" : "+v"(ex[i]));
            }
            FSTAMP(44);
            BAR(); asm volatile("" ::: "memory");
            FSTAMP(45);
            STAMP(1, 3);
#pragma unroll
            for (int i = 0; i < 16; ++i) __builtin_amdgcn_raw_buffer_store_b128(__builtin_bit_cast(u32x4, ex[i] + SUMS(i)), sP, POFF(i), 0, 16);
            FSTAMP(46);
            WAIT_V(0);
            if (ln == 0) __hip_atomic_store(flag, 1u, RLX_AGENT);
        }
        STAMP(1, 6);
        STAMP(1, 7);
        FSTAMP(47);
        FSTAMP(48);
        FDUMP();
        asm volatile(".Lk1_end_a:");
        return;
    }

    {
        unsigned long long gq[2][7];
#pragma unroll
        for (int c = 0; c < 2; ++c)
#pragma unroll
            for (int q = 0; q < 7; ++q) gq[c][q] = __hip_atomic_load(carry + (q + pm >= 7 ? q + pm - 7 : 0) * 1024 + CHAN(c, er), RLX_AGENT);
        f32x2 h[2];
        {
            f32x2 a16[2], run[2]; f32x4 E[2][6], F0[2]; f32x2 F1[2];
#pragma unroll
            for (int c = 0; c < 2; ++c) {
                a16[c] = *(LAS f32x2*)(ctab + c * (16 * 96) + 24);
#pragma unroll
                for (int i = 0; i < 6; ++i) E[c][i] = *(LAS f32x4*)(etab + c * (16 * 144) + i * 16);
                F0[c] = *(LAS f32x4*)(etab + c * (16 * 144) + wr * 32); F1[c] = *(LAS f32x2*)(etab + c * (16 * 144) + wr * 32 + 16);
                run[c] = (f32x2){0.f, 0.f};
            }
#pragma unroll
            for (int w = 0; w < 3; ++w)
                if (wr > w) {
#pragma unroll
                    for (int i = 0; i < 4; ++i)
#pragma unroll
                        for (int c = 0; c < 2; ++c) run[c] = cfma(a16[c], run[c], (f32x2){E[c][2 * w + (i >> 1)][(i & 1) * 2], E[c][2 * w + (i >> 1)][(i & 1) * 2 + 1]});
                }
#pragma unroll
            for (int c = 0; c < 2; ++c) {
                const f32x2 r1 = cfma(a16[c], run[c], (f32x2){F0[c][0], F0[c][1]}), r2 = cfma(a16[c], r1, (f32x2){F0[c][2], F0[c][3]}), r3 = cfma(a16[c], r2, F1[c]);
                h[c] = sel4(eq, run[c], r1, r2, r3);
            }
        }
        f32x4 ur[4], ui[4];
#pragma unroll
        for (int m = 0; m < 4; ++m) { ur[m] = park[m * 128]; ui[m] = park[m * 128 + 64]; }
        const f32x4 cs1 = *(LAS f32x4*)(ctab + 16 * 96);
        FSTAMP(30);
        f32x2 a256[2], aq[2];
#pragma unroll
        for (int c = 0; c < 2; ++c) {
            LAS char* ct = ctab + c * (16 * 96);
            a256[c] = *(LAS f32x2*)(ct + 80); aq[c] = cmul(*(LAS f32x2*)(ct + 16 + eq * 8), *(LAS f32x2*)(ct + 48 + wr * 8));
        }
        unsigned mx = 0;
#pragma unroll
        for (int q = 0; q < 7; ++q) mx = max(mx, max((unsigned)gq[0][q], (unsigned)gq[1][q]));
        if (pm > 0 && __any(mx == 0xFFFFFFFFu)) {
            unsigned spins = 0;
            do {
                mx = 0;
#pragma unroll
                for (int c = 0; c < 2; ++c)
#pragma unroll
                    for (int q = 0; q < 7; ++q) {
                        gq[c][q] = __hip_atomic_load(carry + (q + pm >= 7 ? q + pm - 7 : 0) * 1024 + CHAN(c, er), RLX_AGENT);
                        mx = max(mx, (unsigned)gq[c][q]);
                    }
            } while (__any(mx == 0xFFFFFFFFu) && ++spins < (1u << 18));
        }
        f32x2 cg[2] = {{0.f, 0.f}, {0.f, 0.f}};
#pragma unroll
        for (int q = 0; q < 7; ++q) {
            const unsigned keep = q + pm >= 7 ? 0xFFFFFFFFu : 0u;
#pragma unroll
            for (int c = 0; c < 2; ++c) cg[c] = cfma(a256[c], cg[c], (f32x2){__uint_as_float((unsigned)gq[c][q] & keep), __uint_as_float((unsigned)(gq[c][q] >> 32) & keep)});
        }
        h[0] = cfma(aq[0], cg[0], h[0]); h[1] = cfma(aq[1], cg[1], h[1]);
        STAMP(0, 5);
        FSTAMP(31);
        const unsigned ls = IMG_S(er, eq);
        const f32x2 b1 = {cs1[0], cs1[1]}, bf = {cs1[2], cs1[3]};
#pragma unroll
        for (int hh = 0; hh < 2; ++hh) {
            float vr[8], vi[8], wr8[8], wi8[8];
#pragma unroll
            for (int e = 0; e < 8; ++e) {
                const int m = 2 * hh + (e >> 2), q = e & 3;
                h[0] = cfma(a1, h[0], (f32x2){acc[m][7][q], acc[m][8][q]});
                h[1] = cfma(b1, h[1], (f32x2){ur[m][q], ui[m][q]});
                const float g2 = bflo(gt[m][q]);
                const f32x2 s0 = cmul(cf, h[0]), s1 = cmul(bf, h[1]);
                vr[e] = s0.x * g2; vi[e] = -s0.y * g2; wr8[e] = s1.x * g2; wi8[e] = -s1.y * g2;
            }
            ST16(ls + 40 * A2S + hh * 512, PK8(vr)); ST16(ls + 40 * A2S + hh * 512 + 128, PK8(wr8));
            ST16(ls + hh * 512, PK8(vi)); ST16(ls + hh * 512 + 128, PK8(wi8));
        }
    }
    STAMP(0, 6);
    int ty = threadIdx.x; asm volatile("" : "+v"(ty) :: "memory");
    const int ln = ty & 63, fr2 = ty & 15, fq2 = (ty >> 4) & 3;
    const int wn = wid & 1, kk = wid >> 1;
    LAS char* rH = lds + kk * 8192 + (ln >> 5) * 4096 + (ln & 31) * 8;
    LAS char* rC = lds + 16384 + (wn * 64 + fr2) * 128 + (((4 * kk + fq2) ^ (fr2 & 6)) * 16);
    LAS char* rWb = lds + 16384 + kk * 8192 + (ln >> 5) * 4096 + wn * 1024 + ((ln >> 2) & 7) * 128 + (ln & 3) * 8;
    const int rsw = (ln >> 3) & 3;
    f32x4 ac2[4][8];
#pragma unroll
    for (int n = 0; n < 4; ++n)
#pragma unroll
        for (int m = 0; m < 8; ++m) { ac2[n][m] = (f32x4){0.f, 0.f, 0.f, 0.f}; asm volatile("" : "+v"(ac2[n][m])); }
    STAMP(0, 7);
    FSTAMP(32);
    FDUMP();
    s16x4 HX[8][2], HY[8][2], WX[4][2], WY[4][2];
    int sl = 0;

#define RD1(RD, i, H, W) do { \
        if ((i) < 8) { \
            if (RD == 1) W[(i) >> 1][(i) & 1] = tr4(rWb + sl + ((((i) >> 1) ^ rsw) * 32) + ((i) & 1) * 2048); \
            else if (((i) & 1) == 0) { const s16x8 v_ = *(LAS s16x8*)(rC + sl + ((i) >> 1) * 2048); \
                W[(i) >> 1][0] = __builtin_shufflevector(v_, v_, 0, 1, 2, 3); W[(i) >> 1][1] = __builtin_shufflevector(v_, v_, 4, 5, 6, 7); } \
        } else H[((i) - 8) >> 1][(i) & 1] = tr4(rH + sl + (((i) - 8) >> 1) * 256 + ((i) & 1) * 2048); \
    } while (0)
#define LGKM0() __builtin_amdgcn_s_waitcnt(0xC07F)
#define MM(i, HC, WC) mfma_acc(ac2[(i) & 3][(i) >> 2], cat8(WC[(i) & 3][0], WC[(i) & 3][1]), cat8(HC[(i) >> 2][0], HC[(i) >> 2][1]))
#define STEP(RD, HC, WC, HN, WN) do { \
        _Pragma("unroll") for (int i = 0; i < 32; ++i) { MM(i, HC, WC); if (i < 24) RD1(RD, i, HN, WN); } \
        sl = sl == 4 * 32768 ? 0 : sl + 32768; LGKM0(); \
    } while (0)

    LGKM0();
    BAR();
    STAMP_TAKE(st9);
#pragma unroll
    for (int i = 0; i < 24; ++i) RD1(1, i, HX, WX);
    sl = 32768; LGKM0();
    const int nf = late ? 15 : 11;
#pragma nounroll
    for (int i = 0; i < nf; ++i) {
        if (i == 3) WAIT_V(0);
        BAR(); STEP(1, HX, WX, HY, WY); BAR(); STEP(1, HY, WY, HX, WX);
    }
    BAR(); STEP(1, HX, WX, HY, WY); BAR(); STEP(0, HY, WY, HX, WX);
#pragma nounroll
    for (int i = late ? -SHIFT / 2 : SHIFT / 2; i < 7; ++i) { BAR(); STEP(0, HX, WX, HY, WY); BAR(); STEP(0, HY, WY, HX, WX); }
    BAR(); STEP(0, HX, WX, HY, WY);
    {
        const int sw = rs0 + wid;
        LAS char* dmp = lds + (sw >= 5 ? sw - 5 : sw) * 32768 + fq2 * 256;
#define DUMP(i) *(LAS f32x4*)(dmp + (i) * 1024 + ((fr2 ^ (4 * ((i) & 3) + fq2)) * 16)) = ac2[(i) & 3][(i) >> 2]
#pragma unroll
        for (int i = 0; i < 32; ++i) { MM(i, HY, WY); if (i >= 12) DUMP(i - 12); }
        asm volatile("s_nop 15" ::: "memory");
#pragma unroll
        for (int i = 20; i < 32; ++i) DUMP(i);
    }
    WAIT_L0();
    STAMP_PUT(1, 0, st0); STAMP_PUT(1, 1, st9);
    STAMP(1, 2);
    BAR();
    asm volatile(".Lk1_end_b:");
}

extern "C" void kernel_launch(void* const* d_in, const int* in_sizes, int n_in, void* d_out, int out_size, void* d_ws, size_t ws_size,
                              hipStream_t stream) {
    const u16* x = (const u16*)d_in[0];
    const u16* lre = (const u16*)d_in[1];
    const u16* lim = (const u16*)d_in[2];
    const u16* Bre = (const u16*)d_in[3];
    const u16* Bim = (const u16*)d_in[4];
    const u16* Cre = (const u16*)d_in[5];
    const u16* Cim = (const u16*)d_in[6];
    const u16* logdt = (const u16*)d_in[7];
    const u16* dskip = (const u16*)d_in[8];
    const u16* Wg = (const u16*)d_in[10];
    const u16* Wu = (const u16*)d_in[11];
    const u16* Wd = (const u16*)d_in[12];
    const u16* gw = (const u16*)d_in[13];
    char* ws = (char*)d_ws;
    (void)hipFuncSetAttribute((const void*)k1_fused, hipFuncAttributeMaxDynamicSharedMemorySize, K2_LDS);
    k1_fused<<<256, 512, K2_LDS, stream>>>(x, Wg, Wu, Bre, Bim, lre, lim, logdt, gw, Cre, Cim, Wd, dskip, ws, (float*)d_out);
}
```

```cpp
#include <hip/hip_runtime.h>
#include <hip/hip_bf16.h>

typedef unsigned short u16;
typedef __bf16 bf16x8 __attribute__((ext_vector_type(8)));
typedef __bf16 bf16x2 __attribute__((ext_vector_type(2)));
typedef short s16x4 __attribute__((ext_vector_type(4)));
typedef short s16x8 __attribute__((ext_vector_type(8)));
typedef float f32x4 __attribute__((ext_vector_type(4)));
typedef float f32x2 __attribute__((ext_vector_type(2)));
typedef unsigned u32x4 __attribute__((ext_vector_type(4)));
typedef unsigned u32x2 __attribute__((ext_vector_type(2)));
#define LAS __attribute__((address_space(3)))
#define GAS __attribute__((address_space(1)))
typedef GAS unsigned gu32;
typedef GAS unsigned long long gu64;
typedef __attribute__((address_space(4))) const unsigned long long* kptr;
#define RLX_AGENT __ATOMIC_RELAXED, __HIP_MEMORY_SCOPE_AGENT

constexpr int SEQ = 2048, DM = 1024, NP = 1024, FF = 3584;
constexpr int KSTEPS2 = (2 * NP + FF) / 64;

constexpr unsigned WS_CARRY = 0;
constexpr unsigned WS_G = 7 * 8192;
constexpr unsigned WS_RDY = 65536;
constexpr unsigned WS_FLAG2 = WS_RDY + 1024;
constexpr unsigned WS_HQ = WS_FLAG2 + 16384;
constexpr unsigned WS_HA = WS_HQ + 1024;
constexpr unsigned WS_HR = WS_HA + 1024;
constexpr unsigned WS_SYNC_BYTES = WS_HR + 2048;
constexpr size_t WS_A2 = 1 << 20;
constexpr unsigned A2S = 262144 + 128;
constexpr size_t WS_A2_BYTES = (size_t)(KSTEPS2 + 32) * A2S;

__device__ __forceinline__ float bflo(unsigned v) { return __uint_as_float(v << 16); }
__device__ __forceinline__ float bfhi(unsigned v) { return __uint_as_float(v & 0xffff0000u); }
__device__ __forceinline__ float rbf(float f) { return (float)(__bf16)f; }
__device__ __forceinline__ unsigned pk2(float a, float b) {
    bf16x2 v; v.x = (__bf16)a; v.y = (__bf16)b; return __builtin_bit_cast(unsigned, v);
}
__device__ __forceinline__ float relu(float v) { return __int_as_float(max(__float_as_int(v), 0)); }
__device__ __forceinline__ float dot2(unsigned a, unsigned b, float c) {
    return __builtin_amdgcn_fdot2_f32_bf16(__builtin_bit_cast(bf16x2, a), __builtin_bit_cast(bf16x2, b), c, false);
}

constexpr size_t WS_DBG = 200u << 20;
#define STAMP(kid, i)
#define STAMP_TAKE(v)
#define STAMP_PUT(kid, i, v)

#define FSTAMP(i)
#define FDUMP()
#define WAIT_V(n) asm volatile("s_waitcnt vmcnt(" #n ")" ::: "memory")
#define WAIT_L0() asm volatile("s_waitcnt lgkmcnt(0)" ::: "memory")
#define BAR() __builtin_amdgcn_s_barrier()
#define SCHED() __builtin_amdgcn_sched_barrier(0)
#define SRD(p, bytes) __builtin_amdgcn_make_buffer_rsrc((void*)(p), (short)0, (int)(bytes), 0x00020000)

__device__ __forceinline__ void dma16(unsigned m0v, unsigned voff, __amdgpu_buffer_rsrc_t srd, unsigned soff) {
    asm volatile("s_mov_b32 m0, %0\n\ts_nop 0\n\tbuffer_load_dwordx4 %1, %2, %3 offen lds" ::"s"(m0v), "v"(voff), "s"(srd), "s"(soff) : "m0", "memory");
}
__device__ __forceinline__ bf16x8 lds_b128(LAS char* p) { return __builtin_bit_cast(bf16x8, *(LAS s16x8*)p); }
__device__ __forceinline__ bf16x8 lds_tr(LAS char* p0, LAS char* p1) {
    s16x4 a = __builtin_amdgcn_ds_read_tr16_b64_v4i16((LAS s16x4*)p0);
    s16x4 b = __builtin_amdgcn_ds_read_tr16_b64_v4i16((LAS s16x4*)p1);
    return __builtin_bit_cast(bf16x8, __builtin_shufflevector(a, b, 0, 1, 2, 3, 4, 5, 6, 7));
}
#define MFMA(a, b, c) __builtin_amdgcn_mfma_f32_16x16x32_bf16(a, b, c, 0, 0, 0)
__device__ __forceinline__ f32x2 cmul(f32x2 a, f32x2 b) { return (f32x2){__builtin_fmaf(a.x, b.x, -(a.y * b.y)), __builtin_fmaf(a.x, b.y, a.y * b.x)}; }
__device__ __forceinline__ f32x2 cfma(f32x2 a, f32x2 b, f32x2 c) {
    return (f32x2){__builtin_fmaf(a.x, b.x, __builtin_fmaf(-a.y, b.y, c.x)), __builtin_fmaf(a.x, b.y, __builtin_fmaf(a.y, b.x, c.y))};
}
__device__ __forceinline__ f32x2 sel4(int i, f32x2 a, f32x2 b, f32x2 c, f32x2 d) {
    const bool o = i & 1, u = i & 2;
    const float lx = o ? b.x : a.x, ly = o ? b.y : a.y, hx = o ? d.x : c.x, hy = o ? d.y : c.y;
    return (f32x2){u ? hx : lx, u ? hy : ly};
}

#define STPOL 16
#define PF_W0 1
#define PF_D0 4
#define OUTPOL 16

#define K2_SPLIT 40
#define MAP 0
#define ABL_NOLOAD 0
#define ABL_NOREAD 0
#define ABL_NOMFMA 0
#define CODE_T 10
#define ILV 1
#define SHIFT 2
#define GATE2 1
#define PRO 1
#define MORD 3
#define DEEP 0
#define THIN 1
#define M04 1
#define LATE_FLIP 0
#define DIST 5
constexpr int K2_LDS = 5 * 32768;

__device__ __forceinline__ void dma16c(unsigned m0v, unsigned voff, __amdgpu_buffer_rsrc_t srd, unsigned soff) {
    asm volatile("s_mov_b32 m0, %0\n\ts_nop 0\n\tbuffer_load_dwordx4 %1, %2, %3 offen sc1 lds" ::"s"(m0v), "v"(voff), "s"(srd), "s"(soff) : "m0", "memory");
}
#define BPOL ""
#define APOL " sc1"
__device__ __forceinline__ void dma4c(unsigned m0v, unsigned voff, __amdgpu_buffer_rsrc_t srd, unsigned soff) {
    asm volatile("s_mov_b32 m0, %0\n\ts_nop 0\n\tbuffer_load_dwordx4 %1, %2, %3 offen" APOL " lds\n\tbuffer_load_dwordx4 %1, %2, %3 offen offset:1024" APOL " lds\n\t"
                 "buffer_load_dwordx4 %1, %2, %3 offen offset:2048" APOL " lds\n\tbuffer_load_dwordx4 %1, %2, %3 offen offset:3072" APOL " lds" ::"s"(m0v), "v"(voff), "s"(srd), "s"(soff) : "m0", "memory");
}
__device__ __forceinline__ void dma4(unsigned m0v, unsigned voff, __amdgpu_buffer_rsrc_t srd, unsigned s0, unsigned s1, unsigned s2, unsigned s3) {
    asm volatile("s_mov_b32 m0, %0\n\ts_nop 0\n\tbuffer_load_dwordx4 %1, %2, %3 offen lds\n\tbuffer_load_dwordx4 %1, %2, %4 offen offset:1024 lds\n\t"
                 "buffer_load_dwordx4 %1, %2, %5 offen offset:2048 lds\n\tbuffer_load_dwordx4 %1, %2, %6 offen offset:3072 lds" ::"s"(m0v), "v"(voff), "s"(srd), "s"(s0), "s"(s1), "s"(s2), "s"(s3) : "m0", "memory");
}
__device__ __forceinline__ void mfma_acc(f32x4& c, bf16x8 a, bf16x8 b) {
    asm volatile("v_mfma_f32_16x16x32_bf16 %0, %1, %2, %0" : "+v"(c) : "v"(a), "v"(b) : "memory");
}
__device__ __forceinline__ s16x4 tr4(LAS char* p) { return __builtin_amdgcn_ds_read_tr16_b64_v4i16((LAS s16x4*)p); }
__device__ __forceinline__ bf16x8 cat8(s16x4 a, s16x4 b) { return __builtin_bit_cast(bf16x8, __builtin_shufflevector(a, b, 0, 1, 2, 3, 4, 5, 6, 7)); }

constexpr int K1_ASB = 40960;
constexpr int K1_W = 81920;
constexpr int K1_WBUF = 28704, K1_UOFF = 14352;
constexpr int K1_ETAB = K1_W + 2 * K1_WBUF;
constexpr int K1_CTAB = K1_ETAB + 32 * 144;
constexpr int K1_SINK = K1_CTAB + 32 * 96;
constexpr int K1_GTAB = K1_SINK + 1024;
constexpr int K1_HS = K1_GTAB + 1024;

__global__ __launch_bounds__(512, 2) void k1_fused(const u16* __restrict__ x, const u16* __restrict__ Wg, const u16* __restrict__ Wu,
                                                       const u16* __restrict__ Bre, const u16* __restrict__ Bim, const u16* __restrict__ lre,
                                                       const u16* __restrict__ lim, const u16* __restrict__ logdt, const u16* __restrict__ gw,
                                                       const u16* __restrict__ Cre, const u16* __restrict__ Cim, const u16* __restrict__ Wd,
                                                       const u16* __restrict__ dskip, char* __restrict__ ws, float* __restrict__ out) {
    extern __shared__ __attribute__((aligned(16))) char smem[];
    LAS char* lds = (LAS char*)smem;
    const unsigned ldsb = (unsigned)(size_t)lds;
    const int tid = threadIdx.x, lane = tid & 63;
    const int wid = __builtin_amdgcn_readfirstlane(tid >> 6);
    const int wr = wid & 3, wc = wid >> 2;
    const int bid = blockIdx.x, idx = bid >> 3, j = (bid & 7) * 4 + (idx & 3), pm = idx >> 2;
    const int fr = lane & 15, fq = lane >> 4;
#define ROLE(b_, i_) const int xc = (b_) & 7, late = (xc & 1) ^ LATE_FLIP, mb2 = (xc >> 2) * 8 + ((i_) >> 2), nblk = ((xc >> 1) & 1) * 4 + ((i_) & 3), tile = mb2 * 8 + nblk
    STAMP_TAKE(st0);
#define LT(n)
    FSTAMP(0);

    const char* code0; unsigned codeA, codeB;
    asm volatile("s_getpc_b64 %0\n.Lk1_ref:\n\ts_mov_b32 %1, .Lk1_end_a-.Lk1_ref\n\ts_mov_b32 %2, .Lk1_end_b-.Lk1_ref" : "=s"(code0), "=s"(codeA), "=s"(codeB));
    const unsigned codeN = (codeA > codeB ? codeA : codeB) + 192;
    if (wid == 7) {
        const __amdgpu_buffer_rsrc_t sI = SRD(code0, codeN);
        dma16(ldsb + K1_SINK, (unsigned)(lane * 4096) | (unsigned)((3 - lane) & 0x80000000), sI, (unsigned)(idx * 128));
    }

    const int ch = 32 * j + 8 * (fq & 1) + 4 * (fr >> 3) + 16 * ((fr >> 2) & 1) + (fr & 3);
    const int grow = pm * 256 + j * 8 + wid;
    u32x4 xa, xb, ga, gb; unsigned plr, pli, pld;
    {
        const u16* xr = x + (size_t)grow * DM;
        const unsigned l16 = lane * 16, c2 = ch * 2;
        asm volatile("s_nop 4\n\tglobal_load_dwordx4 %0, %7, %9\n\tglobal_load_dwordx4 %1, %7, %9 offset:1024\n\t"
                     "global_load_dwordx4 %2, %7, %10\n\tglobal_load_dwordx4 %3, %7, %10 offset:1024\n\t"
                     "global_load_ushort %4, %8, %11\n\tglobal_load_ushort %5, %8, %12\n\tglobal_load_ushort %6, %8, %13"
                     : "=&v"(xa), "=&v"(xb), "=&v"(ga), "=&v"(gb), "=&v"(plr), "=&v"(pli), "=&v"(pld)
                     : "v"(l16), "v"(c2), "s"(xr), "s"(gw), "s"(lre), "s"(lim), "s"(logdt) : "memory");
    }

    const bool pf = wid == 7;
    const int q4 = wid + 28, q5 = wid + 35;
    const __amdgpu_buffer_rsrc_t sX = SRD(!pf || !(bid & 32) ? x : (bid & 16) ? Bim : Bre, !pf || !(bid & 32) ? SEQ * DM * 2 : NP * DM * 2);
    const int pb = (bid >> 3) | ((bid & 7) << 5);
    const __amdgpu_buffer_rsrc_t sG = SRD(!pf ? Wg : (pb & 1) ? Wd : (pb & 2) ? Cim : Cre, (pf && !(pb & 1)) ? DM * NP * 2 : DM * FF * 2);
    const __amdgpu_buffer_rsrc_t sU = SRD((pf && (pb & 1)) ? Wg : Wu, DM * FF * 2);
    const __amdgpu_buffer_rsrc_t s4 = SRD(pf ? (const u16*)ws : q4 < 32 ? x : (q4 & 2) ? Bim : Bre, pf ? WS_SYNC_BYTES : q4 < 32 ? SEQ * DM * 2 : NP * DM * 2);
    const __amdgpu_buffer_rsrc_t s5 = SRD(pf ? (const u16*)code0 : (q5 & 2) ? Bim : Bre, pf ? codeN : q5 < 40 ? NP * DM * 2 : 0);

    const int l3 = lane >> 3;
    const unsigned voffX = pf ? (unsigned)(lane * 2048) : (unsigned)(((l3 >> 2) * 16 + (l3 & 3)) * 2048 + (((lane & 7) ^ (l3 & 6)) * 16));
    unsigned voffW;
    { const int gam = wid * 64 + lane, rho = gam / 14, c = gam - rho * 14, k = (rho & 0x13) | ((rho & 4) << 1) | ((rho & 8) >> 1);
      voffW = pf ? (unsigned)(lane * 128) : (unsigned)(k * (FF * 2) + c * 16); }
#define XROW(q) (pm * 256 + ((q) >> 3) * 64 + (((q) >> 2) & 1) * 8 + ((q) & 1) * 32 + (((q) >> 1) & 1) * 4)
#define BROW(q) (32 * j + 8 * (((q) >> 2) & 1) + 4 * ((q) & 1))
    constexpr unsigned WST = 64 * FF * 2, WSH = 32 * FF * 2, FAR = 0x40000000u;
    const unsigned so0 = pf ? 0u : (unsigned)(XROW(wid) * 2048);
    unsigned px = (unsigned)((bid & ((bid & 32) ? 15 : 31)) * 131072 + (bid >> 6) * 128), pxs = 512;
    px = 0x80000000u; pxs = 0;
    const unsigned so1 = pf ? FAR : (unsigned)(XROW(wid + 7) * 2048), so2 = pf ? FAR : (unsigned)(XROW(wid + 14) * 2048), so3 = pf ? FAR : (unsigned)(XROW(wid + 21) * 2048),
                   so4 = pf ? FAR : (unsigned)((q4 < 32 ? XROW(q4) : BROW(q4)) * 2048), so5 = pf ? FAR : (unsigned)(BROW(q5) * 2048);
    const unsigned soW = (unsigned)(j * 224), soW2 = pf ? FAR : soW + WSH;
    const unsigned mSink = ldsb + K1_SINK, mb0 = pf ? mSink : ldsb + wid * 1024, mk = pf ? 0u : ~0u;
#define MZ(off) (mb0 + ((unsigned)(off) & mk))
    const unsigned m50 = q5 < 40 ? MZ(35 * 1024) : mSink, m51 = q5 < 40 ? MZ(35 * 1024 + K1_ASB) : mSink;
    const unsigned ust = pf ? (1u << 20) : WST, gst = !pf ? WST : (pb & 1) ? (1u << 20) : (1u << 19);
    const unsigned su0 = pf ? (unsigned)((pb >> 1) * 8192) + (unsigned)(PF_W0) * ust : soW;
    const unsigned sg0 = pf ? (unsigned)(((pb & 1) ? pb >> 1 : pb >> 2) * 8192) - (unsigned)(PF_D0 + 2) * gst : soW;
#define ST_X(i, ko, b) dma16(MZ((b) * K1_ASB + (i) * 7 * 1024), voffX, sX, ((i) == 0 ? so0 : (i) == 1 ? so1 : (i) == 2 ? so2 : so3) + (ko))
#define ST_S4(ko, b) dma16(MZ((b) * K1_ASB + 28 * 1024), voffX, s4, so4 + (ko))
#define ST_S5(ko, b) dma16((b) ? m51 : m50, voffX, s5, so5 + (ko))
#define ST_G1(t, b) dma16(MZ(K1_W + (b) * K1_WBUF), voffW, sG, sg0 + (t) * gst)
#define ST_G2(t, b) dma16(MZ(K1_W + (b) * K1_WBUF + 7168), voffW, sG, soW2 + (t) * WST)
#define ST_U1(t, b) dma16(MZ(K1_W + (b) * K1_WBUF + K1_UOFF), voffW, sU, su0 + (t) * ust)
#define ST_U2(t, b) dma16(MZ(K1_W + (b) * K1_WBUF + K1_UOFF + 7168), voffW, sU, soW2 + (t) * WST)

    if (!pf) { ST_X(0, 0, 0); ST_X(1, 0, 0); ST_X(2, 0, 0); ST_X(3, 0, 0); ST_S4(0, 0); ST_S5(0, 0); ST_G1(0, 0); ST_G2(0, 0); ST_U2(0, 0); ST_U1(0, 0); }
    FSTAMP(1);

    const int ch0 = (fq ^ (fr & 6)) * 16;
    LAS char* rA0 = lds + wr * 8192 + fr * 128 + ch0;
    LAS char* rA1 = lds + wr * 8192 + fr * 128 + (ch0 ^ 64);
    LAS char* rS0 = lds + 32768 + wc * 4096 + fr * 128 + ch0;
    LAS char* rS1 = lds + 32768 + wc * 4096 + fr * 128 + (ch0 ^ 64);
    const int trow = (16 * (fq >> 1) + 4 * (fq & 1) + ((lane & 15) >> 2)) * 224;
    LAS char* rW = lds + K1_W + trow + wc * 96 + (lane & 3) * 8;
    LAS char* rM = lds + K1_W + trow + 192 + wc * 16 + (lane & 1) * 8 + ((lane >> 1) & 1) * K1_UOFF;

    f32x4 acc[4][9];
#pragma unroll
    for (int m = 0; m < 4; ++m)
#pragma unroll
        for (int n = 0; n < 9; ++n) { acc[m][n] = (f32x4){0.f, 0.f, 0.f, 0.f}; asm volatile("" : "+v"(acc[m][n])); }
    bf16x8 Af[4][2], Bf[3][2];

#define RD_A(b) do { _Pragma("unroll") for (int m = 0; m < 4; ++m) { Af[m][0] = lds_b128(rA0 + (b) * K1_ASB + m * 2048); Af[m][1] = lds_b128(rA1 + (b) * K1_ASB + m * 2048); } } while (0)
#define RD_S(slot, ri, b) do { Bf[slot][0] = lds_b128(rS0 + (b) * K1_ASB + (ri) * 2048); Bf[slot][1] = lds_b128(rS1 + (b) * K1_ASB + (ri) * 2048); } while (0)
#define RD_W(slot, base, off) do { Bf[slot][0] = lds_tr((base) + (off), (base) + (off) + 1792); Bf[slot][1] = lds_tr((base) + (off) + 7168, (base) + (off) + 7168 + 1792); } while (0)
#define MMA3(n0, n1, n2) do { __builtin_amdgcn_s_setprio(1); \
        _Pragma("unroll") for (int m = 0; m < 4; ++m) { \
            mfma_acc(acc[m][n0], Af[m][0], Bf[0][0]); mfma_acc(acc[m][n0], Af[m][1], Bf[0][1]); mfma_acc(acc[m][n1], Af[m][0], Bf[1][0]); mfma_acc(acc[m][n1], Af[m][1], Bf[1][1]); \
            mfma_acc(acc[m][n2], Af[m][0], Bf[2][0]); mfma_acc(acc[m][n2], Af[m][1], Bf[2][1]); } \
        __builtin_amdgcn_s_setprio(0); } while (0)
#define RD_AM(m, b) do { Af[m][0] = lds_b128(rA0 + (b) * K1_ASB + (m) * 2048); Af[m][1] = lds_b128(rA1 + (b) * K1_ASB + (m) * 2048); } while (0)
#define SEC1(t, b) do { RD_S(0, 0, b); RD_S(1, 1, b); ST_U1((t) + 1, (b) ^ 1); RD_W(2, rW, (b) * K1_WBUF); RD_AM(0, b); ST_U2((t) + 1, (b) ^ 1); RD_AM(1, b); RD_AM(2, b); RD_AM(3, b); } while (0)
#define SEC2(k0, ko, b) do { ST_X(0, k0, b); RD_W(0, rW, (b) * K1_WBUF + 32); ST_X(1, ko, b); RD_W(1, rW, (b) * K1_WBUF + 64); ST_X(2, ko, b); RD_W(2, rM, (b) * K1_WBUF); ST_X(3, ko, b); } while (0)
#define SEC3(t, ko, b) do { ST_G1((t) + 2, b); RD_W(0, rW, (b) * K1_WBUF + K1_UOFF); ST_G2((t) + 2, b); RD_W(1, rW, (b) * K1_WBUF + K1_UOFF + 32); ST_S4(ko, b); \
        RD_W(2, rW, (b) * K1_WBUF + K1_UOFF + 64); ST_S5(ko, b); } while (0)
#define GO(n0, n1, n2) do { WAIT_L0(); BAR(); SCHED(); MMA3(n0, n1, n2); SCHED(); } while (0)

#define RELANE() int tz = threadIdx.x; asm volatile("" : "+v"(tz)); const int er = tz & 15, eq = (tz >> 4) & 3; \
    const int ech = 32 * j + 8 * wc + 4 * (er >> 3) + 16 * ((er >> 2) & 1) + (er & 3); (void)ech; (void)eq
    asm volatile("" : "+v"(rA0), "+v"(rA1), "+v"(rS0), "+v"(rS1), "+v"(rW), "+v"(rM));
    gu32* gtab = (gu32*)(ws + WS_G);
    WAIT_V(10);
    if (pf) WAIT_V(0);
    asm volatile("" : "+v"(xa), "+v"(xb), "+v"(ga), "+v"(gb), "+v"(plr), "+v"(pli), "+v"(pld) :: "memory");
    FSTAMP(2);
    {
        float s = 0.f, s2 = 0.f;
#pragma unroll
        for (int e = 0; e < 4; ++e) { s = dot2(xa[e], ga[e], s); s2 = dot2(xb[e], gb[e], s2); }
        s += s2;
#define DPP_ADD(ctrl, rmask) s += __int_as_float(__builtin_amdgcn_update_dpp(0, __float_as_int(s), ctrl, rmask, 0xf, true))
        DPP_ADD(0x111, 0xf); DPP_ADD(0x112, 0xf); DPP_ADD(0x114, 0xf); DPP_ADD(0x118, 0xf); DPP_ADD(0x142, 0xa); DPP_ADD(0x143, 0xc);
        const float g = rbf(__builtin_amdgcn_rcpf(1.f + __builtin_amdgcn_exp2f(-1.4426950408889634f * rbf(s))));
        if (lane == 63) __hip_atomic_store(gtab + grow, pk2(g, 1.f - g), RLX_AGENT);
    }
    FSTAMP(3);
    if (!pf) BAR();
    if (!pf) { ST_X(0, 128, 1); ST_X(1, 128, 1); ST_X(2, 128, 1); ST_X(3, 128, 1); ST_S4(128, 1); ST_S5(128, 1); ST_G1(1, 1); ST_G2(1, 1); FSTAMP(4); WAIT_V(11); }
    else {
        const float lr = bflo(plr), li = bflo(pli), dt = rbf(expf(bflo(pld)));
        const float e = expf(dt * lr); float sn, cs; sincosf(dt * li, &sn, &cs);
        const f32x2 a1 = {e * cs, e * sn};
        const float nr = a1.x - 1.f, den = lr * lr + li * li;
        f32x2 a16 = cmul(a1, a1); a16 = cmul(a16, a16); a16 = cmul(a16, a16); a16 = cmul(a16, a16);
        const f32x2 a32 = cmul(a16, a16), a48 = cmul(a32, a16), a64 = cmul(a32, a32), a128 = cmul(a64, a64), a192 = cmul(a128, a64), a256 = cmul(a128, a128);
        if (lane < 32) {
            LAS f32x4* ct = (LAS f32x4*)(lds + K1_CTAB + lane * 96);
            ct[0] = (f32x4){a1.x, a1.y, 2.f * ((nr * lr + a1.y * li) / den), 2.f * ((a1.y * lr - nr * li) / den)};
            ct[1] = (f32x4){1.f, 0.f, a16.x, a16.y}; ct[2] = (f32x4){a32.x, a32.y, a48.x, a48.y};
            ct[3] = (f32x4){1.f, 0.f, a64.x, a64.y}; ct[4] = (f32x4){a128.x, a128.y, a192.x, a192.y};
            ct[5] = (f32x4){a256.x, a256.y, 0.f, 0.f};
        }
        BAR();
        ST_X(0, px, 0); ST_U1(0, 0);
        dma16c(ldsb + K1_HS + 1024, (unsigned)(lane * 16), s4, WS_HR + (bid >> 7) * 1024);
        dma16c(ldsb + K1_HS, (unsigned)(lane * 16), s4, bid == 0 ? WS_HA : 0x80000000u);
    }
    FSTAMP(5);
    if (wc == 1) BAR();
    STAMP_TAKE(st1);
    LT(1);
    FSTAMP(6);
#pragma nounroll
    for (int t = 0; t < 16; t += 2) {
        asm volatile("" : "+s"(t));
        const unsigned ko = t == 14 ? 0x80000000u : (unsigned)(t + 2) * 128u;
        const unsigned k0 = pf ? px + pxs : ko, k1 = pf ? px + 2 * pxs : ko + 128;
        px += 2 * pxs;
        BAR(); FSTAMP(8 + t); SEC1(t, 0); if (!pf) WAIT_V(10);
        GO(7, 8, 0);
        BAR(); SEC2(k0, ko, 0);
        GO(1, 2, 6);
        BAR(); SEC3(t, ko, 0); if (!pf) WAIT_V(10);
        GO(3, 4, 5);
        BAR(); FSTAMP(9 + t); SEC1(t + 1, 1); if (!pf) WAIT_V(10);
        GO(7, 8, 0);
        BAR(); SEC2(k1, ko + 128, 1);
        GO(1, 2, 6);
        BAR(); SEC3(t + 1, ko + 128, 1);
        if (!pf) WAIT_V(10);
        else {
            unsigned l128 = voffW; int hb = bid; kptr kh = (kptr)__builtin_amdgcn_kernarg_segment_ptr();
            asm volatile("" : "+v"(l128)); asm volatile("" : "+s"(hb), "+s"(kh));
            const unsigned l16 = l128 >> 3, OUT = 0x80000000u;
            const int hi_ = hb >> 3, hj = (hb & 7) * 4 + (hi_ & 3), hp = hi_ >> 2;
            LAS unsigned* hst = (LAS unsigned*)(lds + K1_HS + 2048);
#define RFL(v) ((unsigned)__builtin_amdgcn_readfirstlane(v))
#define HDMA(m, vo, srd, so) asm volatile("s_nop 4\n\ts_mov_b32 m0, %0\n\ts_nop 0\n\tbuffer_load_dwordx4 %1, %2, %3 offen sc1 lds" ::"s"((unsigned)(m)), "v"(vo), "s"(srd), "s"((unsigned)(so)) : "m0", "memory")
#define HLD(op, v, vo) asm volatile("s_nop 4\n\t" op " %0, %1, %2, 0 offen sc1\n\ts_waitcnt vmcnt(0)" : "=v"(v) : "v"(vo), "s"(s4) : "memory")
#define HST32(off, v) __builtin_amdgcn_raw_buffer_store_b32((unsigned)(v), s4, off, 0, 16)
#define HST64(off, lo_, hi2_) do { __builtin_amdgcn_raw_buffer_store_b64((u32x2){(unsigned)(lo_), (unsigned)(hi2_)}, s4, off, 0, 16); asm volatile("" ::: "memory"); } while (0)
#define REPLY(q_, tag_) do { _Pragma("unroll") for (int i = 0; i < 4; ++i) HST64(WS_HR + l16 * 2 + i * 8, (q_)[i], tag_); } while (0)
#define LANE0(off) (l16 == 0 ? (unsigned)(off) : OUT)
#define AT(v) int t##v = t; asm volatile("" : "+s"(t##v)); if (t##v == v)
            if ((t & 2) == 0) {
                WAIT_V(20);
                AT(0) {
                    const unsigned ask = *(LAS unsigned*)(lds + K1_HS + 1024 + (hb & 127) * 8) + 1u;
                    const u32x4 old = *(LAS u32x4*)(lds + K1_HS + l16);
                    HST32(LANE0(WS_HQ + hb * 4), ask);
                    unsigned tag = RFL(old[0]) + 1u;
                    while (__any(old[0] == tag || old[1] == tag || old[2] == tag || old[3] == tag)) ++tag;
                    hst[0] = ask; hst[1] = tag; hst[2] = 0u;
                }
                AT(4) { if (hb == 0) { const u32x4 q = *(LAS u32x4*)(lds + K1_HS + l16); const unsigned tag = hst[1]; REPLY(q, tag); } }
                AT(8) {
                    const u32x2 r = *(LAS u32x2*)(lds + K1_HS + 1024 + (hb & 127) * 8);
                    const bool hit = r[0] == hst[0];
                    HST32(hit ? LANE0(WS_HA + hb * 4) : OUT, r[1]);
                    hst[hit ? 1 : 3] = r[1]; hst[2] = hit;
                }
                AT(12) {
                    u32x4 ans = *(LAS u32x4*)(lds + K1_HS + l16);
                    unsigned tag = hst[1], done = hst[2];
                    if (!__all(done && ans[0] == tag && ans[1] == tag && ans[2] == tag && ans[3] == tag)) {
                        const unsigned ask = RFL(hst[0]); unsigned spins = 0;
                        tag = RFL(tag); done = RFL(done);
#pragma nounroll
                        do {
                            if (hb == 0) { u32x4 q; HLD("buffer_load_dwordx4", q, WS_HQ + l16); REPLY(q, tag); }
                            u32x2 r; HLD("buffer_load_dwordx2", r, WS_HR + hb * 8);
                            if (!done && RFL(r[0]) == ask) { tag = RFL(r[1]); done = 1u; HST32(LANE0(WS_HA + hb * 4), tag); }
                            HLD("buffer_load_dwordx4", ans, WS_HA + l16);
                        } while (!__all(done && ans[0] == tag && ans[1] == tag && ans[2] == tag && ans[3] == tag) && ++spins < (1u << 18));
                    }
                    HDMA(ldsb + K1_GTAB, l16, s4, WS_G + hp * 1024);
                }
            }
            int tq = t; asm volatile("" : "+s"(tq));
            if (tq & 2) {
                AT(2) {
                    ROLE(hb, hi_);
                    HST64((hp < 7 ? WS_CARRY + hp * 8192 + hj * 256 : OUT) + (l16 < 32 * 16 ? l16 >> 1 : OUT >> 1), ~0u, ~0u);
                    HST32(l16 == 4 * 16 ? WS_RDY + (hp * 32 + hj) * 4 : (late ? OUT : WS_FLAG2 + tile * 128) + (l16 < 4 * 16 ? l16 * 2 : OUT >> 1), ~0u);
                    HDMA(ldsb + K1_HS, l16, s4, hb == 0 ? WS_HQ : OUT);
                    px = OUT; pxs = 0;
                }
                AT(6) {
                    const __amdgpu_buffer_rsrc_t sA = SRD((char*)kh[13] + WS_A2, WS_A2_BYTES), sO = SRD(kh[14], SEQ * DM * 4);
                    const unsigned vo = (l128 >> 7) * A2S, vo2 = l128 << 10;
                    HDMA(ldsb + K1_SINK, vo * 32, sA, (hi_ + 1) * A2S - 128); HDMA(ldsb + K1_SINK, vo2 * 32, sO, hi_ * 131072);
                    HDMA(ldsb + K1_HS + 1024, l16, s4, WS_HR + (hb >> 7) * 1024);
                }
                AT(10) {
                    HDMA(ldsb + K1_SINK, l128 * 32, s5, 8192 + hi_ * 128);
                    HDMA(ldsb + K1_HS, l16, s4, WS_HA);
                }
                AT(14) WAIT_V(20);
            }
        }
        GO(3, 4, 5);
    }
    asm volatile("s_nop 15" ::: "memory");
    if (wc == 0) BAR();
    RELANE();
    STAMP_PUT(0, 0, st0); STAMP_PUT(0, 1, st1);
    STAMP(0, 2);
    LT(2);
    FSTAMP(24);
    kptr ka = (kptr)__builtin_amdgcn_kernarg_segment_ptr(); asm volatile("" : "+s"(ka));
#define KARG(T, i) ((T)ka[i])
    char* const ws2 = KARG(char*, 13);

    LAS char* ctab = lds + K1_CTAB + (wc * 16 + er) * 96;
    LAS char* etab = lds + K1_ETAB + (wc * 16 + er) * 144;
    const f32x4 cst = *(LAS f32x4*)ctab;
    const f32x2 a1 = {cst[0], cst[1]}, cf = {cst[2], cst[3]};
    u32x4 gt[4];
#pragma unroll
    for (int m = 0; m < 4; ++m) gt[m] = *(LAS u32x4*)(lds + K1_GTAB + wr * 256 + eq * 64 + m * 16);
    FSTAMP(25);
    const __amdgpu_buffer_rsrc_t sA2 = SRD(ws2 + WS_A2, WS_A2_BYTES);
#define ST16(voff, v) __builtin_amdgcn_raw_buffer_store_b128(v, sA2, voff, 0, STPOL)
#define PK8(v) (u32x4){pk2(v[0], v[1]), pk2(v[2], v[3]), pk2(v[4], v[5]), pk2(v[6], v[7])}
    const int mblk = pm * 2 + (wr >> 1);
#define IMG_Q(r, q) (unsigned)(((q) >> 1) * 256 + ((q) & 1) * 16 + ((r) & 3) * 32 + mblk * 16384 + (wr & 1) * 1024)
#define IMG_T(r, q) (IMG_Q(r, q) + (unsigned)((((r) >> 2) & 1) * 2048 + ((r) >> 3) * 128 + (j >> 2) * A2S + (j & 3) * 4096))
#define IMG_M(r, q) (IMG_Q(r, q) + (unsigned)((((r) >> 2) & 1) * 2048 + ((r) >> 3) * 512 + (24 + (j >> 2)) * A2S + (j & 3) * 4096))
#define IMG_S(r, q) (IMG_Q(r, q) + (unsigned)((((r) >> 2) & 1) * 4096 + ((r) >> 3) * 2048 + (32 + (j >> 1)) * A2S + (j & 1) * 8192))
    const unsigned lo = IMG_T(er, eq);
#define GLU_HALF(n, hh, o) do { float v[8]; \
        _Pragma("unroll") for (int e = 0; e < 8; ++e) { const int m = 2 * (hh) + (e >> 2), q = e & 3; v[e] = relu(acc[m][n][q]) * (acc[m][3 + (n)][q] * bfhi(gt[m][q])); } \
        o = PK8(v); } while (0)
#define GLU_TILE(n) do { u32x4 o_; GLU_HALF(n, 0, o_); ST16(lo + (wc * 48 + 8 * (n)) * A2S, o_); GLU_HALF(n, 1, o_); ST16(lo + (wc * 48 + 8 * (n)) * A2S + 512, o_); } while (0)
    GLU_TILE(0);
    GLU_TILE(1);
    asm volatile("" ::: "memory");
    {
        f32x2 h = {acc[0][7][0], acc[0][8][0]};
#pragma unroll
        for (int e = 1; e < 16; ++e) h = cfma(a1, h, (f32x2){acc[e >> 2][7][e & 3], acc[e >> 2][8][e & 3]});
        *(LAS f32x2*)(etab + (wr * 4 + eq) * 8) = h;
    }
    WAIT_V(0); WAIT_L0();
    FSTAMP(26);
    BAR(); asm volatile("" ::: "memory");
    gu32* rdy = (gu32*)(ws2 + WS_RDY);
    if (tz == 192) __hip_atomic_store(rdy + pm * 32 + j, 1u, RLX_AGENT);
    gu64* carry = (gu64*)(ws2 + WS_CARRY);
#define CHAN(c, r) (32 * j + 8 * (c) + 4 * ((r) >> 3) + 16 * (((r) >> 2) & 1) + ((r) & 3))
    if (wid < 2 && pm < 7) {
        const f32x2 a16 = *(LAS f32x2*)(lds + K1_CTAB + (wid * 16 + er) * 96 + 24);
        f32x4 E[8];
#pragma unroll
        for (int i = 0; i < 8; ++i) E[i] = *(LAS f32x4*)(lds + K1_ETAB + (wid * 16 + er) * 144 + i * 16);
        f32x2 run = {E[0][0], E[0][1]};
#pragma unroll
        for (int i = 1; i < 16; ++i) run = cfma(a16, run, (f32x2){E[i >> 1][(i & 1) * 2], E[i >> 1][(i & 1) * 2 + 1]});
        unsigned ux = __float_as_uint(run.x), uy = __float_as_uint(run.y);
        if (ux == 0xFFFFFFFFu) ux = 0x7FC00000u;
        if (eq == 0) __hip_atomic_store(carry + pm * 1024 + CHAN(wid, er), ((unsigned long long)uy << 32) | ux, RLX_AGENT);
    }
    STAMP(0, 3);
    FSTAMP(27);
    GLU_TILE(2);
    {
        float v[8];
        const bool lowh = er < 8;
#pragma unroll
        for (int e = 0; e < 8; ++e) {
            const int m = e >> 2, q = e & 3;
            const float v0 = lowh ? relu(acc[m][6][q]) : acc[m][6][q] * bfhi(gt[m][q]);
            const float v1 = lowh ? relu(acc[2 + m][6][q]) : acc[2 + m][6][q] * bfhi(gt[2 + m][q]);
            const float rcv = __int_as_float(__builtin_amdgcn_mov_dpp(__float_as_int(lowh ? v1 : v0), 0x128, 0xf, 0xf, true));
            v[e] = (lowh ? v0 : v1) * rcv;
        }
        ST16(IMG_M(er, eq) + wc * 128, PK8(v));
    }
    LAS f32x4* park = (LAS f32x4*)(lds + 3 * 32768 + wr * 8192 + (tz & 63) * 16);
    if (wc == 1) {
#pragma unroll
        for (int m = 0; m < 4; ++m) { park[m * 128] = acc[m][7]; park[m * 128 + 64] = acc[m][8]; }
    }
    STAMP(0, 4);
    WAIT_L0();
    FSTAMP(28);
    BAR(); asm volatile("" ::: "memory");
    FSTAMP(29);

    ROLE(bid, idx);
    const int n0 = nblk * 128;
    const int rs0 = late ? (48 + SHIFT) % 5 : (40 - SHIFT) % 5;

    if (wc == 1) {
        int ty = threadIdx.x; asm volatile("" : "+v"(ty) :: "memory");
        const int ln = ty & 63, e3 = ln >> 3;
#define LEVEL(lvl) do { if (ty == 256) __hip_atomic_store(rdy + pm * 32 + j, (unsigned)(lvl), RLX_AGENT); } while (0)
        const __amdgpu_buffer_rsrc_t sC = SRD(late ? KARG(const u16*, 10) : KARG(const u16*, 9), DM * NP * 2), sW = SRD(KARG(const u16*, 11), FF * DM * 2);
        const unsigned voffA = (unsigned)(ln * 16);
        const unsigned voffC = (unsigned)(e3 * 2048 + (((ln & 7) ^ (e3 & 6)) * 16));
        const unsigned voffD = (unsigned)(((e3 >> 2) * 8 + (e3 & 3)) * 2048 + ((((ln >> 1) & 3) ^ (e3 >> 1)) * 32) + (ln & 1) * 16);
        const unsigned soA = (unsigned)((late ? 0 : 48) * A2S + mb2 * 16384 + wr * 4096);
        const unsigned soC = (unsigned)((n0 + 32 * wr) * 2048);
        const unsigned soD = (unsigned)(((late ? 0 : 48) + 112 * wr) * 2048 + n0 * 2);
        const unsigned mB = ldsb + wr * 4096;
#define DMA_A(i, k, sl) dma16c(mB + (sl) + (i) * 1024, voffA, sA2, soA + (k) * A2S + (i) * 1024)
#define DMA_C(srd, i, p, sl) dma16(mB + (sl) + 16384 + (i) * 1024, voffC, srd, soC + (p) * 128 + (i) * (8 * 2048))
#define DMA_D(i, row, sl) dma16(mB + (sl) + 16384 + (i) * 1024, voffD, sW, soD + (row) * 2048 + ((i) >> 1) * (4 * 2048) + ((i) & 1) * 128)
#define POLL(v, p) asm volatile("global_load_dword %0, %1, off sc1\n\ts_waitcnt vmcnt(0)" : "=v"(v) : "v"(p) : "memory")
#define ASK(v, p) asm volatile("global_load_dword %0, %1, off sc1" : "=v"(v) : "v"(p) : "memory")
#define NEED(v, p, lvl) do { asm volatile("" : "+v"(v) :: "memory"); if (!__all((int)(v) >= (lvl))) { unsigned spins_ = 0, w_; \
            do { __builtin_amdgcn_s_sleep(1); POLL(w_, p); } while (!__all((int)w_ >= (lvl)) && ++spins_ < (1u << 18)); } } while (0)
        gu32* rl = rdy + (mb2 >> 1) * 32 + (ln & 31);
#define XP(k_) (((((k_) & 3) << 1) | (((k_) >> 2) & 1)) ^ (xc & 1))
#pragma unroll
        for (int p = 0; p < 3; ++p)
#pragma unroll
            for (int i = 0; i < 4; ++i) DMA_D(i, 448 * XP(p), p * 32768);
        unsigned fv;
        {
            const int other = ((ln >> 2) ^ xc) & 1;
            unsigned spins = 0;
            do POLL(fv, rl); while (!__all((int)fv >= 1 || (GATE2 && other)) && ++spins < (1u << 18));
        }
        FSTAMP(31);
#pragma unroll
        for (int p = 0; p < 3; ++p)
#pragma unroll
            for (int i = 0; i < 4; ++i) DMA_A(i, XP(p), p * 32768);
        WAIT_V(8); FSTAMP(32); BAR(); FSTAMP(33); LT(3);
        int k = 3, sl = 3 * 32768;
#define A4(img, sl) dma4c(mB + (sl), voffA, sA2, soA + (img) * A2S)
#define D4(row, sl) do { const unsigned b_ = soD + (row) * 2048; \
            asm volatile("s_mov_b32 m0, %0\n\ts_nop 0\n\tbuffer_load_dwordx4 %1, %2, %3 offen" BPOL " lds\n\tbuffer_load_dwordx4 %1, %2, %4 offen offset:2048" BPOL " lds\n\t" \
                         "buffer_load_dwordx4 %1, %2, %5 offen offset:3072" BPOL " lds\n\ts_mov_b32 m0, %6\n\ts_nop 0\n\tbuffer_load_dwordx4 %1, %2, %3 offen offset:128" BPOL " lds" \
                         ::"s"(mB + (sl) + 16384), "v"(voffD), "s"(sW), "s"(b_), "s"(b_ + 8192 - 2048), "s"(b_ + 8192 + 128 - 3072), "s"(mB + (sl) + 16384 + 1024 - 128) : "m0", "memory"); } while (0)
#define C4(srd, p, sl) do { const unsigned b_ = soC + (p) * 128; dma4(mB + (sl) + 16384, voffC, srd, b_, b_ + 16384 - 1024, b_ + 32768 - 2048, b_ + 49152 - 3072); } while (0)
#define STAGE_W() do { const int q_ = k < 16 ? (k & 3) | ((k & 4) << 1) | ((k & 8) >> 1) : k, x_ = XP(q_), row_ = 448 * x_ + ((q_ >> 3) == 3 ? 96 : 16 * (q_ >> 3)); \
            A4((q_ & ~7) + x_, sl); D4(row_, sl); ++k; sl = sl == 4 * 32768 ? 0 : sl + 32768; } while (0)
#define LSYNC() do { WAIT_V(16); BAR(); } while (0)
        STAGE_W(); WAIT_V(12); BAR();
        STAGE_W(); LSYNC(); STAGE_W(); LSYNC();
        ASK(fv, rl);
        STAGE_W(); LSYNC(); STAGE_W(); LSYNC();
        NEED(fv, rl, 1);
        STAGE_W(); LSYNC(); STAGE_W(); LSYNC();
        LEVEL(2);
#pragma nounroll
        while (k < 13) { STAGE_W(); LSYNC(); }
        ASK(fv, rl);
        STAGE_W(); LSYNC(); STAGE_W(); LSYNC(); STAGE_W(); LSYNC();
        NEED(fv, rl, 2);
        const int nw = late ? 32 : 24;
#pragma nounroll
        while (k < nw) { STAGE_W(); LSYNC(); }
        const int np = late ? 16 : 16 - SHIFT;
#pragma nounroll
        for (int p = 0; p < np; ++p) {
            A4(k, sl); C4(sC, p, sl);
            ++k; sl = sl == 4 * 32768 ? 0 : sl + 32768;
            LSYNC();
        }
        if (SHIFT && late) {
            const __amdgpu_buffer_rsrc_t sR = SRD(KARG(const u16*, 9), DM * NP * 2);
#pragma nounroll
            for (int p = 16 - SHIFT; p < 16; ++p) {
                A4(72 + p, sl); C4(sR, p, sl);
                sl = sl == 4 * 32768 ? 0 : sl + 32768;
                LSYNC();
            }
        }
        const int tb = mb2 * 128 + (wr >> 1) * 64 + (wr & 1) * 8 + (ln >> 5) * 16, dcol = n0 + (ln & 31) * 4;
#define ROW(i) (tb + ((i) >> 3) * 32 + ((i) & 7))
        LT(4);
        FSTAMP(40);
        gu32* flag = (gu32*)(ws2 + WS_FLAG2 + tile * 128 + wr * 32);
        const __amdgpu_buffer_rsrc_t sP = SRD(KARG(float*, 14), SEQ * DM * 4);
#define POFF(i) ((ROW(i) * DM + dcol) * 4)
        const int wn1 = (ln >> 4) & 1, s0 = rs0 + wn1, s1 = rs0 + 2 + wn1;
        const int inp = wr * 8192 + ((ln >> 2) & 3) * 1024 + (ln & 3) * 256, sw = (ln & 15) ^ ((ln >> 5) * 8);
        LAS char* q0 = lds + (s0 >= 5 ? s0 - 5 : s0) * 32768 + inp;
        LAS char* q1 = lds + (s1 >= 5 ? s1 - 5 : s1) * 32768 + inp;
#define SUMS(i) (*(LAS f32x4*)(q0 + ((i) >> 3) * 4096 + ((sw ^ ((i) & 7)) * 16)) + *(LAS f32x4*)(q1 + ((i) >> 3) * 4096 + ((sw ^ ((i) & 7)) * 16)))
        f32x4 ex[16];
        if (late) {
            const __amdgpu_buffer_rsrc_t sO = SRD(KARG(float*, 14), SEQ * DM * 4);
            WAIT_V(8); BAR();
            FSTAMP(41);
            ASK(fv, flag);
            WAIT_V(0); BAR();
            FSTAMP(42);
            NEED(fv, flag, 1);
            FSTAMP(43);
#pragma unroll
            for (int i = 0; i < 16; ++i) ex[i] = __builtin_bit_cast(f32x4, __builtin_amdgcn_raw_buffer_load_b128(sP, POFF(i), 0, 16));
            FSTAMP(44);
            BAR(); asm volatile("" ::: "memory");
            FSTAMP(45);
            STAMP(1, 3);
#pragma unroll
            for (int i = 0; i < 16; ++i) __builtin_amdgcn_raw_buffer_store_b128(__builtin_bit_cast(u32x4, ex[i] + SUMS(i)), sO, (ROW(i) * DM + dcol) * 4, 0, OUTPOL);
        } else {
            const __amdgpu_buffer_rsrc_t sXk = SRD(KARG(const u16*, 0), SEQ * DM * 2), sDk = SRD(KARG(const u16*, 12), DM * 2), sGk = SRD(ws2 + WS_G, 8192);
            WAIT_V(8); BAR();
            FSTAMP(41);
            u32x2 dk = __builtin_amdgcn_raw_buffer_load_b64(sDk, dcol * 2, 0, 0), xv[16]; u32x4 gwd[4];
#pragma unroll
            for (int i = 0; i < 4; ++i) gwd[i] = __builtin_amdgcn_raw_buffer_load_b128(sGk, ROW(4 * i) * 4, 0, 16);
#pragma unroll
            for (int i = 0; i < 16; ++i) xv[i] = __builtin_amdgcn_raw_buffer_load_b64(sXk, (tb * DM + dcol) * 2 + (i & 1) * 2048, (((i) >> 3) * 32 + ((i) & 6)) * 2048, 0);
            WAIT_V(21); BAR();
            FSTAMP(42);
            asm volatile("" : "+v"(dk), "+v"(gwd[0]), "+v"(gwd[1]), "+v"(gwd[2]), "+v"(gwd[3]));
#pragma unroll
            for (int i = 0; i < 16; ++i) {
                const float g = bflo(gwd[i >> 2][i & 3]);
                ex[i] = (f32x4){g * rbf(bflo(xv[i][0]) * bflo(dk[0])), g * rbf(bfhi(xv[i][0]) * bfhi(dk[0])),
                                g * rbf(bflo(xv[i][1]) * bflo(dk[1])), g * rbf(bfhi(xv[i][1]) * bfhi(dk[1]))};
                asm volatile("" : "+v"(ex[i]));
            }
            FSTAMP(44);
            BAR(); asm volatile("" ::: "memory");
            FSTAMP(45);
            STAMP(1, 3);
#pragma unroll
            for (int i = 0; i < 16; ++i) __builtin_amdgcn_raw_buffer_store_b128(__builtin_bit_cast(u32x4, ex[i] + SUMS(i)), sP, POFF(i), 0, 16);
            FSTAMP(46);
            WAIT_V(0);
            if (ln == 0) __hip_atomic_store(flag, 1u, RLX_AGENT);
        }
        STAMP(1, 6);
        STAMP(1, 7);
        FSTAMP(47);
        FSTAMP(48);
        FDUMP();
        asm volatile(".Lk1_end_a:");
        return;
    }

    {
        unsigned long long gq[2][7];
#pragma unroll
        for (int c = 0; c < 2; ++c)
#pragma unroll
            for (int q = 0; q < 7; ++q) gq[c][q] = __hip_atomic_load(carry + (q + pm >= 7 ? q + pm - 7 : 0) * 1024 + CHAN(c, er), RLX_AGENT);
        f32x2 h[2];
        {
            f32x2 a16[2], run[2]; f32x4 E[2][6], F0[2]; f32x2 F1[2];
#pragma unroll
            for (int c = 0; c < 2; ++c) {
                a16[c] = *(LAS f32x2*)(ctab + c * (16 * 96) + 24);
#pragma unroll
                for (int i = 0; i < 6; ++i) E[c][i] = *(LAS f32x4*)(etab + c * (16 * 144) + i * 16);
                F0[c] = *(LAS f32x4*)(etab + c * (16 * 144) + wr * 32); F1[c] = *(LAS f32x2*)(etab + c * (16 * 144) + wr * 32 + 16);
                run[c] = (f32x2){0.f, 0.f};
            }
#pragma unroll
            for (int w = 0; w < 3; ++w)
                if (wr > w) {
#pragma unroll
                    for (int i = 0; i < 4; ++i)
#pragma unroll
                        for (int c = 0; c < 2; ++c) run[c] = cfma(a16[c], run[c], (f32x2){E[c][2 * w + (i >> 1)][(i & 1) * 2], E[c][2 * w + (i >> 1)][(i & 1) * 2 + 1]});
                }
#pragma unroll
            for (int c = 0; c < 2; ++c) {
                const f32x2 r1 = cfma(a16[c], run[c], (f32x2){F0[c][0], F0[c][1]}), r2 = cfma(a16[c], r1, (f32x2){F0[c][2], F0[c][3]}), r3 = cfma(a16[c], r2, F1[c]);
                h[c] = sel4(eq, run[c], r1, r2, r3);
            }
        }
        f32x4 ur[4], ui[4];
#pragma unroll
        for (int m = 0; m < 4; ++m) { ur[m] = park[m * 128]; ui[m] = park[m * 128 + 64]; }
        const f32x4 cs1 = *(LAS f32x4*)(ctab + 16 * 96);
        FSTAMP(30);
        f32x2 a256[2], aq[2];
#pragma unroll
        for (int c = 0; c < 2; ++c) {
            LAS char* ct = ctab + c * (16 * 96);
            a256[c] = *(LAS f32x2*)(ct + 80); aq[c] = cmul(*(LAS f32x2*)(ct + 16 + eq * 8), *(LAS f32x2*)(ct + 48 + wr * 8));
        }
        unsigned mx = 0;
#pragma unroll
        for (int q = 0; q < 7; ++q) mx = max(mx, max((unsigned)gq[0][q], (unsigned)gq[1][q]));
        if (pm > 0 && __any(mx == 0xFFFFFFFFu)) {
            unsigned spins = 0;
            do {
                mx = 0;
#pragma unroll
                for (int c = 0; c < 2; ++c)
#pragma unroll
                    for (int q = 0; q < 7; ++q) {
                        gq[c][q] = __hip_atomic_load(carry + (q + pm >= 7 ? q + pm - 7 : 0) * 1024 + CHAN(c, er), RLX_AGENT);
                        mx = max(mx, (unsigned)gq[c][q]);
                    }
            } while (__any(mx == 0xFFFFFFFFu) && ++spins < (1u << 18));
        }
        f32x2 cg[2] = {{0.f, 0.f}, {0.f, 0.f}};
#pragma unroll
        for (int q = 0; q < 7; ++q) {
            const unsigned keep = q + pm >= 7 ? 0xFFFFFFFFu : 0u;
#pragma unroll
            for (int c = 0; c < 2; ++c) cg[c] = cfma(a256[c], cg[c], (f32x2){__uint_as_float((unsigned)gq[c][q] & keep), __uint_as_float((unsigned)(gq[c][q] >> 32) & keep)});
        }
        h[0] = cfma(aq[0], cg[0], h[0]); h[1] = cfma(aq[1], cg[1], h[1]);
        STAMP(0, 5);
        FSTAMP(31);
        const unsigned ls = IMG_S(er, eq);
        const f32x2 b1 = {cs1[0], cs1[1]}, bf = {cs1[2], cs1[3]};
#pragma unroll
        for (int hh = 0; hh < 2; ++hh) {
            float vr[8], vi[8], wr8[8], wi8[8];
#pragma unroll
            for (int e = 0; e < 8; ++e) {
                const int m = 2 * hh + (e >> 2), q = e & 3;
                h[0] = cfma(a1, h[0], (f32x2){acc[m][7][q], acc[m][8][q]});
                h[1] = cfma(b1, h[1], (f32x2){ur[m][q], ui[m][q]});
                const float g2 = bflo(gt[m][q]);
                const f32x2 s0 = cmul(cf, h[0]), s1 = cmul(bf, h[1]);
                vr[e] = s0.x * g2; vi[e] = -s0.y * g2; wr8[e] = s1.x * g2; wi8[e] = -s1.y * g2;
            }
            ST16(ls + 40 * A2S + hh * 512, PK8(vr)); ST16(ls + 40 * A2S + hh * 512 + 128, PK8(wr8));
            ST16(ls + hh * 512, PK8(vi)); ST16(ls + hh * 512 + 128, PK8(wi8));
        }
    }
    STAMP(0, 6);
    int ty = threadIdx.x; asm volatile("" : "+v"(ty) :: "memory");
    const int ln = ty & 63, fr2 = ty & 15, fq2 = (ty >> 4) & 3;
    const int wn = wid & 1, kk = wid >> 1;
    LAS char* rH = lds + kk * 8192 + (ln >> 5) * 4096 + (ln & 31) * 8;
    LAS char* rC = lds + 16384 + (wn * 64 + fr2) * 128 + (((4 * kk + fq2) ^ (fr2 & 6)) * 16);
    LAS char* rWb = lds + 16384 + kk * 8192 + (ln >> 5) * 4096 + wn * 1024 + ((ln >> 2) & 7) * 128 + (ln & 3) * 8;
    const int rsw = (ln >> 3) & 3;
    f32x4 ac2[4][8];
#pragma unroll
    for (int n = 0; n < 4; ++n)
#pragma unroll
        for (int m = 0; m < 8; ++m) { ac2[n][m] = (f32x4){0.f, 0.f, 0.f, 0.f}; asm volatile("" : "+v"(ac2[n][m])); }
    STAMP(0, 7);
    FSTAMP(32);
    FDUMP();
    s16x4 HX[8][2], HY[8][2], WX[4][2], WY[4][2];
    int sl = 0;

#define RD1(RD, i, H, W) do { \
        if ((i) < 8) { \
            if (RD == 1) W[(i) >> 1][(i) & 1] = tr4(rWb + sl + ((((i) >> 1) ^ rsw) * 32) + ((i) & 1) * 2048); \
            else if (((i) & 1) == 0) { const s16x8 v_ = *(LAS s16x8*)(rC + sl + ((i) >> 1) * 2048); \
                W[(i) >> 1][0] = __builtin_shufflevector(v_, v_, 0, 1, 2, 3); W[(i) >> 1][1] = __builtin_shufflevector(v_, v_, 4, 5, 6, 7); } \
        } else H[((i) - 8) >> 1][(i) & 1] = tr4(rH + sl + (((i) - 8) >> 1) * 256 + ((i) & 1) * 2048); \
    } while (0)
#define LGKM0() __builtin_amdgcn_s_waitcnt(0xC07F)
#define MM(i, HC, WC) mfma_acc(ac2[(i) & 3][(i) >> 2], cat8(WC[(i) & 3][0], WC[(i) & 3][1]), cat8(HC[(i) >> 2][0], HC[(i) >> 2][1]))
#define STEP(RD, HC, WC, HN, WN) do { \
        _Pragma("unroll") for (int i = 0; i < 32; ++i) { MM(i, HC, WC); if (i < 24) RD1(RD, i, HN, WN); } \
        sl = sl == 4 * 32768 ? 0 : sl + 32768; LGKM0(); \
    } while (0)

    LGKM0();
    BAR();
    STAMP_TAKE(st9);
#pragma unroll
    for (int i = 0; i < 24; ++i) RD1(1, i, HX, WX);
    sl = 32768; LGKM0();
    const int nf = late ? 15 : 11;
#pragma nounroll
    for (int i = 0; i < nf; ++i) {
        if (i == 3) WAIT_V(0);
        BAR(); STEP(1, HX, WX, HY, WY); BAR(); STEP(1, HY, WY, HX, WX);
    }
    BAR(); STEP(1, HX, WX, HY, WY); BAR(); STEP(0, HY, WY, HX, WX);
#pragma nounroll
    for (int i = late ? -SHIFT / 2 : SHIFT / 2; i < 7; ++i) { BAR(); STEP(0, HX, WX, HY, WY); BAR(); STEP(0, HY, WY, HX, WX); }
    BAR(); STEP(0, HX, WX, HY, WY);
    {
        const int sw = rs0 + wid;
        LAS char* dmp = lds + (sw >= 5 ? sw - 5 : sw) * 32768 + fq2 * 256;
#define DUMP(i) *(LAS f32x4*)(dmp + (i) * 1024 + ((fr2 ^ (4 * ((i) & 3) + fq2)) * 16)) = ac2[(i) & 3][(i) >> 2]
#pragma unroll
        for (int i = 0; i < 32; ++i) { MM(i, HY, WY); if (i >= 12) DUMP(i - 12); }
        asm volatile("s_nop 15" ::: "memory");
#pragma unroll
        for (int i = 20; i < 32; ++i) DUMP(i);
    }
    WAIT_L0();
    STAMP_PUT(1, 0, st0); STAMP_PUT(1, 1, st9);
    STAMP(1, 2);
    BAR();
    asm volatile(".Lk1_end_b:");
}

extern "C" void kernel_launch(void* const* d_in, const int* in_sizes, int n_in, void* d_out, int out_size, void* d_ws, size_t ws_size,
                              hipStream_t stream) {
    const u16* x = (const u16*)d_in[0];
    const u16* lre = (const u16*)d_in[1];
    const u16* lim = (const u16*)d_in[2];
    const u16* Bre = (const u16*)d_in[3];
    const u16* Bim = (const u16*)d_in[4];
    const u16* Cre = (const u16*)d_in[5];
    const u16* Cim = (const u16*)d_in[6];
    const u16* logdt = (const u16*)d_in[7];
    const u16* dskip = (const u16*)d_in[8];
    const u16* Wg = (const u16*)d_in[10];
    const u16* Wu = (const u16*)d_in[11];
    const u16* Wd = (const u16*)d_in[12];
    const u16* gw = (const u16*)d_in[13];
    char* ws = (char*)d_ws;
    (void)hipFuncSetAttribute((const void*)k1_fused, hipFuncAttributeMaxDynamicSharedMemorySize, K2_LDS);
    k1_fused<<<256, 512, K2_LDS, stream>>>(x, Wg, Wu, Bre, Bim, lre, lim, logdt, gw, Cre, Cim, Wd, dskip, ws, (float*)d_out);
}
```

```cpp
#include <hip/hip_runtime.h>
#include <hip/hip_bf16.h>

typedef unsigned short u16;
typedef __bf16 bf16x8 __attribute__((ext_vector_type(8)));
typedef __bf16 bf16x2 __attribute__((ext_vector_type(2)));
typedef short s16x4 __attribute__((ext_vector_type(4)));
typedef short s16x8 __attribute__((ext_vector_type(8)));
typedef float f32x4 __attribute__((ext_vector_type(4)));
typedef float f32x2 __attribute__((ext_vector_type(2)));
typedef unsigned u32x4 __attribute__((ext_vector_type(4)));
typedef unsigned u32x2 __attribute__((ext_vector_type(2)));
#define LAS __attribute__((address_space(3)))
#define GAS __attribute__((address_space(1)))
typedef GAS unsigned gu32;
typedef GAS unsigned long long gu64;
typedef __attribute__((address_space(4))) const unsigned long long* kptr;
#define RLX_AGENT __ATOMIC_RELAXED, __HIP_MEMORY_SCOPE_AGENT

constexpr int SEQ = 2048, DM = 1024, NP = 1024, FF = 3584;
constexpr int KSTEPS2 = (2 * NP + FF) / 64;

constexpr unsigned WS_CARRY = 0;
constexpr unsigned WS_G = 7 * 8192;
constexpr unsigned WS_RDY = 65536;
constexpr unsigned WS_FLAG2 = WS_RDY + 1024;
constexpr unsigned WS_HQ = WS_FLAG2 + 16384;
constexpr unsigned WS_HA = WS_HQ + 1024;
constexpr unsigned WS_HR = WS_HA + 1024;
constexpr unsigned WS_SYNC_BYTES = WS_HR + 2048;
constexpr size_t WS_A2 = 1 << 20;
constexpr unsigned A2S = 262144 + 128;
constexpr size_t WS_A2_BYTES = (size_t)KSTEPS2 * A2S;

__device__ __forceinline__ float bflo(unsigned v) { return __uint_as_float(v << 16); }
__device__ __forceinline__ float bfhi(unsigned v) { return __uint_as_float(v & 0xffff0000u); }
__device__ __forceinline__ float rbf(float f) { return (float)(__bf16)f; }
__device__ __forceinline__ unsigned pk2(float a, float b) {
    bf16x2 v; v.x = (__bf16)a; v.y = (__bf16)b; return __builtin_bit_cast(unsigned, v);
}
__device__ __forceinline__ float relu(float v) { return __int_as_float(max(__float_as_int(v), 0)); }
__device__ __forceinline__ float dot2(unsigned a, unsigned b, float c) {
    return __builtin_amdgcn_fdot2_f32_bf16(__builtin_bit_cast(bf16x2, a), __builtin_bit_cast(bf16x2, b), c, false);
}

constexpr size_t WS_DBG = 200u << 20;
#define STAMP(kid, i)
#define STAMP_TAKE(v)
#define STAMP_PUT(kid, i, v)

#define FSTAMP(i)
#define FDUMP()
#define WAIT_V(n) asm volatile("s_waitcnt vmcnt(" #n ")" ::: "memory")
#define WAIT_L0() asm volatile("s_waitcnt lgkmcnt(0)" ::: "memory")
#define LGKM0() __builtin_amdgcn_s_waitcnt(0xC07F)
#define BAR() __builtin_amdgcn_s_barrier()
#define SCHED() __builtin_amdgcn_sched_barrier(0)
#define SRD(p, bytes) __builtin_amdgcn_make_buffer_rsrc((void*)(p), (short)0, (int)(bytes), 0x00020000)

__device__ __forceinline__ void dma16(unsigned m0v, unsigned voff, __amdgpu_buffer_rsrc_t srd, unsigned soff) {
    asm volatile("s_mov_b32 m0, %0\n\ts_nop 0\n\tbuffer_load_dwordx4 %1, %2, %3 offen lds" ::"s"(m0v), "v"(voff), "s"(srd), "s"(soff) : "m0", "memory");
}
__device__ __forceinline__ bf16x8 lds_b128(LAS char* p) { return __builtin_bit_cast(bf16x8, *(LAS s16x8*)p); }
__device__ __forceinline__ bf16x8 lds_tr(LAS char* p0, LAS char* p1) {
    s16x4 a = __builtin_amdgcn_ds_read_tr16_b64_v4i16((LAS s16x4*)p0);
    s16x4 b = __builtin_amdgcn_ds_read_tr16_b64_v4i16((LAS s16x4*)p1);
    return __builtin_bit_cast(bf16x8, __builtin_shufflevector(a, b, 0, 1, 2, 3, 4, 5, 6, 7));
}
#define MFMA(a, b, c) __builtin_amdgcn_mfma_f32_16x16x32_bf16(a, b, c, 0, 0, 0)
__device__ __forceinline__ f32x2 cmul(f32x2 a, f32x2 b) { return (f32x2){__builtin_fmaf(a.x, b.x, -(a.y * b.y)), __builtin_fmaf(a.x, b.y, a.y * b.x)}; }
__device__ __forceinline__ f32x2 cfma(f32x2 a, f32x2 b, f32x2 c) {
    return (f32x2){__builtin_fmaf(a.x, b.x, __builtin_fmaf(-a.y, b.y, c.x)), __builtin_fmaf(a.x, b.y, __builtin_fmaf(a.y, b.x, c.y))};
}
__device__ __forceinline__ f32x2 sel4(int i, f32x2 a, f32x2 b, f32x2 c, f32x2 d) {
    const bool o = i & 1, u = i & 2;
    const float lx = o ? b.x : a.x, ly = o ? b.y : a.y, hx = o ? d.x : c.x, hy = o ? d.y : c.y;
    return (f32x2){u ? hx : lx, u ? hy : ly};
}

#define STPOL 16
#define PF_W0 1
#define PF_D0 4
#define OUTPOL 16

#define K2_SPLIT 40
#define MAP 0
#define ABL_NOLOAD 0
#define ABL_NOREAD 0
#define ABL_NOMFMA 0
#define CODE_T 10
#define ILV 1
#define SHIFT 2
#define GATE2 1
#define PRO 0
#define MORD 3
#define DEEP 0
#define THIN 1
#define M04 1
#define ASKPOS 0
#define PSLEEP 4
#define LATE_FLIP 0
#define DIST 5
constexpr int K2_LDS = 5 * 32768;

__device__ __forceinline__ void dma16c(unsigned m0v, unsigned voff, __amdgpu_buffer_rsrc_t srd, unsigned soff) {
    asm volatile("s_mov_b32 m0, %0\n\ts_nop 0\n\tbuffer_load_dwordx4 %1, %2, %3 offen sc1 lds" ::"s"(m0v), "v"(voff), "s"(srd), "s"(soff) : "m0", "memory");
}
#define BPOL ""
#define APOL " sc1"
__device__ __forceinline__ void dma4c(unsigned m0v, unsigned voff, __amdgpu_buffer_rsrc_t srd, unsigned soff) {
    asm volatile("s_mov_b32 m0, %0\n\ts_nop 0\n\tbuffer_load_dwordx4 %1, %2, %3 offen" APOL " lds\n\tbuffer_load_dwordx4 %1, %2, %3 offen offset:1024" APOL " lds\n\t"
                 "buffer_load_dwordx4 %1, %2, %3 offen offset:2048" APOL " lds\n\tbuffer_load_dwordx4 %1, %2, %3 offen offset:3072" APOL " lds" ::"s"(m0v), "v"(voff), "s"(srd), "s"(soff) : "m0", "memory");
}
__device__ __forceinline__ void dma4(unsigned m0v, unsigned voff, __amdgpu_buffer_rsrc_t srd, unsigned s0, unsigned s1, unsigned s2, unsigned s3) {
    asm volatile("s_mov_b32 m0, %0\n\ts_nop 0\n\tbuffer_load_dwordx4 %1, %2, %3 offen lds\n\tbuffer_load_dwordx4 %1, %2, %4 offen offset:1024 lds\n\t"
                 "buffer_load_dwordx4 %1, %2, %5 offen offset:2048 lds\n\tbuffer_load_dwordx4 %1, %2, %6 offen offset:3072 lds" ::"s"(m0v), "v"(voff), "s"(srd), "s"(s0), "s"(s1), "s"(s2), "s"(s3) : "m0", "memory");
}
__device__ __forceinline__ void mfma_acc(f32x4& c, bf16x8 a, bf16x8 b) {
    asm volatile("v_mfma_f32_16x16x32_bf16 %0, %1, %2, %0" : "+v"(c) : "v"(a), "v"(b) : "memory");
}
__device__ __forceinline__ void mfma_acc2(f32x4& c, bf16x8 a0, bf16x8 b0, bf16x8 a1, bf16x8 b1) {
    asm volatile("v_mfma_f32_16x16x32_bf16 %0, %1, %2, %0\n\tv_mfma_f32_16x16x32_bf16 %0, %3, %4, %0" : "+v"(c) : "v"(a0), "v"(b0), "v"(a1), "v"(b1) : "memory");
}
__device__ __forceinline__ s16x4 tr4(LAS char* p) { return __builtin_amdgcn_ds_read_tr16_b64_v4i16((LAS s16x4*)p); }
__device__ __forceinline__ bf16x8 cat8(s16x4 a, s16x4 b) { return __builtin_bit_cast(bf16x8, __builtin_shufflevector(a, b, 0, 1, 2, 3, 4, 5, 6, 7)); }

constexpr int K1_ASB = 40960;
constexpr int K1_W = 81920;
constexpr int K1_WBUF = 28704, K1_UOFF = 14352;
constexpr int K1_ETAB = K1_W + 2 * K1_WBUF;
constexpr int K1_CTAB = K1_ETAB + 32 * 144;
constexpr int K1_SINK = K1_CTAB + 32 * 96;
constexpr int K1_GTAB = K1_SINK + 1024;
constexpr int K1_HS = K1_GTAB + 1024;

__global__ __launch_bounds__(512, 2) void k1_fused(const u16* __restrict__ x, const u16* __restrict__ Wg, const u16* __restrict__ Wu,
                                                       const u16* __restrict__ Bre, const u16* __restrict__ Bim, const u16* __restrict__ lre,
                                                       const u16* __restrict__ lim, const u16* __restrict__ logdt, const u16* __restrict__ gw,
                                                       const u16* __restrict__ Cre, const u16* __restrict__ Cim, const u16* __restrict__ Wd,
                                                       const u16* __restrict__ dskip, char* __restrict__ ws, float* __restrict__ out) {
    extern __shared__ __attribute__((aligned(16))) char smem[];
    LAS char* lds = (LAS char*)smem;
    const unsigned ldsb = (unsigned)(size_t)lds;
    const int tid = threadIdx.x, lane = tid & 63;
    const int wid = __builtin_amdgcn_readfirstlane(tid >> 6);
    const int wr = wid & 3, wc = wid >> 2;
    const int bid = blockIdx.x, idx = bid >> 3, j = (bid & 7) * 4 + (idx & 3), pm = idx >> 2;
    const int fr = lane & 15, fq = lane >> 4;
#define ROLE(b_, i_) const int xc = (b_) & 7, late = (xc & 1) ^ LATE_FLIP, mb2 = (xc >> 2) * 8 + ((i_) >> 2), nblk = ((xc >> 1) & 1) * 4 + ((i_) & 3), tile = mb2 * 8 + nblk
    STAMP_TAKE(st0);
#define LT(n)
#define LITE_DECL()
#define LT1()
    FSTAMP(0);

    const char* code0; unsigned codeA, codeB;
    asm volatile("s_getpc_b64 %0\n.Lk1_ref:\n\ts_mov_b32 %1, .Lk1_end_a-.Lk1_ref\n\ts_mov_b32 %2, .Lk1_end_b-.Lk1_ref" : "=s"(code0), "=s"(codeA), "=s"(codeB));
    const __amdgpu_buffer_rsrc_t sI = SRD(code0, (codeA > codeB ? codeA : codeB) + 192);
    const unsigned mSink = ldsb + K1_SINK;
    const bool pf = wid == 7;
    if (pf) { dma16(mSink, (unsigned)(lane * 128), sI, 0u); dma16(mSink, (unsigned)(lane * 128), sI, 8192u); }

    const int ch = 32 * j + 8 * (fq & 1) + 4 * (fr >> 3) + 16 * ((fr >> 2) & 1) + (fr & 3);
    const int grow = pm * 256 + j * 8 + wid;
    u32x4 xa, xb, ga, gb; unsigned plr, pli, pld;
    {
        const u16* xr = x + (size_t)grow * DM;
        const unsigned l16 = lane * 16, c2 = ch * 2;
        asm volatile("s_nop 4\n\tglobal_load_dwordx4 %0, %7, %9\n\tglobal_load_dwordx4 %1, %7, %9 offset:1024\n\t"
                     "global_load_dwordx4 %2, %7, %10\n\tglobal_load_dwordx4 %3, %7, %10 offset:1024\n\t"
                     "global_load_ushort %4, %8, %11\n\tglobal_load_ushort %5, %8, %12\n\tglobal_load_ushort %6, %8, %13"
                     : "=&v"(xa), "=&v"(xb), "=&v"(ga), "=&v"(gb), "=&v"(plr), "=&v"(pli), "=&v"(pld)
                     : "v"(l16), "v"(c2), "s"(xr), "s"(gw), "s"(lre), "s"(lim), "s"(logdt) : "memory");
    }

    const int q4 = wid + 28, q5 = wid + 35;
    const __amdgpu_buffer_rsrc_t sX = SRD(x, SEQ * DM * 2), sG = SRD(Wg, DM * FF * 2), sU = SRD(Wu, DM * FF * 2);
    const __amdgpu_buffer_rsrc_t s4 = SRD(q4 < 32 ? x : (q4 & 2) ? Bim : Bre, q4 < 32 ? SEQ * DM * 2 : NP * DM * 2);
    const __amdgpu_buffer_rsrc_t s5 = SRD((q5 & 2) ? Bim : Bre, q5 < 40 ? NP * DM * 2 : 0);
    const int l3 = lane >> 3;
    const unsigned voffX = (unsigned)(((l3 >> 2) * 16 + (l3 & 3)) * 2048 + (((lane & 7) ^ (l3 & 6)) * 16));
    unsigned voffW;
    { const int rho = (tid * 4682) >> 16, c = tid - rho * 14, k = (rho & 0x13) | ((rho & 4) << 1) | ((rho & 8) >> 1);
      voffW = (unsigned)(k * (FF * 2) + c * 16); }
#define XROW(q) (pm * 256 + ((q) >> 3) * 64 + (((q) >> 2) & 1) * 8 + ((q) & 1) * 32 + (((q) >> 1) & 1) * 4)
#define BROW(q) (32 * j + 8 * (((q) >> 2) & 1) + 4 * ((q) & 1))
    constexpr unsigned WST = 64 * FF * 2, WSH = 32 * FF * 2;
    const unsigned so0 = (unsigned)(XROW(wid) * 2048), so1 = (unsigned)(XROW(wid + 7) * 2048), so2 = (unsigned)(XROW(wid + 14) * 2048), so3 = (unsigned)(XROW(wid + 21) * 2048),
                   so4 = (unsigned)((q4 < 32 ? XROW(q4) : BROW(q4)) * 2048), so5 = (unsigned)(BROW(q5) * 2048);
    const unsigned soW = (unsigned)(j * 224), mb0 = ldsb + wid * 1024;
    const unsigned m50 = q5 < 40 ? mb0 + 35 * 1024 : mSink, m51 = q5 < 40 ? mb0 + 35 * 1024 + K1_ASB : mSink;
#define ST_X(i, ko, b) dma16(mb0 + (b) * K1_ASB + (i) * 7 * 1024, voffX, sX, ((i) == 0 ? so0 : (i) == 1 ? so1 : (i) == 2 ? so2 : so3) + (ko))
#define ST_S4(ko, b) dma16(mb0 + (b) * K1_ASB + 28 * 1024, voffX, s4, so4 + (ko))
#define ST_S5(ko, b) dma16((b) ? m51 : m50, voffX, s5, so5 + (ko))
#define ST_G(h, t, b) dma16(mb0 + K1_W + (b) * K1_WBUF + (h) * 7168, voffW, sG, soW + (t) * WST + (h) * WSH)
#define ST_U(h, t, b) dma16(mb0 + K1_W + (b) * K1_WBUF + K1_UOFF + (h) * 7168, voffW, sU, soW + (t) * WST + (h) * WSH)
    if (!pf) { ST_X(0, 0, 0); ST_X(1, 0, 0); ST_X(2, 0, 0); ST_X(3, 0, 0); ST_S4(0, 0); ST_S5(0, 0); ST_G(0, 0, 0); ST_G(1, 0, 0); ST_U(0, 0, 0); ST_U(1, 0, 0); }
    FSTAMP(1);

    const int ch0 = (fq ^ (fr & 6)) * 16;
    LAS char* rA0 = lds + wr * 8192 + fr * 128 + ch0;
    LAS char* rA1 = lds + wr * 8192 + fr * 128 + (ch0 ^ 64);
    LAS char* rS0 = lds + 32768 + wc * 4096 + fr * 128 + ch0;
    LAS char* rS1 = lds + 32768 + wc * 4096 + fr * 128 + (ch0 ^ 64);
    const int trow = (16 * (fq >> 1) + 4 * (fq & 1) + ((lane & 15) >> 2)) * 224;
    LAS char* rW = lds + K1_W + trow + wc * 96 + (lane & 3) * 8;
    LAS char* rM = lds + K1_W + trow + 192 + wc * 16 + (lane & 1) * 8 + ((lane >> 1) & 1) * K1_UOFF;

    f32x4 acc[4][9];
#pragma unroll
    for (int m = 0; m < 4; ++m)
#pragma unroll
        for (int n = 0; n < 9; ++n) { acc[m][n] = (f32x4){0.f, 0.f, 0.f, 0.f}; asm volatile("" : "+v"(acc[m][n])); }
    bf16x8 Af[4][2], Bf[3][2];

#define RD_A(m, b) do { Af[m][0] = lds_b128(rA0 + (b) * K1_ASB + (m) * 2048); Af[m][1] = lds_b128(rA1 + (b) * K1_ASB + (m) * 2048); } while (0)
#define RD_S(slot, ri, b) do { Bf[slot][0] = lds_b128(rS0 + (b) * K1_ASB + (ri) * 2048); Bf[slot][1] = lds_b128(rS1 + (b) * K1_ASB + (ri) * 2048); } while (0)
#define RD_W(slot, base, off) do { Bf[slot][0] = lds_tr((base) + (off), (base) + (off) + 1792); Bf[slot][1] = lds_tr((base) + (off) + 7168, (base) + (off) + 7168 + 1792); } while (0)
#define SEC1(b, d0, d1) do { RD_S(0, 0, b); RD_S(1, 1, b); d0; RD_W(2, rW, (b) * K1_WBUF); RD_A(0, b); d1; RD_A(1, b); RD_A(2, b); RD_A(3, b); } while (0)
#define SEC2(b, d0, d1, d2, d3) do { d0; RD_W(0, rW, (b) * K1_WBUF + 32); d1; RD_W(1, rW, (b) * K1_WBUF + 64); d2; RD_W(2, rM, (b) * K1_WBUF); d3; } while (0)
#define SEC3(b, d0, d1, d2, d3) do { d0; RD_W(0, rW, (b) * K1_WBUF + K1_UOFF); d1; RD_W(1, rW, (b) * K1_WBUF + K1_UOFF + 32); d2; RD_W(2, rW, (b) * K1_WBUF + K1_UOFF + 64); d3; } while (0)
#define GO(n0, n1, n2) do { asm volatile("" ::: "memory"); LGKM0(); BAR(); SCHED(); \
        _Pragma("unroll") for (int m = 0; m < 4; ++m) { \
            mfma_acc2(acc[m][n0], Af[m][0], Bf[0][0], Af[m][1], Bf[0][1]); mfma_acc2(acc[m][n1], Af[m][0], Bf[1][0], Af[m][1], Bf[1][1]); \
            mfma_acc2(acc[m][n2], Af[m][0], Bf[2][0], Af[m][1], Bf[2][1]); } \
        SCHED(); } while (0)

#define RELANE() int tz = threadIdx.x; asm volatile("" : "+v"(tz)); const int er = tz & 15, eq = (tz >> 4) & 3
    asm volatile("" : "+v"(rA0), "+v"(rA1), "+v"(rS0), "+v"(rS1), "+v"(rW), "+v"(rM));
    WAIT_V(10);
    if (pf) WAIT_V(0);
    asm volatile("" : "+v"(xa), "+v"(xb), "+v"(ga), "+v"(gb), "+v"(plr), "+v"(pli), "+v"(pld) :: "memory");
    FSTAMP(2);
    {
        float s = 0.f, s2 = 0.f;
#pragma unroll
        for (int e = 0; e < 4; ++e) { s = dot2(xa[e], ga[e], s); s2 = dot2(xb[e], gb[e], s2); }
        s += s2;
#define DPP_ADD(ctrl, rmask) s += __int_as_float(__builtin_amdgcn_update_dpp(0, __float_as_int(s), ctrl, rmask, 0xf, true))
        DPP_ADD(0x111, 0xf); DPP_ADD(0x112, 0xf); DPP_ADD(0x114, 0xf); DPP_ADD(0x118, 0xf); DPP_ADD(0x142, 0xa); DPP_ADD(0x143, 0xc);
        const float g = rbf(__builtin_amdgcn_rcpf(1.f + __builtin_amdgcn_exp2f(-1.4426950408889634f * rbf(s))));
        if (lane == 63) __hip_atomic_store((gu32*)(ws + WS_G) + grow, pk2(g, 1.f - g), RLX_AGENT);
    }
    FSTAMP(3);
    LITE_DECL();
    if (!pf) {
        BAR();
        ST_X(0, 128, 1); ST_X(1, 128, 1); ST_X(2, 128, 1); ST_X(3, 128, 1); ST_S4(128, 1); ST_S5(128, 1); ST_G(0, 1, 1); ST_G(1, 1, 1);
        WAIT_V(11);
        if (wc == 1) BAR();
        LT1();
        FSTAMP(6);
#pragma nounroll
        for (int t = 0; t < 16; t += 2) {
            asm volatile("" : "+s"(t));
            const unsigned ko = t == 14 ? 0x80000000u : (unsigned)(t + 2) * 128u;
            BAR(); FSTAMP(8 + t); SEC1(0, ST_U(0, t + 1, 1), ST_U(1, t + 1, 1)); WAIT_V(10);
            GO(7, 8, 0);
            BAR(); SEC2(0, ST_X(0, ko, 0), ST_X(1, ko, 0), ST_X(2, ko, 0), ST_X(3, ko, 0));
            GO(1, 2, 6);
            BAR(); SEC3(0, ST_G(0, t + 2, 0), ST_G(1, t + 2, 0), ST_S4(ko, 0), ST_S5(ko, 0)); WAIT_V(10);
            GO(3, 4, 5);
            BAR(); FSTAMP(9 + t); SEC1(1, ST_U(0, t + 2, 0), ST_U(1, t + 2, 0)); WAIT_V(10);
            GO(7, 8, 0);
            BAR(); SEC2(1, ST_X(0, ko + 128, 1), ST_X(1, ko + 128, 1), ST_X(2, ko + 128, 1), ST_X(3, ko + 128, 1));
            GO(1, 2, 6);
            BAR(); SEC3(1, ST_G(0, t + 3, 1), ST_G(1, t + 3, 1), ST_S4(ko + 128, 1), ST_S5(ko + 128, 1)); WAIT_V(10);
            GO(3, 4, 5);
        }
    }
    int w7 = wid; asm volatile("" : "+s"(w7));
    if (w7 == 7) {
        {
            const float lr = bflo(plr), li = bflo(pli), dt = rbf(expf(bflo(pld)));
            const float e = expf(dt * lr); float sn, cs; sincosf(dt * li, &sn, &cs);
            const f32x2 a1 = {e * cs, e * sn};
            const float nr = a1.x - 1.f, den = lr * lr + li * li;
            f32x2 a16 = cmul(a1, a1); a16 = cmul(a16, a16); a16 = cmul(a16, a16); a16 = cmul(a16, a16);
            const f32x2 a32 = cmul(a16, a16), a48 = cmul(a32, a16), a64 = cmul(a32, a32), a128 = cmul(a64, a64), a192 = cmul(a128, a64), a256 = cmul(a128, a128);
            if (lane < 32) {
                LAS f32x4* ct = (LAS f32x4*)(lds + K1_CTAB + lane * 96);
                ct[0] = (f32x4){a1.x, a1.y, 2.f * ((nr * lr + a1.y * li) / den), 2.f * ((a1.y * lr - nr * li) / den)};
                ct[1] = (f32x4){1.f, 0.f, a16.x, a16.y}; ct[2] = (f32x4){a32.x, a32.y, a48.x, a48.y};
                ct[3] = (f32x4){1.f, 0.f, a64.x, a64.y}; ct[4] = (f32x4){a128.x, a128.y, a192.x, a192.y};
                ct[5] = (f32x4){a256.x, a256.y, 0.f, 0.f};
            }
        }
        const int pb = (bid >> 3) | ((bid & 7) << 5);
        const __amdgpu_buffer_rsrc_t sPW = SRD((pb & 1) ? Wg : Wu, DM * FF * 2), sPD = SRD((pb & 1) ? Wd : (pb & 2) ? Cim : Cre, (pb & 1) ? DM * FF * 2 : DM * NP * 2);
        const __amdgpu_buffer_rsrc_t sH = SRD(ws, WS_SYNC_BYTES);
        const unsigned l128 = lane * 128, OUT = 0x80000000u;
        const unsigned pds = (pb & 1) ? (1u << 20) : (1u << 19), pw0 = (unsigned)((pb >> 1) * 8192), pd0 = (unsigned)(((pb & 1) ? pb >> 1 : pb >> 2) * 8192);
#define PF_W(r) dma16(mSink, l128, sPW, pw0 + (unsigned)(r) * (1u << 20))
#define PF_D(r) dma16(mSink, l128, sPD, pd0 + (unsigned)(r) * pds)
#define RFL(v) ((unsigned)__builtin_amdgcn_readfirstlane(v))
#define HDMA(m, vo, srd, so) asm volatile("s_nop 4\n\ts_mov_b32 m0, %0\n\ts_nop 0\n\tbuffer_load_dwordx4 %1, %2, %3 offen sc1 lds" ::"s"((unsigned)(m)), "v"(vo), "s"(srd), "s"((unsigned)(so)) : "m0", "memory")
#define HLD(op, v, vo) asm volatile("s_nop 4\n\t" op " %0, %1, %2, 0 offen sc1\n\ts_waitcnt vmcnt(0)" : "=v"(v) : "v"(vo), "s"(sH) : "memory")
#define HST32(off, v) do { __builtin_amdgcn_raw_buffer_store_b32((unsigned)(v), sH, off, 0, 16); asm volatile("" ::: "memory"); } while (0)
#define HST64(off, lo_, hi2_) do { __builtin_amdgcn_raw_buffer_store_b64((u32x2){(unsigned)(lo_), (unsigned)(hi2_)}, sH, off, 0, 16); asm volatile("" ::: "memory"); } while (0)
#define REPLY(q_) do { _Pragma("unroll") for (int i = 0; i < 4; ++i) HST64(WS_HR + l16 * 2 + i * 8, (q_)[i], tag); } while (0)
#define LANE0(off) (l16 == 0 ? (unsigned)(off) : OUT)
#define ALLANS(a_) __all((a_)[0] == tag && (a_)[1] == tag && (a_)[2] == tag && (a_)[3] == tag)
#define ATX(cond, ...) do { int t_ = t; asm volatile("" : "+s"(t_)); if (cond) { unsigned l16 = l128; asm volatile("" : "+v"(l16)); l16 >>= 3; __VA_ARGS__ } } while (0)
#define AT(v, ...) ATX(t_ == (v), __VA_ARGS__)
#define AT2(v, ...) ATX((unsigned)(t_ - (v)) <= 2u, __VA_ARGS__)
        unsigned ask = 0, tag = 0, done = 0;
        const int boss = bid == 0 ? 0 : 64;
        BAR();
        HDMA(ldsb + K1_HS + 1024, l128 >> 3, sH, WS_HR + (bid >> 7) * 1024);
        HDMA(ldsb + K1_HS, l128 >> 3, sH, bid == 0 ? WS_HA : OUT);
        PF_W(1);
        BAR();
        LT1();
        FSTAMP(6);
#pragma nounroll
        for (int t = 0; t < 16; t += 2) {
            asm volatile("" : "+s"(t));
            BAR(); FSTAMP(8 + t); SEC1(0, , );
            AT(10,
                HDMA(mSink, l128, sI, 10240); HDMA(mSink, l128, sI, 10240 + 8192); HDMA(mSink, l128, sI, 10240 + 16384););
            AT(12, HDMA(ldsb + K1_HS, l16, sH, WS_HA););
            GO(7, 8, 0);
            BAR(); SEC2(0, , , , );
            AT(0,
                ROLE(bid, idx);
                HST64((pm < 7 ? WS_CARRY + pm * 8192 + j * 256 : OUT) + (l16 < 32 * 16 ? l16 >> 1 : OUT >> 1), ~0u, ~0u);
                HST32(l16 == 4 * 16 ? WS_RDY + (pm * 32 + j) * 4 : (late ? OUT : WS_FLAG2 + tile * 128) + (l16 < 4 * 16 ? l16 * 2 : OUT >> 1), ~0u););
            AT(2,
                const __amdgpu_buffer_rsrc_t sA = SRD(ws + WS_A2, WS_A2_BYTES), sO = SRD(out, SEQ * DM * 4);
                const unsigned vo = (l16 >> 4) * A2S, vo2 = l16 << 13;
                HDMA(mSink, vo, sA, A2S - 128); HDMA(mSink, vo, sA, 65 * A2S - 128); HDMA(mSink, vo2, sO, 0u););
            AT2(4 + boss, WAIT_V(2); const u32x4 q = *(LAS u32x4*)(lds + K1_HS + l16); REPLY(q););
            AT2(6, HDMA(ldsb + K1_HS + 1024, l16, sH, WS_HR + (bid >> 7) * 1024););
            GO(1, 2, 6);
            BAR(); SEC3(0, , PF_W(t + 2), PF_D(t - PF_D0), );
            GO(3, 4, 5);
            BAR(); FSTAMP(9 + t); SEC1(1, , );
            AT2(2 + boss, HDMA(ldsb + K1_HS, l16, sH, WS_HQ););
            AT(14, WAIT_V(0););
            GO(7, 8, 0);
            BAR(); SEC2(1, , , , );
            AT(0,
                WAIT_V(5);
                ask = RFL(*(LAS unsigned*)(lds + K1_HS + 1024 + (bid & 127) * 8)) + 1u;
                const u32x4 old = *(LAS u32x4*)(lds + K1_HS + l16);
                HST32(LANE0(WS_HQ + bid * 4), ask);
                tag = RFL(old[0]) + 1u;
                while (__any(old[0] == tag || old[1] == tag || old[2] == tag || old[3] == tag)) ++tag;);
            AT2(6 + (int)done * 64,
                WAIT_V(2);
                const u32x2 r = *(LAS u32x2*)(lds + K1_HS + 1024 + (bid & 127) * 8);
                if (RFL(r[0]) == ask) { tag = RFL(r[1]); done = 1u; HST32(LANE0(WS_HA + bid * 4), tag); });
            AT(12,
                WAIT_V(2);
                u32x4 ans = *(LAS u32x4*)(lds + K1_HS + l16);
                if (!(done && ALLANS(ans))) {
                    unsigned spins = 0;
                    _Pragma("nounroll") do {
                        if (bid == 0) { u32x4 q; HLD("buffer_load_dwordx4", q, WS_HQ + l16); REPLY(q); }
                        if (!done) {
                            u32x2 r; HLD("buffer_load_dwordx2", r, WS_HR + bid * 8);
                            if (RFL(r[0]) == ask) { tag = RFL(r[1]); done = 1u; HST32(LANE0(WS_HA + bid * 4), tag); }
                        }
                        HLD("buffer_load_dwordx4", ans, WS_HA + l16);
                    } while (!(done && ALLANS(ans)) && ++spins < (1u << 18));
                }
                HDMA(ldsb + K1_GTAB, l16, sH, WS_G + pm * 1024););
            GO(1, 2, 6);
            BAR(); SEC3(1, , PF_W(t + 3), PF_D(t + 1 - PF_D0), );
            GO(3, 4, 5);
        }
    }
    asm volatile("s_nop 15" ::: "memory");
    if (wc == 0) BAR();
    RELANE();
    STAMP(0, 2);
    LT(2);
    FSTAMP(24);
    kptr ka = (kptr)__builtin_amdgcn_kernarg_segment_ptr(); asm volatile("" : "+s"(ka));
#define KARG(T, i) ((T)ka[i])
    char* const ws2 = KARG(char*, 13);

    LAS char* ctab = lds + K1_CTAB + (wc * 16 + er) * 96;
    LAS char* etab = lds + K1_ETAB + (wc * 16 + er) * 144;
    const f32x4 cst = *(LAS f32x4*)ctab;
    const f32x2 a1 = {cst[0], cst[1]}, cf = {cst[2], cst[3]};
    u32x4 gt[4];
#pragma unroll
    for (int m = 0; m < 4; ++m) gt[m] = *(LAS u32x4*)(lds + K1_GTAB + wr * 256 + eq * 64 + m * 16);
    FSTAMP(25);
    const __amdgpu_buffer_rsrc_t sA2 = SRD(ws2 + WS_A2, WS_A2_BYTES);
#define ST16(voff, v) __builtin_amdgcn_raw_buffer_store_b128(v, sA2, voff, 0, STPOL)
#define PK8(v) (u32x4){pk2(v[0], v[1]), pk2(v[2], v[3]), pk2(v[4], v[5]), pk2(v[6], v[7])}
    const int mblk = pm * 2 + (wr >> 1);
#define IMG_Q(r, q) (unsigned)(((q) >> 1) * 256 + ((q) & 1) * 16 + ((r) & 3) * 32 + mblk * 16384 + (wr & 1) * 1024)
#define IMG_T(r, q) (IMG_Q(r, q) + (unsigned)((((r) >> 2) & 1) * 2048 + ((r) >> 3) * 128 + (j >> 2) * A2S + (j & 3) * 4096))
#define IMG_M(r, q) (IMG_Q(r, q) + (unsigned)((((r) >> 2) & 1) * 2048 + ((r) >> 3) * 512 + (24 + (j >> 2)) * A2S + (j & 3) * 4096))
#define IMG_S(r, q) (IMG_Q(r, q) + (unsigned)((((r) >> 2) & 1) * 4096 + ((r) >> 3) * 2048 + (32 + (j >> 1)) * A2S + (j & 1) * 8192))
    const unsigned lo = IMG_T(er, eq);
#define GLU_HALF(n, hh, o) do { float v[8]; \
        _Pragma("unroll") for (int e = 0; e < 8; ++e) { const int m = 2 * (hh) + (e >> 2), q = e & 3; v[e] = relu(acc[m][n][q]) * (acc[m][3 + (n)][q] * bfhi(gt[m][q])); } \
        o = PK8(v); } while (0)
#define GLU_TILE(n) do { u32x4 o_; GLU_HALF(n, 0, o_); ST16(lo + (wc * 48 + 8 * (n)) * A2S, o_); GLU_HALF(n, 1, o_); ST16(lo + (wc * 48 + 8 * (n)) * A2S + 512, o_); } while (0)
    GLU_TILE(0);
    GLU_TILE(1);
    asm volatile("" ::: "memory");
    {
        f32x2 h = {acc[0][7][0], acc[0][8][0]};
#pragma unroll
        for (int e = 1; e < 16; ++e) h = cfma(a1, h, (f32x2){acc[e >> 2][7][e & 3], acc[e >> 2][8][e & 3]});
        *(LAS f32x2*)(etab + (wr * 4 + eq) * 8) = h;
    }
    WAIT_V(0); WAIT_L0();
    FSTAMP(26);
    BAR(); asm volatile("" ::: "memory");
    gu32* rdy = (gu32*)(ws2 + WS_RDY);
    if (tz == 192) __hip_atomic_store(rdy + pm * 32 + j, 1u, RLX_AGENT);
    gu64* carry = (gu64*)(ws2 + WS_CARRY);
#define CHAN(c, r) (32 * j + 8 * (c) + 4 * ((r) >> 3) + 16 * (((r) >> 2) & 1) + ((r) & 3))
    if (wid < 2 && pm < 7) {
        const f32x2 a16 = *(LAS f32x2*)(lds + K1_CTAB + (wid * 16 + er) * 96 + 24);
        f32x4 E[8];
#pragma unroll
        for (int i = 0; i < 8; ++i) E[i] = *(LAS f32x4*)(lds + K1_ETAB + (wid * 16 + er) * 144 + i * 16);
        f32x2 run = {E[0][0], E[0][1]};
#pragma unroll
        for (int i = 1; i < 16; ++i) run = cfma(a16, run, (f32x2){E[i >> 1][(i & 1) * 2], E[i >> 1][(i & 1) * 2 + 1]});
        unsigned ux = __float_as_uint(run.x), uy = __float_as_uint(run.y);
        if (ux == 0xFFFFFFFFu) ux = 0x7FC00000u;
        if (eq == 0) __hip_atomic_store(carry + pm * 1024 + CHAN(wid, er), ((unsigned long long)uy << 32) | ux, RLX_AGENT);
    }
    STAMP(0, 3);
    FSTAMP(27);
    GLU_TILE(2);
    {
        float v[8];
        const bool lowh = er < 8;
#pragma unroll
        for (int e = 0; e < 8; ++e) {
            const int m = e >> 2, q = e & 3;
            const float v0 = lowh ? relu(acc[m][6][q]) : acc[m][6][q] * bfhi(gt[m][q]);
            const float v1 = lowh ? relu(acc[2 + m][6][q]) : acc[2 + m][6][q] * bfhi(gt[2 + m][q]);
            const float rcv = __int_as_float(__builtin_amdgcn_mov_dpp(__float_as_int(lowh ? v1 : v0), 0x128, 0xf, 0xf, true));
            v[e] = (lowh ? v0 : v1) * rcv;
        }
        ST16(IMG_M(er, eq) + wc * 128, PK8(v));
    }
    LAS f32x4* park = (LAS f32x4*)(lds + 3 * 32768 + wr * 8192 + (tz & 63) * 16);
    if (wc == 1) {
#pragma unroll
        for (int m = 0; m < 4; ++m) { park[m * 128] = acc[m][7]; park[m * 128 + 64] = acc[m][8]; }
    }
    STAMP(0, 4);
    WAIT_L0();
    FSTAMP(28);
    BAR(); asm volatile("" ::: "memory");
    FSTAMP(29);

    ROLE(bid, idx);
    const int n0 = nblk * 128;
    const int rs0 = late ? (48 + SHIFT) % 5 : (40 - SHIFT) % 5;

    if (wc == 1) {
        int ty = threadIdx.x; asm volatile("" : "+v"(ty) :: "memory");
        const int ln = ty & 63, e3 = ln >> 3;
#define LEVEL(lvl) do { if (ty == 256) __hip_atomic_store(rdy + pm * 32 + j, (unsigned)(lvl), RLX_AGENT); } while (0)
        const __amdgpu_buffer_rsrc_t sC = SRD(late ? KARG(const u16*, 10) : KARG(const u16*, 9), DM * NP * 2), sW = SRD(KARG(const u16*, 11), FF * DM * 2);
        const unsigned voffA = (unsigned)(ln * 16);
        const unsigned voffC = (unsigned)(e3 * 2048 + (((ln & 7) ^ (e3 & 6)) * 16));
        const unsigned voffD = (unsigned)(((e3 >> 2) * 8 + (e3 & 3)) * 2048 + ((((ln >> 1) & 3) ^ (e3 >> 1)) * 32) + (ln & 1) * 16);
        const unsigned soA = (unsigned)((late ? 0 : 48) * A2S + mb2 * 16384 + wr * 4096);
        const unsigned soC = (unsigned)((n0 + 32 * wr) * 2048);
        const unsigned soD = (unsigned)(((late ? 0 : 48) + 112 * wr) * 2048 + n0 * 2);
        const unsigned mB = ldsb + wr * 4096;
#define DMA_A(i, k, sl) dma16c(mB + (sl) + (i) * 1024, voffA, sA2, soA + (k) * A2S + (i) * 1024)
#define DMA_C(srd, i, p, sl) dma16(mB + (sl) + 16384 + (i) * 1024, voffC, srd, soC + (p) * 128 + (i) * (8 * 2048))
#define DMA_D(i, row, sl) dma16(mB + (sl) + 16384 + (i) * 1024, voffD, sW, soD + (row) * 2048 + ((i) >> 1) * (4 * 2048) + ((i) & 1) * 128)
#define POLL(v, p) asm volatile("global_load_dword %0, %1, off sc1\n\ts_waitcnt vmcnt(0)" : "=v"(v) : "v"(p) : "memory")
#define ASK(v, p) asm volatile("global_load_dword %0, %1, off sc1" : "=v"(v) : "v"(p) : "memory")
#define NEED(v, p, lvl) do { asm volatile("" : "+v"(v) :: "memory"); if (!__all((int)(v) >= (lvl))) { unsigned spins_ = 0, w_; \
            do { __builtin_amdgcn_s_sleep(1); POLL(w_, p); } while (!__all((int)w_ >= (lvl)) && ++spins_ < (1u << 18)); } } while (0)
        gu32* rl = rdy + (mb2 >> 1) * 32 + (ln & 31);
#define XP(k_) (((((k_) & 3) << 1) | (((k_) >> 2) & 1)) ^ (xc & 1))
        unsigned fv;
#pragma unroll
        for (int p = 0; p < 3; ++p)
#pragma unroll
            for (int i = 0; i < 4; ++i) DMA_D(i, 448 * XP(p), p * 32768);
        POLL(fv, rl);
        {
            const int other = ((ln >> 2) ^ xc) & 1;
            unsigned spins = 0;
            while (!__all((int)fv >= 1 || (GATE2 && other)) && ++spins < (1u << 18)) { __builtin_amdgcn_s_sleep(PSLEEP); POLL(fv, rl); }
        }
        FSTAMP(31);
#pragma unroll
        for (int p = 0; p < 3; ++p)
#pragma unroll
            for (int i = 0; i < 4; ++i) DMA_A(i, XP(p), p * 32768);
        WAIT_V(8); FSTAMP(32); BAR(); FSTAMP(33); LT(3);
        int k = 3, sl = 3 * 32768;
#define A4(img, sl) dma4c(mB + (sl), voffA, sA2, soA + (img) * A2S)
#define D4(row, sl) do { const unsigned b_ = soD + (row) * 2048; \
            asm volatile("s_mov_b32 m0, %0\n\ts_nop 0\n\tbuffer_load_dwordx4 %1, %2, %3 offen" BPOL " lds\n\tbuffer_load_dwordx4 %1, %2, %4 offen offset:2048" BPOL " lds\n\t" \
                         "buffer_load_dwordx4 %1, %2, %5 offen offset:3072" BPOL " lds\n\ts_mov_b32 m0, %6\n\ts_nop 0\n\tbuffer_load_dwordx4 %1, %2, %3 offen offset:128" BPOL " lds" \
                         ::"s"(mB + (sl) + 16384), "v"(voffD), "s"(sW), "s"(b_), "s"(b_ + 8192 - 2048), "s"(b_ + 8192 + 128 - 3072), "s"(mB + (sl) + 16384 + 1024 - 128) : "m0", "memory"); } while (0)
#define C4(srd, p, sl) do { const unsigned b_ = soC + (p) * 128; dma4(mB + (sl) + 16384, voffC, srd, b_, b_ + 16384 - 1024, b_ + 32768 - 2048, b_ + 49152 - 3072); } while (0)
#define STAGE_W() do { const int q_ = k < 16 ? (k & 3) | ((k & 4) << 1) | ((k & 8) >> 1) : k, x_ = XP(q_), row_ = 448 * x_ + ((q_ >> 3) == 3 ? 96 : 16 * (q_ >> 3)); \
            A4((q_ & ~7) + x_, sl); D4(row_, sl); ++k; sl = sl == 4 * 32768 ? 0 : sl + 32768; } while (0)
#define LSYNC() do { WAIT_V(16); BAR(); } while (0)
        STAGE_W(); WAIT_V(12); BAR();
        STAGE_W(); LSYNC(); STAGE_W(); LSYNC();
        ASK(fv, rl);
        STAGE_W(); LSYNC(); STAGE_W(); LSYNC();
        NEED(fv, rl, 1);
        STAGE_W(); LSYNC(); STAGE_W(); LSYNC();
        LEVEL(2);
#pragma nounroll
        while (k < 13) { STAGE_W(); LSYNC(); }
        ASK(fv, rl);
        STAGE_W(); LSYNC(); STAGE_W(); LSYNC(); STAGE_W(); LSYNC();
        NEED(fv, rl, 2);
        const int nw = late ? 32 : 24;
#pragma nounroll
        while (k < nw) { STAGE_W(); LSYNC(); }
        const int np = late ? 16 : 16 - SHIFT;
#pragma nounroll
        for (int p = 0; p < np; ++p) {
            A4(k, sl); C4(sC, p, sl);
            ++k; sl = sl == 4 * 32768 ? 0 : sl + 32768;
            LSYNC();
        }
        if (SHIFT && late) {
            const __amdgpu_buffer_rsrc_t sR = SRD(KARG(const u16*, 9), DM * NP * 2);
#pragma nounroll
            for (int p = 16 - SHIFT; p < 16; ++p) {
                A4(72 + p, sl); C4(sR, p, sl);
                sl = sl == 4 * 32768 ? 0 : sl + 32768;
                LSYNC();
            }
        }
        const int tb = mb2 * 128 + (wr >> 1) * 64 + (wr & 1) * 8 + (ln >> 5) * 16, dcol = n0 + (ln & 31) * 4;
#define ROW(i) (tb + ((i) >> 3) * 32 + ((i) & 7))
        LT(4);
        FSTAMP(40);
        gu32* flag = (gu32*)(ws2 + WS_FLAG2 + tile * 128 + wr * 32);
        const __amdgpu_buffer_rsrc_t sP = SRD(KARG(float*, 14), SEQ * DM * 4);
#define POFF(i) ((ROW(i) * DM + dcol) * 4)
        const int wn1 = (ln >> 4) & 1, s0 = rs0 + wn1, s1 = rs0 + 2 + wn1;
        const int inp = wr * 8192 + ((ln >> 2) & 3) * 1024 + (ln & 3) * 256, sw = (ln & 15) ^ ((ln >> 5) * 8);
        LAS char* q0 = lds + (s0 >= 5 ? s0 - 5 : s0) * 32768 + inp;
        LAS char* q1 = lds + (s1 >= 5 ? s1 - 5 : s1) * 32768 + inp;
#define SUMS(i) (*(LAS f32x4*)(q0 + ((i) >> 3) * 4096 + ((sw ^ ((i) & 7)) * 16)) + *(LAS f32x4*)(q1 + ((i) >> 3) * 4096 + ((sw ^ ((i) & 7)) * 16)))
        f32x4 ex[16];
        if (late) {
            const __amdgpu_buffer_rsrc_t sO = SRD(KARG(float*, 14), SEQ * DM * 4);
            WAIT_V(8); BAR();
            FSTAMP(41);
            ASK(fv, flag);
            WAIT_V(0); BAR();
            FSTAMP(42);
            NEED(fv, flag, 1);
            FSTAMP(43);
#pragma unroll
            for (int i = 0; i < 16; ++i) ex[i] = __builtin_bit_cast(f32x4, __builtin_amdgcn_raw_buffer_load_b128(sP, POFF(i), 0, 16));
            FSTAMP(44);
            BAR(); asm volatile("" ::: "memory");
            FSTAMP(45);
            STAMP(1, 3);
#pragma unroll
            for (int i = 0; i < 16; ++i) __builtin_amdgcn_raw_buffer_store_b128(__builtin_bit_cast(u32x4, ex[i] + SUMS(i)), sO, (ROW(i) * DM + dcol) * 4, 0, OUTPOL);
        } else {
            const __amdgpu_buffer_rsrc_t sXk = SRD(KARG(const u16*, 0), SEQ * DM * 2), sDk = SRD(KARG(const u16*, 12), DM * 2), sGk = SRD(ws2 + WS_G, 8192);
            WAIT_V(8); BAR();
            FSTAMP(41);
            u32x2 dk = __builtin_amdgcn_raw_buffer_load_b64(sDk, dcol * 2, 0, 0), xv[16]; u32x4 gwd[4];
#pragma unroll
            for (int i = 0; i < 4; ++i) gwd[i] = __builtin_amdgcn_raw_buffer_load_b128(sGk, ROW(4 * i) * 4, 0, 16);
#pragma unroll
            for (int i = 0; i < 16; ++i) xv[i] = __builtin_amdgcn_raw_buffer_load_b64(sXk, (tb * DM + dcol) * 2 + (i & 1) * 2048, (((i) >> 3) * 32 + ((i) & 6)) * 2048, 0);
            WAIT_V(21); BAR();
            FSTAMP(42);
            asm volatile("" : "+v"(dk), "+v"(gwd[0]), "+v"(gwd[1]), "+v"(gwd[2]), "+v"(gwd[3]));
#pragma unroll
            for (int i = 0; i < 16; ++i) {
                const float g = bflo(gwd[i >> 2][i & 3]);
                ex[i] = (f32x4){g * rbf(bflo(xv[i][0]) * bflo(dk[0])), g * rbf(bfhi(xv[i][0]) * bfhi(dk[0])),
                                g * rbf(bflo(xv[i][1]) * bflo(dk[1])), g * rbf(bfhi(xv[i][1]) * bfhi(dk[1]))};
                asm volatile("" : "+v"(ex[i]));
            }
            FSTAMP(44);
            BAR(); asm volatile("" ::: "memory");
            FSTAMP(45);
            STAMP(1, 3);
#pragma unroll
            for (int i = 0; i < 16; ++i) __builtin_amdgcn_raw_buffer_store_b128(__builtin_bit_cast(u32x4, ex[i] + SUMS(i)), sP, POFF(i), 0, 16);
            FSTAMP(46);
            WAIT_V(0);
            if (ln == 0) __hip_atomic_store(flag, 1u, RLX_AGENT);
        }
        STAMP(1, 6);
        STAMP(1, 7);
        FSTAMP(47);
        FSTAMP(48);
        FDUMP();
        asm volatile(".Lk1_end_a:");
        return;
    }

    {
        unsigned long long gq[2][7];
#pragma unroll
        for (int c = 0; c < 2; ++c)
#pragma unroll
            for (int q = 0; q < 7; ++q) gq[c][q] = __hip_atomic_load(carry + (q + pm >= 7 ? q + pm - 7 : 0) * 1024 + CHAN(c, er), RLX_AGENT);
        f32x2 h[2];
        {
            f32x2 a16[2], run[2]; f32x4 E[2][6], F0[2]; f32x2 F1[2];
#pragma unroll
            for (int c = 0; c < 2; ++c) {
                a16[c] = *(LAS f32x2*)(ctab + c * (16 * 96) + 24);
#pragma unroll
                for (int i = 0; i < 6; ++i) E[c][i] = *(LAS f32x4*)(etab + c * (16 * 144) + i * 16);
                F0[c] = *(LAS f32x4*)(etab + c * (16 * 144) + wr * 32); F1[c] = *(LAS f32x2*)(etab + c * (16 * 144) + wr * 32 + 16);
                run[c] = (f32x2){0.f, 0.f};
            }
#pragma unroll
            for (int w = 0; w < 3; ++w)
                if (wr > w) {
#pragma unroll
                    for (int i = 0; i < 4; ++i)
#pragma unroll
                        for (int c = 0; c < 2; ++c) run[c] = cfma(a16[c], run[c], (f32x2){E[c][2 * w + (i >> 1)][(i & 1) * 2], E[c][2 * w + (i >> 1)][(i & 1) * 2 + 1]});
                }
#pragma unroll
            for (int c = 0; c < 2; ++c) {
                const f32x2 r1 = cfma(a16[c], run[c], (f32x2){F0[c][0], F0[c][1]}), r2 = cfma(a16[c], r1, (f32x2){F0[c][2], F0[c][3]}), r3 = cfma(a16[c], r2, F1[c]);
                h[c] = sel4(eq, run[c], r1, r2, r3);
            }
        }
        f32x4 ur[4], ui[4];
#pragma unroll
        for (int m = 0; m < 4; ++m) { ur[m] = park[m * 128]; ui[m] = park[m * 128 + 64]; }
        const f32x4 cs1 = *(LAS f32x4*)(ctab + 16 * 96);
        FSTAMP(30);
        f32x2 a256[2], aq[2];
#pragma unroll
        for (int c = 0; c < 2; ++c) {
            LAS char* ct = ctab + c * (16 * 96);
            a256[c] = *(LAS f32x2*)(ct + 80); aq[c] = cmul(*(LAS f32x2*)(ct + 16 + eq * 8), *(LAS f32x2*)(ct + 48 + wr * 8));
        }
        unsigned mx = 0;
#pragma unroll
        for (int q = 0; q < 7; ++q) mx = max(mx, max((unsigned)gq[0][q], (unsigned)gq[1][q]));
        if (pm > 0 && __any(mx == 0xFFFFFFFFu)) {
            unsigned spins = 0;
            do {
                mx = 0;
#pragma unroll
                for (int c = 0; c < 2; ++c)
#pragma unroll
                    for (int q = 0; q < 7; ++q) {
                        gq[c][q] = __hip_atomic_load(carry + (q + pm >= 7 ? q + pm - 7 : 0) * 1024 + CHAN(c, er), RLX_AGENT);
                        mx = max(mx, (unsigned)gq[c][q]);
                    }
            } while (__any(mx == 0xFFFFFFFFu) && ++spins < (1u << 18));
        }
        f32x2 cg[2] = {{0.f, 0.f}, {0.f, 0.f}};
#pragma unroll
        for (int q = 0; q < 7; ++q) {
            const unsigned keep = q + pm >= 7 ? 0xFFFFFFFFu : 0u;
#pragma unroll
            for (int c = 0; c < 2; ++c) cg[c] = cfma(a256[c], cg[c], (f32x2){__uint_as_float((unsigned)gq[c][q] & keep), __uint_as_float((unsigned)(gq[c][q] >> 32) & keep)});
        }
        h[0] = cfma(aq[0], cg[0], h[0]); h[1] = cfma(aq[1], cg[1], h[1]);
        STAMP(0, 5);
        FSTAMP(31);
        const unsigned ls = IMG_S(er, eq);
        const f32x2 b1 = {cs1[0], cs1[1]}, bf = {cs1[2], cs1[3]};
#pragma unroll
        for (int hh = 0; hh < 2; ++hh) {
            float vr[8], vi[8], wr8[8], wi8[8];
#pragma unroll
            for (int e = 0; e < 8; ++e) {
                const int m = 2 * hh + (e >> 2), q = e & 3;
                h[0] = cfma(a1, h[0], (f32x2){acc[m][7][q], acc[m][8][q]});
                h[1] = cfma(b1, h[1], (f32x2){ur[m][q], ui[m][q]});
                const float g2 = bflo(gt[m][q]);
                const f32x2 s0 = cmul(cf, h[0]), s1 = cmul(bf, h[1]);
                vr[e] = s0.x * g2; vi[e] = -s0.y * g2; wr8[e] = s1.x * g2; wi8[e] = -s1.y * g2;
            }
            ST16(ls + 40 * A2S + hh * 512, PK8(vr)); ST16(ls + 40 * A2S + hh * 512 + 128, PK8(wr8));
            ST16(ls + hh * 512, PK8(vi)); ST16(ls + hh * 512 + 128, PK8(wi8));
        }
    }
    STAMP(0, 6);
    int ty = threadIdx.x; asm volatile("" : "+v"(ty) :: "memory");
    const int ln = ty & 63, fr2 = ty & 15, fq2 = (ty >> 4) & 3;
    const int wn = wid & 1, kk = wid >> 1;
    LAS char* rH = lds + kk * 8192 + (ln >> 5) * 4096 + (ln & 31) * 8;
    LAS char* rC = lds + 16384 + (wn * 64 + fr2) * 128 + (((4 * kk + fq2) ^ (fr2 & 6)) * 16);
    LAS char* rWb = lds + 16384 + kk * 8192 + (ln >> 5) * 4096 + wn * 1024 + ((ln >> 2) & 7) * 128 + (ln & 3) * 8;
    const int rsw = (ln >> 3) & 3;
    f32x4 ac2[4][8];
    STAMP(0, 7);
    FSTAMP(32);
    FDUMP();
    s16x4 HX[8][2], HY[8][2], WX[4][2], WY[4][2];
    int sl = 0;

#define RD1(RD, i, H, W) do { \
        if ((i) < 8) { \
            if (RD == 1) W[(i) >> 1][(i) & 1] = tr4(rWb + sl + ((((i) >> 1) ^ rsw) * 32) + ((i) & 1) * 2048); \
            else if (((i) & 1) == 0) { const s16x8 v_ = *(LAS s16x8*)(rC + sl + ((i) >> 1) * 2048); \
                W[(i) >> 1][0] = __builtin_shufflevector(v_, v_, 0, 1, 2, 3); W[(i) >> 1][1] = __builtin_shufflevector(v_, v_, 4, 5, 6, 7); } \
        } else H[((i) - 8) >> 1][(i) & 1] = tr4(rH + sl + (((i) - 8) >> 1) * 256 + ((i) & 1) * 2048); \
    } while (0)
#define MM(i, HC, WC) mfma_acc(ac2[(i) & 3][(i) >> 2], cat8(WC[(i) & 3][0], WC[(i) & 3][1]), cat8(HC[(i) >> 2][0], HC[(i) >> 2][1]))
#define STEP(RD, HC, WC, HN, WN) do { \
        _Pragma("unroll") for (int i = 0; i < 32; ++i) { MM(i, HC, WC); if (i < 24) RD1(RD, i, HN, WN); } \
        sl = sl == 4 * 32768 ? 0 : sl + 32768; LGKM0(); \
    } while (0)

    LGKM0();
    BAR();
    STAMP_TAKE(st9);
#pragma unroll
    for (int i = 0; i < 24; ++i) RD1(1, i, HX, WX);
    sl = 32768; LGKM0();
    BAR();
#pragma unroll
    for (int i = 0; i < 32; ++i) {
        asm volatile("v_mfma_f32_16x16x32_bf16 %0, %1, %2, 0" : "=&v"(ac2[i & 3][i >> 2]) : "v"(cat8(WX[i & 3][0], WX[i & 3][1])), "v"(cat8(HX[i >> 2][0], HX[i >> 2][1])) : "memory");
        if (i < 24) RD1(1, i, HY, WY);
    }
    sl = 2 * 32768; LGKM0();
    BAR(); STEP(1, HY, WY, HX, WX);
    const int nf = late ? 15 : 11;
#pragma nounroll
    for (int i = 1; i < nf; ++i) {
        if (i == 3) WAIT_V(0);
        BAR(); STEP(1, HX, WX, HY, WY); BAR(); STEP(1, HY, WY, HX, WX);
    }
    BAR(); STEP(1, HX, WX, HY, WY); BAR(); STEP(0, HY, WY, HX, WX);
#pragma nounroll
    for (int i = late ? -SHIFT / 2 : SHIFT / 2; i < 7; ++i) { BAR(); STEP(0, HX, WX, HY, WY); BAR(); STEP(0, HY, WY, HX, WX); }
    BAR(); STEP(0, HX, WX, HY, WY);
    {
        const int sw = rs0 + wid;
        LAS char* dmp = lds + (sw >= 5 ? sw - 5 : sw) * 32768 + fq2 * 256;
#define DUMP(i) *(LAS f32x4*)(dmp + (i) * 1024 + ((fr2 ^ (4 * ((i) & 3) + fq2)) * 16)) = ac2[(i) & 3][(i) >> 2]
#pragma unroll
        for (int i = 0; i < 32; ++i) { MM(i, HY, WY); if (i >= 12) DUMP(i - 12); }
        asm volatile("s_nop 15" ::: "memory");
#pragma unroll
        for (int i = 20; i < 32; ++i) DUMP(i);
    }
    WAIT_L0();
    STAMP_PUT(1, 0, st0); STAMP_PUT(1, 1, st9);
    STAMP(1, 2);
    BAR();
    asm volatile(".Lk1_end_b:");
}

extern "C" void kernel_launch(void* const* d_in, const int* in_sizes, int n_in, void* d_out, int out_size, void* d_ws, size_t ws_size,
                              hipStream_t stream) {
    const u16* x = (const u16*)d_in[0];
    const u16* lre = (const u16*)d_in[1];
    const u16* lim = (const u16*)d_in[2];
    const u16* Bre = (const u16*)d_in[3];
    const u16* Bim = (const u16*)d_in[4];
    const u16* Cre = (const u16*)d_in[5];
    const u16* Cim = (const u16*)d_in[6];
    const u16* logdt = (const u16*)d_in[7];
    const u16* dskip = (const u16*)d_in[8];
    const u16* Wg = (const u16*)d_in[10];
    const u16* Wu = (const u16*)d_in[11];
    const u16* Wd = (const u16*)d_in[12];
    const u16* gw = (const u16*)d_in[13];
    char* ws = (char*)d_ws;
    (void)hipFuncSetAttribute((const void*)k1_fused, hipFuncAttributeMaxDynamicSharedMemorySize, K2_LDS);
    k1_fused<<<256, 512, K2_LDS, stream>>>(x, Wg, Wu, Bre, Bim, lre, lim, logdt, gw, Cre, Cim, Wd, dskip, ws, (float*)d_out);
}
```

```cpp
#include <hip/hip_runtime.h>
#include <hip/hip_bf16.h>


typedef unsigned short u16;
typedef __bf16 bf16x8 __attribute__((ext_vector_type(8)));
typedef __bf16 bf16x2 __attribute__((ext_vector_type(2)));
typedef short s16x4 __attribute__((ext_vector_type(4)));
typedef short s16x8 __attribute__((ext_vector_type(8)));
typedef float f32x4 __attribute__((ext_vector_type(4)));
typedef float f32x2 __attribute__((ext_vector_type(2)));
typedef unsigned u32x4 __attribute__((ext_vector_type(4)));
typedef unsigned u32x2 __attribute__((ext_vector_type(2)));
#define LAS __attribute__((address_space(3)))
#define GAS __attribute__((address_space(1)))
typedef GAS unsigned gu32;
typedef GAS unsigned long long gu64;
typedef __attribute__((address_space(4))) const unsigned long long* kptr;
#define RLX_AGENT __ATOMIC_RELAXED, __HIP_MEMORY_SCOPE_AGENT

constexpr int SEQ = 2048, DM = 1024, NP = 1024, FF = 3584;
constexpr int KSTEPS2 = (2 * NP + FF) / 64;

constexpr unsigned WS_CARRY = 0;
constexpr unsigned WS_G = 7 * 8192;
constexpr unsigned WS_RDY = 65536;
constexpr unsigned WS_FLAG2 = WS_RDY + 1024;
constexpr unsigned WS_HQ = WS_FLAG2 + 16384;
constexpr unsigned WS_HA = WS_HQ + 1024;
constexpr unsigned WS_HR = WS_HA + 1024;
constexpr unsigned WS_SYNC_BYTES = WS_HR + 2048;
constexpr size_t WS_A2 = 1 << 20;
constexpr unsigned A2S = 262144 + 128;
constexpr size_t WS_A2_BYTES = (size_t)KSTEPS2 * A2S;

__device__ __forceinline__ float bflo(unsigned v) { return __uint_as_float(v << 16); }
__device__ __forceinline__ float bfhi(unsigned v) { return __uint_as_float(v & 0xffff0000u); }
__device__ __forceinline__ float rbf(float f) { return (float)(__bf16)f; }
__device__ __forceinline__ unsigned pk2(float a, float b) {
    bf16x2 v; v.x = (__bf16)a; v.y = (__bf16)b; return __builtin_bit_cast(unsigned, v);
}
__device__ __forceinline__ float relu(float v) { return __int_as_float(max(__float_as_int(v), 0)); }
__device__ __forceinline__ float dot2(unsigned a, unsigned b, float c) {
    return __builtin_amdgcn_fdot2_f32_bf16(__builtin_bit_cast(bf16x2, a), __builtin_bit_cast(bf16x2, b), c, false);
}

#define WAIT_V(n) asm volatile("s_waitcnt vmcnt(" #n ")" ::: "memory")
#define WAIT_L0() asm volatile("s_waitcnt lgkmcnt(0)" ::: "memory")
#define LGKM0() __builtin_amdgcn_s_waitcnt(0xC07F)
#define BAR() do { asm volatile("" ::: "memory"); __builtin_amdgcn_s_barrier(); asm volatile("" ::: "memory"); } while (0)
#define SCHED() __builtin_amdgcn_sched_barrier(0)
#define SRD(p, bytes) __builtin_amdgcn_make_buffer_rsrc((void*)(p), (short)0, (int)(bytes), 0x00020000)

__device__ __forceinline__ void dma16(unsigned m0v, unsigned voff, __amdgpu_buffer_rsrc_t srd, unsigned soff) {
    asm volatile("s_mov_b32 m0, %0\n\ts_nop 0\n\tbuffer_load_dwordx4 %1, %2, %3 offen lds" ::"s"(m0v), "v"(voff), "s"(srd), "s"(soff) : "m0", "memory");
}
__device__ __forceinline__ bf16x8 lds_b128(LAS char* p) { return __builtin_bit_cast(bf16x8, *(LAS s16x8*)p); }
__device__ __forceinline__ bf16x8 lds_tr(LAS char* p0, LAS char* p1) {
    s16x4 a = __builtin_amdgcn_ds_read_tr16_b64_v4i16((LAS s16x4*)p0);
    s16x4 b = __builtin_amdgcn_ds_read_tr16_b64_v4i16((LAS s16x4*)p1);
    return __builtin_bit_cast(bf16x8, __builtin_shufflevector(a, b, 0, 1, 2, 3, 4, 5, 6, 7));
}
__device__ __forceinline__ f32x2 cmul(f32x2 a, f32x2 b) { return (f32x2){__builtin_fmaf(a.x, b.x, -(a.y * b.y)), __builtin_fmaf(a.x, b.y, a.y * b.x)}; }
__device__ __forceinline__ f32x2 cfma(f32x2 a, f32x2 b, f32x2 c) {
    return (f32x2){__builtin_fmaf(a.x, b.x, __builtin_fmaf(-a.y, b.y, c.x)), __builtin_fmaf(a.x, b.y, __builtin_fmaf(a.y, b.x, c.y))};
}
__device__ __forceinline__ f32x2 sel4(int i, f32x2 a, f32x2 b, f32x2 c, f32x2 d) {
    const bool o = i & 1, u = i & 2;
    const float lx = o ? b.x : a.x, ly = o ? b.y : a.y, hx = o ? d.x : c.x, hy = o ? d.y : c.y;
    return (f32x2){u ? hx : lx, u ? hy : ly};
}

constexpr int K2_LDS = 5 * 32768;
constexpr int SHIFT = 2;
constexpr int PF_D0 = 4;

__device__ __forceinline__ void dma4c(unsigned m0v, unsigned voff, __amdgpu_buffer_rsrc_t srd, unsigned soff) {
    asm volatile("s_mov_b32 m0, %0\n\ts_nop 0\n\tbuffer_load_dwordx4 %1, %2, %3 offen sc1 lds\n\tbuffer_load_dwordx4 %1, %2, %3 offen offset:1024 sc1 lds\n\t"
                 "buffer_load_dwordx4 %1, %2, %3 offen offset:2048 sc1 lds\n\tbuffer_load_dwordx4 %1, %2, %3 offen offset:3072 sc1 lds" ::"s"(m0v), "v"(voff), "s"(srd), "s"(soff) : "m0", "memory");
}
__device__ __forceinline__ void dma4(unsigned m0v, unsigned voff, __amdgpu_buffer_rsrc_t srd, unsigned s0, unsigned s1, unsigned s2, unsigned s3) {
    asm volatile("s_mov_b32 m0, %0\n\ts_nop 0\n\tbuffer_load_dwordx4 %1, %2, %3 offen lds\n\tbuffer_load_dwordx4 %1, %2, %4 offen offset:1024 lds\n\t"
                 "buffer_load_dwordx4 %1, %2, %5 offen offset:2048 lds\n\tbuffer_load_dwordx4 %1, %2, %6 offen offset:3072 lds" ::"s"(m0v), "v"(voff), "s"(srd), "s"(s0), "s"(s1), "s"(s2), "s"(s3) : "m0", "memory");
}
__device__ __forceinline__ void mfma_acc(f32x4& c, bf16x8 a, bf16x8 b) {
    asm volatile("v_mfma_f32_16x16x32_bf16 %0, %1, %2, %0" : "+v"(c) : "v"(a), "v"(b) : "memory");
}
__device__ __forceinline__ void mfma_acc2(f32x4& c, bf16x8 a0, bf16x8 b0, bf16x8 a1, bf16x8 b1) {
    asm volatile("v_mfma_f32_16x16x32_bf16 %0, %1, %2, %0\n\tv_mfma_f32_16x16x32_bf16 %0, %3, %4, %0" : "+v"(c) : "v"(a0), "v"(b0), "v"(a1), "v"(b1) : "memory");
}
__device__ __forceinline__ s16x4 tr4(LAS char* p) { return __builtin_amdgcn_ds_read_tr16_b64_v4i16((LAS s16x4*)p); }
__device__ __forceinline__ bf16x8 cat8(s16x4 a, s16x4 b) { return __builtin_bit_cast(bf16x8, __builtin_shufflevector(a, b, 0, 1, 2, 3, 4, 5, 6, 7)); }

constexpr int K1_ASB = 40960;
constexpr int K1_W = 81920;
constexpr int K1_WBUF = 28704, K1_UOFF = 14352;
constexpr int K1_ETAB = K1_W + 2 * K1_WBUF;
constexpr int K1_CTAB = K1_ETAB + 32 * 144;
constexpr int K1_SINK = K1_CTAB + 32 * 96;
constexpr int K1_GTAB = K1_SINK + 1024;
constexpr int K1_HS = K1_GTAB + 1024;

__global__ __launch_bounds__(512, 2) void k1_fused(const u16* __restrict__ x, const u16* __restrict__ Wg, const u16* __restrict__ Wu,
                                                       const u16* __restrict__ Bre, const u16* __restrict__ Bim, const u16* __restrict__ lre,
                                                       const u16* __restrict__ lim, const u16* __restrict__ logdt, const u16* __restrict__ gw,
                                                       const u16* __restrict__ Cre, const u16* __restrict__ Cim, const u16* __restrict__ Wd,
                                                       const u16* __restrict__ dskip, char* __restrict__ ws, float* __restrict__ out) {
    extern __shared__ __attribute__((aligned(16))) char smem[];
    LAS char* lds = (LAS char*)smem;
    const unsigned ldsb = (unsigned)(size_t)lds;
    const int tid = threadIdx.x, lane = tid & 63;
    const int wid = __builtin_amdgcn_readfirstlane(tid >> 6);
    const int wr = wid & 3, wc = wid >> 2;
    const int bid = blockIdx.x, idx = bid >> 3, j = (bid & 7) * 4 + (idx & 3), pm = idx >> 2;
    const int fr = lane & 15, fq = lane >> 4;
#define ROLE(b_, i_) const int xc = (b_) & 7, late = (xc & 1), mb2 = (xc >> 2) * 8 + ((i_) >> 2), nblk = ((xc >> 1) & 1) * 4 + ((i_) & 3), tile = mb2 * 8 + nblk
    const char* code0; unsigned codeA, codeB, codeM;
    asm volatile("s_getpc_b64 %0\n.Lk1_ref:\n\ts_mov_b32 %1, .Lk1_end_a-.Lk1_ref\n\ts_mov_b32 %2, .Lk1_end_b-.Lk1_ref\n\ts_mov_b32 %3, .Lk1_mid-.Lk1_ref"
                 : "=s"(code0), "=s"(codeA), "=s"(codeB), "=s"(codeM));
    const __amdgpu_buffer_rsrc_t sI = SRD(code0, (codeA > codeB ? codeA : codeB) + 192);
    const unsigned mSink = ldsb + K1_SINK;
    const bool pf = wid == 7;
    if (pf) { dma16(mSink, (unsigned)(lane * 128), sI, 0u); dma16(mSink, (unsigned)(lane * 128), sI, 8192u); }

    const int ch = 32 * j + 8 * (fq & 1) + 4 * (fr >> 3) + 16 * ((fr >> 2) & 1) + (fr & 3);
    const int grow = pm * 256 + j * 8 + wid;
    u32x4 xa, xb, ga, gb; unsigned plr, pli, pld;
    {
        const u16* xr = x + (size_t)grow * DM;
        const unsigned l16 = lane * 16, c2 = ch * 2;
        asm volatile("s_nop 4\n\tglobal_load_dwordx4 %0, %7, %9\n\tglobal_load_dwordx4 %1, %7, %9 offset:1024\n\t"
                     "global_load_dwordx4 %2, %7, %10\n\tglobal_load_dwordx4 %3, %7, %10 offset:1024\n\t"
                     "global_load_ushort %4, %8, %11\n\tglobal_load_ushort %5, %8, %12\n\tglobal_load_ushort %6, %8, %13"
                     : "=&v"(xa), "=&v"(xb), "=&v"(ga), "=&v"(gb), "=&v"(plr), "=&v"(pli), "=&v"(pld)
                     : "v"(l16), "v"(c2), "s"(xr), "s"(gw), "s"(lre), "s"(lim), "s"(logdt) : "memory");
    }

    const int q4 = wid + 28, q5 = wid + 35;
    const __amdgpu_buffer_rsrc_t sX = SRD(x, SEQ * DM * 2), sG = SRD(Wg, DM * FF * 2), sU = SRD(Wu, DM * FF * 2);
    const __amdgpu_buffer_rsrc_t s4 = SRD(q4 < 32 ? x : (q4 & 2) ? Bim : Bre, q4 < 32 ? SEQ * DM * 2 : NP * DM * 2);
    const __amdgpu_buffer_rsrc_t s5 = SRD((q5 & 2) ? Bim : Bre, q5 < 40 ? NP * DM * 2 : 0);
    const int l3 = lane >> 3;
    const unsigned voffX = (unsigned)(((l3 >> 2) * 16 + (l3 & 3)) * 2048 + (((lane & 7) ^ (l3 & 6)) * 16));
    unsigned voffW;
    { const int rho = (tid * 4682) >> 16, c = tid - rho * 14, k = (rho & 0x13) | ((rho & 4) << 1) | ((rho & 8) >> 1);
      voffW = (unsigned)(k * (FF * 2) + c * 16); }
#define XROW(q) (pm * 256 + ((q) >> 3) * 64 + (((q) >> 2) & 1) * 8 + ((q) & 1) * 32 + (((q) >> 1) & 1) * 4)
#define BROW(q) (32 * j + 8 * (((q) >> 2) & 1) + 4 * ((q) & 1))
    constexpr unsigned WST = 64 * FF * 2, WSH = 32 * FF * 2;
    const unsigned so0 = (unsigned)(XROW(wid) * 2048), so1 = (unsigned)(XROW(wid + 7) * 2048), so2 = (unsigned)(XROW(wid + 14) * 2048), so3 = (unsigned)(XROW(wid + 21) * 2048),
                   so4 = (unsigned)((q4 < 32 ? XROW(q4) : BROW(q4)) * 2048), so5 = (unsigned)(BROW(q5) * 2048);
    const unsigned soW = (unsigned)(j * 224), mb0 = ldsb + wid * 1024;
    const unsigned m50 = q5 < 40 ? mb0 + 35 * 1024 : mSink, m51 = q5 < 40 ? mb0 + 35 * 1024 + K1_ASB : mSink;
#define ST_X(i, ko, b) dma16(mb0 + (b) * K1_ASB + (i) * 7 * 1024, voffX, sX, ((i) == 0 ? so0 : (i) == 1 ? so1 : (i) == 2 ? so2 : so3) + (ko))
#define ST_S4(ko, b) dma16(mb0 + (b) * K1_ASB + 28 * 1024, voffX, s4, so4 + (ko))
#define ST_S5(ko, b) dma16((b) ? m51 : m50, voffX, s5, so5 + (ko))
#define ST_G(h, t, b) dma16(mb0 + K1_W + (b) * K1_WBUF + (h) * 7168, voffW, sG, soW + (t) * WST + (h) * WSH)
#define ST_U(h, t, b) dma16(mb0 + K1_W + (b) * K1_WBUF + K1_UOFF + (h) * 7168, voffW, sU, soW + (t) * WST + (h) * WSH)
    if (!pf) { ST_X(0, 0, 0); ST_X(1, 0, 0); ST_X(2, 0, 0); ST_X(3, 0, 0); ST_S4(0, 0); ST_S5(0, 0); ST_G(0, 0, 0); ST_G(1, 0, 0); ST_U(0, 0, 0); ST_U(1, 0, 0); }

    const int ch0 = (fq ^ (fr & 6)) * 16;
    LAS char* rA0 = lds + wr * 8192 + fr * 128 + ch0;
    LAS char* rA1 = lds + wr * 8192 + fr * 128 + (ch0 ^ 64);
    LAS char* rS0 = lds + 32768 + wc * 4096 + fr * 128 + ch0;
    LAS char* rS1 = lds + 32768 + wc * 4096 + fr * 128 + (ch0 ^ 64);
    const int trow = (16 * (fq >> 1) + 4 * (fq & 1) + ((lane & 15) >> 2)) * 224;
    LAS char* rW = lds + K1_W + trow + wc * 96 + (lane & 3) * 8;
    LAS char* rM = lds + K1_W + trow + 192 + wc * 16 + (lane & 1) * 8 + ((lane >> 1) & 1) * K1_UOFF;

    f32x4 acc[4][9];
#pragma unroll
    for (int m = 0; m < 4; ++m)
#pragma unroll
        for (int n = 0; n < 9; ++n) { acc[m][n] = (f32x4){0.f, 0.f, 0.f, 0.f}; asm volatile("" : "+v"(acc[m][n])); }
    bf16x8 Af[4][2], Bf[3][2];

#define RD_A(m, b) do { Af[m][0] = lds_b128(rA0 + (b) * K1_ASB + (m) * 2048); Af[m][1] = lds_b128(rA1 + (b) * K1_ASB + (m) * 2048); } while (0)
#define RD_S(slot, ri, b) do { Bf[slot][0] = lds_b128(rS0 + (b) * K1_ASB + (ri) * 2048); Bf[slot][1] = lds_b128(rS1 + (b) * K1_ASB + (ri) * 2048); } while (0)
#define RD_W(slot, base, off) do { Bf[slot][0] = lds_tr((base) + (off), (base) + (off) + 1792); Bf[slot][1] = lds_tr((base) + (off) + 7168, (base) + (off) + 7168 + 1792); } while (0)
#define SEC1(b, d0, d1) do { RD_S(0, 0, b); RD_S(1, 1, b); d0; RD_W(2, rW, (b) * K1_WBUF); RD_A(0, b); d1; RD_A(1, b); RD_A(2, b); RD_A(3, b); } while (0)
#define SEC2(b, d0, d1, d2, d3) do { d0; RD_W(0, rW, (b) * K1_WBUF + 32); d1; RD_W(1, rW, (b) * K1_WBUF + 64); d2; RD_W(2, rM, (b) * K1_WBUF); d3; } while (0)
#define SEC3(b, d0, d1, d2, d3) do { d0; RD_W(0, rW, (b) * K1_WBUF + K1_UOFF); d1; RD_W(1, rW, (b) * K1_WBUF + K1_UOFF + 32); d2; RD_W(2, rW, (b) * K1_WBUF + K1_UOFF + 64); d3; } while (0)
#define GO(n0, n1, n2) do { asm volatile("" ::: "memory"); LGKM0(); BAR(); SCHED(); \
        _Pragma("unroll") for (int m = 0; m < 4; ++m) { \
            mfma_acc2(acc[m][n0], Af[m][0], Bf[0][0], Af[m][1], Bf[0][1]); mfma_acc2(acc[m][n1], Af[m][0], Bf[1][0], Af[m][1], Bf[1][1]); \
            mfma_acc2(acc[m][n2], Af[m][0], Bf[2][0], Af[m][1], Bf[2][1]); } \
        SCHED(); } while (0)

#define RELANE() int tz = threadIdx.x; asm volatile("" : "+v"(tz)); const int er = tz & 15, eq = (tz >> 4) & 3
    asm volatile("" : "+v"(rA0), "+v"(rA1), "+v"(rS0), "+v"(rS1), "+v"(rW), "+v"(rM));
    WAIT_V(10);
    if (pf) WAIT_V(0);
    asm volatile("" : "+v"(xa), "+v"(xb), "+v"(ga), "+v"(gb), "+v"(plr), "+v"(pli), "+v"(pld) :: "memory");
    {
        float s = 0.f, s2 = 0.f;
#pragma unroll
        for (int e = 0; e < 4; ++e) { s = dot2(xa[e], ga[e], s); s2 = dot2(xb[e], gb[e], s2); }
        s += s2;
#define DPP_ADD(ctrl, rmask) s += __int_as_float(__builtin_amdgcn_update_dpp(0, __float_as_int(s), ctrl, rmask, 0xf, true))
        DPP_ADD(0x111, 0xf); DPP_ADD(0x112, 0xf); DPP_ADD(0x114, 0xf); DPP_ADD(0x118, 0xf); DPP_ADD(0x142, 0xa); DPP_ADD(0x143, 0xc);
        const float g = rbf(__builtin_amdgcn_rcpf(1.f + __builtin_amdgcn_exp2f(-1.4426950408889634f * rbf(s))));
        if (lane == 63) __hip_atomic_store((gu32*)(ws + WS_G) + grow, pk2(g, 1.f - g), RLX_AGENT);
    }
    if (!pf) {
        BAR();
        ST_X(0, 128, 1); ST_X(1, 128, 1); ST_X(2, 128, 1); ST_X(3, 128, 1); ST_S4(128, 1); ST_S5(128, 1); ST_G(0, 1, 1); ST_G(1, 1, 1);
        WAIT_V(11);
        if (wc == 1) BAR();
#pragma nounroll
        for (int t = 0; t < 16; t += 2) {
            asm volatile("" : "+s"(t));
            const unsigned ko = t == 14 ? 0x80000000u : (unsigned)(t + 2) * 128u;
            BAR(); SEC1(0, ST_U(0, t + 1, 1), ST_U(1, t + 1, 1)); WAIT_V(10);
            GO(7, 8, 0);
            BAR(); SEC2(0, ST_X(0, ko, 0), ST_X(1, ko, 0), ST_X(2, ko, 0), ST_X(3, ko, 0));
            GO(1, 2, 6);
            BAR(); SEC3(0, ST_G(0, t + 2, 0), ST_G(1, t + 2, 0), ST_S4(ko, 0), ST_S5(ko, 0)); WAIT_V(10);
            GO(3, 4, 5);
            BAR(); SEC1(1, ST_U(0, t + 2, 0), ST_U(1, t + 2, 0)); WAIT_V(10);
            GO(7, 8, 0);
            BAR(); SEC2(1, ST_X(0, ko + 128, 1), ST_X(1, ko + 128, 1), ST_X(2, ko + 128, 1), ST_X(3, ko + 128, 1));
            GO(1, 2, 6);
            BAR(); SEC3(1, ST_G(0, t + 3, 1), ST_G(1, t + 3, 1), ST_S4(ko + 128, 1), ST_S5(ko + 128, 1)); WAIT_V(10);
            GO(3, 4, 5);
        }
    }
    int w7 = wid; asm volatile("" : "+s"(w7));
    if (w7 == 7) {
        {
            const float lr = bflo(plr), li = bflo(pli), dt = rbf(expf(bflo(pld)));
            const float e = expf(dt * lr); float sn, cs; sincosf(dt * li, &sn, &cs);
            const f32x2 a1 = {e * cs, e * sn};
            const float nr = a1.x - 1.f, den = lr * lr + li * li;
            f32x2 a16 = cmul(a1, a1); a16 = cmul(a16, a16); a16 = cmul(a16, a16); a16 = cmul(a16, a16);
            const f32x2 a32 = cmul(a16, a16), a48 = cmul(a32, a16), a64 = cmul(a32, a32), a128 = cmul(a64, a64), a192 = cmul(a128, a64), a256 = cmul(a128, a128);
            if (lane < 32) {
                LAS f32x4* ct = (LAS f32x4*)(lds + K1_CTAB + lane * 96);
                ct[0] = (f32x4){a1.x, a1.y, 2.f * ((nr * lr + a1.y * li) / den), 2.f * ((a1.y * lr - nr * li) / den)};
                ct[1] = (f32x4){1.f, 0.f, a16.x, a16.y}; ct[2] = (f32x4){a32.x, a32.y, a48.x, a48.y};
                ct[3] = (f32x4){1.f, 0.f, a64.x, a64.y}; ct[4] = (f32x4){a128.x, a128.y, a192.x, a192.y};
                ct[5] = (f32x4){a256.x, a256.y, 0.f, 0.f};
            }
        }
        const int pb = (bid >> 3) | ((bid & 7) << 5);
        const __amdgpu_buffer_rsrc_t sPW = SRD((pb & 1) ? Wg : Wu, DM * FF * 2), sPD = SRD((pb & 1) ? Wd : (pb & 2) ? Cim : Cre, (pb & 1) ? DM * FF * 2 : DM * NP * 2);
        const __amdgpu_buffer_rsrc_t sH = SRD(ws, WS_SYNC_BYTES);
        const unsigned l128 = lane * 128, OUT = 0x80000000u;
        const unsigned pds = (pb & 1) ? (1u << 20) : (1u << 19), pw0 = (unsigned)((pb >> 1) * 8192), pd0 = (unsigned)(((pb & 1) ? pb >> 1 : pb >> 2) * 8192);
#define PF_W(r) dma16(mSink, l128, sPW, pw0 + (unsigned)(r) * (1u << 20))
#define PF_D(r) dma16(mSink, l128, sPD, pd0 + (unsigned)(r) * pds)
#define RFL(v) ((unsigned)__builtin_amdgcn_readfirstlane(v))
#define HDMA(m, vo, srd, so) asm volatile("s_nop 4\n\ts_mov_b32 m0, %0\n\ts_nop 0\n\tbuffer_load_dwordx4 %1, %2, %3 offen sc1 lds" ::"s"((unsigned)(m)), "v"(vo), "s"(srd), "s"((unsigned)(so)) : "m0", "memory")
#define HLD(op, v, vo) asm volatile("s_nop 4\n\t" op " %0, %1, %2, 0 offen sc1\n\ts_waitcnt vmcnt(0)" : "=v"(v) : "v"(vo), "s"(sH) : "memory")
#define HST32(off, v) do { __builtin_amdgcn_raw_buffer_store_b32((unsigned)(v), sH, off, 0, 16); asm volatile("" ::: "memory"); } while (0)
#define HST64(off, lo_, hi2_) do { __builtin_amdgcn_raw_buffer_store_b64((u32x2){(unsigned)(lo_), (unsigned)(hi2_)}, sH, off, 0, 16); asm volatile("" ::: "memory"); } while (0)
#define REPLY(q_) do { _Pragma("unroll") for (int i = 0; i < 4; ++i) HST64(WS_HR + l16 * 2 + i * 8, (q_)[i], tag); } while (0)
#define LANE0(off) (l16 == 0 ? (unsigned)(off) : OUT)
#define ALLANS(a_) __all((a_)[0] == tag && (a_)[1] == tag && (a_)[2] == tag && (a_)[3] == tag)
#define ATX(cond, ...) do { int t_ = t; asm volatile("" : "+s"(t_)); if (cond) { unsigned l16 = l128; asm volatile("" : "+v"(l16)); l16 >>= 3; __VA_ARGS__ } } while (0)
#define AT(v, ...) ATX(t_ == (v), __VA_ARGS__)
#define AT2(v, ...) ATX((unsigned)(t_ - (v)) <= 2u, __VA_ARGS__)
        unsigned ask = 0, tag = 0, done = 0;
        const int boss = bid == 0 ? 0 : 64;
        BAR();
        HDMA(ldsb + K1_HS + 1024, l128 >> 3, sH, WS_HR + (bid >> 7) * 1024);
        HDMA(ldsb + K1_HS, l128 >> 3, sH, bid == 0 ? WS_HA : OUT);
        PF_W(1);
        BAR();
#pragma nounroll
        for (int t = 0; t < 16; t += 2) {
            asm volatile("" : "+s"(t));
            BAR(); SEC1(0, , );
            AT(10,
                HDMA(mSink, l128, sI, codeM); HDMA(mSink, l128, sI, codeM + 8192); HDMA(mSink, l128, sI, codeM + 16384););
            AT(12, HDMA(ldsb + K1_HS, l16, sH, WS_HA););
            GO(7, 8, 0);
            BAR(); SEC2(0, , , , );
            AT(0,
                ROLE(bid, idx);
                HST64((pm < 7 ? WS_CARRY + pm * 8192 + j * 256 : OUT) + (l16 < 32 * 16 ? l16 >> 1 : OUT >> 1), ~0u, ~0u);
                HST32(l16 == 4 * 16 ? WS_RDY + (pm * 32 + j) * 4 : (late ? OUT : WS_FLAG2 + tile * 128) + (l16 < 4 * 16 ? l16 * 2 : OUT >> 1), ~0u););
            AT(2,
                const __amdgpu_buffer_rsrc_t sA = SRD(ws + WS_A2, WS_A2_BYTES), sO = SRD(out, SEQ * DM * 4);
                const unsigned vo = (l16 >> 4) * A2S, vo2 = l16 << 13;
                HDMA(mSink, vo, sA, A2S - 128); HDMA(mSink, vo, sA, 65 * A2S - 128); HDMA(mSink, vo2, sO, 0u););
            AT2(4 + boss, WAIT_V(2); const u32x4 q = *(LAS u32x4*)(lds + K1_HS + l16); REPLY(q););
            AT2(6, HDMA(ldsb + K1_HS + 1024, l16, sH, WS_HR + (bid >> 7) * 1024););
            GO(1, 2, 6);
            BAR(); SEC3(0, , PF_W(t + 2), PF_D(t - PF_D0), );
            GO(3, 4, 5);
            BAR(); SEC1(1, , );
            AT2(2 + boss, HDMA(ldsb + K1_HS, l16, sH, WS_HQ););
            AT(14, WAIT_V(0););
            GO(7, 8, 0);
            BAR(); SEC2(1, , , , );
            AT(0,
                WAIT_V(5);
                ask = RFL(*(LAS unsigned*)(lds + K1_HS + 1024 + (bid & 127) * 8)) + 1u;
                const u32x4 old = *(LAS u32x4*)(lds + K1_HS + l16);
                HST32(LANE0(WS_HQ + bid * 4), ask);
                tag = RFL(old[0]) + 1u;
                while (__any(old[0] == tag || old[1] == tag || old[2] == tag || old[3] == tag)) ++tag;);
            AT2(6 + (int)done * 64,
                WAIT_V(2);
                const u32x2 r = *(LAS u32x2*)(lds + K1_HS + 1024 + (bid & 127) * 8);
                if (RFL(r[0]) == ask) { tag = RFL(r[1]); done = 1u; HST32(LANE0(WS_HA + bid * 4), tag); });
            AT(12,
                WAIT_V(2);
                u32x4 ans = *(LAS u32x4*)(lds + K1_HS + l16);
                if (!(done && ALLANS(ans))) {
                    unsigned spins = 0;
                    _Pragma("nounroll") do {
                        if (bid == 0) { u32x4 q; HLD("buffer_load_dwordx4", q, WS_HQ + l16); REPLY(q); }
                        if (!done) {
                            u32x2 r; HLD("buffer_load_dwordx2", r, WS_HR + bid * 8);
                            if (RFL(r[0]) == ask) { tag = RFL(r[1]); done = 1u; HST32(LANE0(WS_HA + bid * 4), tag); }
                        }
                        HLD("buffer_load_dwordx4", ans, WS_HA + l16);
                    } while (!(done && ALLANS(ans)) && ++spins < (1u << 18));
                }
                HDMA(ldsb + K1_GTAB, l16, sH, WS_G + pm * 1024););
            GO(1, 2, 6);
            BAR(); SEC3(1, , PF_W(t + 3), PF_D(t + 1 - PF_D0), );
            GO(3, 4, 5);
        }
    }
    asm volatile(".Lk1_mid:\n\ts_nop 15" ::: "memory"); SCHED();
    if (wc == 0) BAR();
    RELANE();
    kptr ka = (kptr)__builtin_amdgcn_kernarg_segment_ptr(); asm volatile("" : "+s"(ka));
#define KARG(T, i) ((T)ka[i])
    char* const ws2 = KARG(char*, 13);

    LAS char* ctab = lds + K1_CTAB + (wc * 16 + er) * 96;
    LAS char* etab = lds + K1_ETAB + (wc * 16 + er) * 144;
    const f32x4 cst = *(LAS f32x4*)ctab;
    const f32x2 a1 = {cst[0], cst[1]}, cf = {cst[2], cst[3]};
    u32x4 gt[4];
#pragma unroll
    for (int m = 0; m < 4; ++m) gt[m] = *(LAS u32x4*)(lds + K1_GTAB + wr * 256 + eq * 64 + m * 16);
    const __amdgpu_buffer_rsrc_t sA2 = SRD(ws2 + WS_A2, WS_A2_BYTES);
#define ST16(voff, v) __builtin_amdgcn_raw_buffer_store_b128(v, sA2, voff, 0, 16)
#define PK8(v) (u32x4){pk2(v[0], v[1]), pk2(v[2], v[3]), pk2(v[4], v[5]), pk2(v[6], v[7])}
    const int mblk = pm * 2 + (wr >> 1);
#define IMG_Q(r, q) (unsigned)(((q) >> 1) * 256 + ((q) & 1) * 16 + ((r) & 3) * 32 + mblk * 16384 + (wr & 1) * 1024)
#define IMG_T(r, q) (IMG_Q(r, q) + (unsigned)((((r) >> 2) & 1) * 2048 + ((r) >> 3) * 128 + (j >> 2) * A2S + (j & 3) * 4096))
#define IMG_M(r, q) (IMG_Q(r, q) + (unsigned)((((r) >> 2) & 1) * 2048 + ((r) >> 3) * 512 + (24 + (j >> 2)) * A2S + (j & 3) * 4096))
#define IMG_S(r, q) (IMG_Q(r, q) + (unsigned)((((r) >> 2) & 1) * 4096 + ((r) >> 3) * 2048 + (32 + (j >> 1)) * A2S + (j & 1) * 8192))
    const unsigned lo = IMG_T(er, eq);
#define GLU_HALF(n, hh, o) do { float v[8]; \
        _Pragma("unroll") for (int e = 0; e < 8; ++e) { const int m = 2 * (hh) + (e >> 2), q = e & 3; v[e] = relu(acc[m][n][q]) * (acc[m][3 + (n)][q] * bfhi(gt[m][q])); } \
        o = PK8(v); } while (0)
#define GLU_TILE(n) do { u32x4 o_; GLU_HALF(n, 0, o_); ST16(lo + (wc * 48 + 8 * (n)) * A2S, o_); GLU_HALF(n, 1, o_); ST16(lo + (wc * 48 + 8 * (n)) * A2S + 512, o_); } while (0)
    GLU_TILE(0);
    GLU_TILE(1);
    asm volatile("" ::: "memory");
    {
        f32x2 h = {acc[0][7][0], acc[0][8][0]};
#pragma unroll
        for (int e = 1; e < 16; ++e) h = cfma(a1, h, (f32x2){acc[e >> 2][7][e & 3], acc[e >> 2][8][e & 3]});
        *(LAS f32x2*)(etab + (wr * 4 + eq) * 8) = h;
    }
    WAIT_V(0); WAIT_L0();
    BAR();
    gu32* rdy = (gu32*)(ws2 + WS_RDY);
    if (tz == 192) __hip_atomic_store(rdy + pm * 32 + j, 1u, RLX_AGENT);
    gu64* carry = (gu64*)(ws2 + WS_CARRY);
#define CHAN(c, r) (32 * j + 8 * (c) + 4 * ((r) >> 3) + 16 * (((r) >> 2) & 1) + ((r) & 3))
    if (wid < 2 && pm < 7) {
        const f32x2 a16 = *(LAS f32x2*)(lds + K1_CTAB + (wid * 16 + er) * 96 + 24);
        f32x4 E[8];
#pragma unroll
        for (int i = 0; i < 8; ++i) E[i] = *(LAS f32x4*)(lds + K1_ETAB + (wid * 16 + er) * 144 + i * 16);
        f32x2 run = {E[0][0], E[0][1]};
#pragma unroll
        for (int i = 1; i < 16; ++i) run = cfma(a16, run, (f32x2){E[i >> 1][(i & 1) * 2], E[i >> 1][(i & 1) * 2 + 1]});
        unsigned ux = __float_as_uint(run.x), uy = __float_as_uint(run.y);
        if (ux == 0xFFFFFFFFu) ux = 0x7FC00000u;
        if (eq == 0) __hip_atomic_store(carry + pm * 1024 + CHAN(wid, er), ((unsigned long long)uy << 32) | ux, RLX_AGENT);
    }
    GLU_TILE(2);
    {
        float v[8];
        const bool lowh = er < 8;
#pragma unroll
        for (int e = 0; e < 8; ++e) {
            const int m = e >> 2, q = e & 3;
            const float v0 = lowh ? relu(acc[m][6][q]) : acc[m][6][q] * bfhi(gt[m][q]);
            const float v1 = lowh ? relu(acc[2 + m][6][q]) : acc[2 + m][6][q] * bfhi(gt[2 + m][q]);
            const float rcv = __int_as_float(__builtin_amdgcn_mov_dpp(__float_as_int(lowh ? v1 : v0), 0x128, 0xf, 0xf, true));
            v[e] = (lowh ? v0 : v1) * rcv;
        }
        ST16(IMG_M(er, eq) + wc * 128, PK8(v));
    }
    LAS f32x4* park = (LAS f32x4*)(lds + 3 * 32768 + wr * 8192 + (tz & 63) * 16);
    if (wc == 1) {
#pragma unroll
        for (int m = 0; m < 4; ++m) { park[m * 128] = acc[m][7]; park[m * 128 + 64] = acc[m][8]; }
    }
    WAIT_L0();
    BAR();

    ROLE(bid, idx);
    const int n0 = nblk * 128;
    const int rs0 = late ? (48 + SHIFT) % 5 : (40 - SHIFT) % 5;

    if (wc == 1) {
        int ty = threadIdx.x; asm volatile("" : "+v"(ty) :: "memory");
        const int ln = ty & 63, e3 = ln >> 3;
#define LEVEL(lvl) do { if (ty == 256) __hip_atomic_store(rdy + pm * 32 + j, (unsigned)(lvl), RLX_AGENT); } while (0)
        const __amdgpu_buffer_rsrc_t sC = SRD(late ? KARG(const u16*, 10) : KARG(const u16*, 9), DM * NP * 2), sW = SRD(KARG(const u16*, 11), FF * DM * 2);
        const unsigned voffA = (unsigned)(ln * 16);
        const unsigned voffC = (unsigned)(e3 * 2048 + (((ln & 7) ^ (e3 & 6)) * 16));
        const unsigned voffD = (unsigned)(((e3 >> 2) * 8 + (e3 & 3)) * 2048 + ((((ln >> 1) & 3) ^ (e3 >> 1)) * 32) + (ln & 1) * 16);
        const unsigned soA = (unsigned)((late ? 0 : 48) * A2S + mb2 * 16384 + wr * 4096);
        const unsigned soC = (unsigned)((n0 + 32 * wr) * 2048);
        const unsigned soD = (unsigned)(((late ? 0 : 48) + 112 * wr) * 2048 + n0 * 2);
        const unsigned mB = ldsb + wr * 4096;
#define A4(img, sl) dma4c(mB + (sl), voffA, sA2, soA + (img) * A2S)
#define D4(row, sl) do { const unsigned b_ = soD + (row) * 2048; \
            asm volatile("s_mov_b32 m0, %0\n\ts_nop 0\n\tbuffer_load_dwordx4 %1, %2, %3 offen lds\n\tbuffer_load_dwordx4 %1, %2, %4 offen offset:2048 lds\n\t" \
                         "buffer_load_dwordx4 %1, %2, %5 offen offset:3072 lds\n\ts_mov_b32 m0, %6\n\ts_nop 0\n\tbuffer_load_dwordx4 %1, %2, %3 offen offset:128 lds" \
                         ::"s"(mB + (sl) + 16384), "v"(voffD), "s"(sW), "s"(b_), "s"(b_ + 8192 - 2048), "s"(b_ + 8192 + 128 - 3072), "s"(mB + (sl) + 16384 + 1024 - 128) : "m0", "memory"); } while (0)
#define C4(srd, p, sl) do { const unsigned b_ = soC + (p) * 128; dma4(mB + (sl) + 16384, voffC, srd, b_, b_ + 16384 - 1024, b_ + 32768 - 2048, b_ + 49152 - 3072); } while (0)
#define POLL(v, p) asm volatile("global_load_dword %0, %1, off sc1\n\ts_waitcnt vmcnt(0)" : "=v"(v) : "v"(p) : "memory")
#define ASK(v, p) asm volatile("global_load_dword %0, %1, off sc1" : "=v"(v) : "v"(p) : "memory")
#define NEED(v, p, lvl) do { asm volatile("" : "+v"(v) :: "memory"); if (!__all((int)(v) >= (lvl))) { unsigned spins_ = 0, w_; \
            do { __builtin_amdgcn_s_sleep(1); POLL(w_, p); } while (!__all((int)w_ >= (lvl)) && ++spins_ < (1u << 18)); } } while (0)
        gu32* rl = rdy + (mb2 >> 1) * 32 + (ln & 31);
#define XP(k_) (((((k_) & 3) << 1) | (((k_) >> 2) & 1)) ^ (xc & 1))
        unsigned fv;
        D4(448 * XP(0), 0); D4(448 * XP(1), 32768); D4(448 * XP(2), 2 * 32768);
        POLL(fv, rl);
        {
            const int other = ((ln >> 2) ^ xc) & 1;
            unsigned spins = 0;
            while (!__all((int)fv >= 1 || other) && ++spins < (1u << 18)) { __builtin_amdgcn_s_sleep(4); POLL(fv, rl); }
        }
        A4(XP(0), 0); A4(XP(1), 32768); A4(XP(2), 2 * 32768);
        WAIT_V(8); BAR();
        int k = 3, sl = 3 * 32768;
#define NEXT_SLOT() sl = sl == 4 * 32768 ? 0 : sl + 32768
#define STAGE_W() do { const int q_ = k < 16 ? (k & 3) | ((k & 4) << 1) | ((k & 8) >> 1) : k, x_ = XP(q_), row_ = 448 * x_ + ((q_ >> 3) == 3 ? 96 : 16 * (q_ >> 3)); \
            A4((q_ & ~7) + x_, sl); D4(row_, sl); ++k; NEXT_SLOT(); } while (0)
#define LSYNC() do { WAIT_V(16); BAR(); } while (0)
        STAGE_W(); WAIT_V(12); BAR();
        STAGE_W(); LSYNC(); STAGE_W(); LSYNC();
        ASK(fv, rl);
        STAGE_W(); LSYNC(); STAGE_W(); LSYNC();
        NEED(fv, rl, 1);
        STAGE_W(); LSYNC(); STAGE_W(); LSYNC();
        LEVEL(2);
#pragma nounroll
        while (k < 13) { STAGE_W(); LSYNC(); }
        ASK(fv, rl);
        STAGE_W(); LSYNC(); STAGE_W(); LSYNC(); STAGE_W(); LSYNC();
        NEED(fv, rl, 2);
        const int nw = late ? 32 : 24;
#pragma nounroll
        while (k < nw) { STAGE_W(); LSYNC(); }
        const int np = late ? 16 : 16 - SHIFT;
#pragma nounroll
        for (int p = 0; p < np; ++p) { A4(k, sl); C4(sC, p, sl); ++k; NEXT_SLOT(); LSYNC(); }
        if (late) {
            const __amdgpu_buffer_rsrc_t sR = SRD(KARG(const u16*, 9), DM * NP * 2);
#pragma nounroll
            for (int p = 16 - SHIFT; p < 16; ++p) { A4(72 + p, sl); C4(sR, p, sl); NEXT_SLOT(); LSYNC(); }
        }
        const int tb = mb2 * 128 + (wr >> 1) * 64 + (wr & 1) * 8 + (ln >> 5) * 16, dcol = n0 + (ln & 31) * 4;
#define ROW(i) (tb + ((i) >> 3) * 32 + ((i) & 7))
        gu32* flag = (gu32*)(ws2 + WS_FLAG2 + tile * 128 + wr * 32);
        const __amdgpu_buffer_rsrc_t sP = SRD(KARG(float*, 14), SEQ * DM * 4);
#define POFF(i) ((ROW(i) * DM + dcol) * 4)
        const int wn1 = (ln >> 4) & 1, s0 = rs0 + wn1, s1 = rs0 + 2 + wn1;
        const int inp = wr * 8192 + ((ln >> 2) & 3) * 1024 + (ln & 3) * 256, sw = (ln & 15) ^ ((ln >> 5) * 8);
        LAS char* q0 = lds + (s0 >= 5 ? s0 - 5 : s0) * 32768 + inp;
        LAS char* q1 = lds + (s1 >= 5 ? s1 - 5 : s1) * 32768 + inp;
#define SUMS(i) (*(LAS f32x4*)(q0 + ((i) >> 3) * 4096 + ((sw ^ ((i) & 7)) * 16)) + *(LAS f32x4*)(q1 + ((i) >> 3) * 4096 + ((sw ^ ((i) & 7)) * 16)))
        f32x4 ex[16];
        if (late) {
            WAIT_V(8); BAR();
            ASK(fv, flag);
            WAIT_V(0); BAR();
            NEED(fv, flag, 1);
#pragma unroll
            for (int i = 0; i < 16; ++i) ex[i] = __builtin_bit_cast(f32x4, __builtin_amdgcn_raw_buffer_load_b128(sP, POFF(i), 0, 16));
            BAR();
#pragma unroll
            for (int i = 0; i < 16; ++i) __builtin_amdgcn_raw_buffer_store_b128(__builtin_bit_cast(u32x4, ex[i] + SUMS(i)), sP, POFF(i), 0, 16);
        } else {
            const __amdgpu_buffer_rsrc_t sXk = SRD(KARG(const u16*, 0), SEQ * DM * 2), sDk = SRD(KARG(const u16*, 12), DM * 2), sGk = SRD(ws2 + WS_G, 8192);
            WAIT_V(8); BAR();
            u32x2 dk = __builtin_amdgcn_raw_buffer_load_b64(sDk, dcol * 2, 0, 0), xv[16]; u32x4 gwd[4];
#pragma unroll
            for (int i = 0; i < 4; ++i) gwd[i] = __builtin_amdgcn_raw_buffer_load_b128(sGk, ROW(4 * i) * 4, 0, 16);
#pragma unroll
            for (int i = 0; i < 16; ++i) xv[i] = __builtin_amdgcn_raw_buffer_load_b64(sXk, (tb * DM + dcol) * 2 + (i & 1) * 2048, (((i) >> 3) * 32 + ((i) & 6)) * 2048, 0);
            WAIT_V(21); BAR();
            asm volatile("" : "+v"(dk), "+v"(gwd[0]), "+v"(gwd[1]), "+v"(gwd[2]), "+v"(gwd[3]));
#pragma unroll
            for (int i = 0; i < 16; ++i) {
                const float g = bflo(gwd[i >> 2][i & 3]);
                ex[i] = (f32x4){g * rbf(bflo(xv[i][0]) * bflo(dk[0])), g * rbf(bfhi(xv[i][0]) * bfhi(dk[0])),
                                g * rbf(bflo(xv[i][1]) * bflo(dk[1])), g * rbf(bfhi(xv[i][1]) * bfhi(dk[1]))};
                asm volatile("" : "+v"(ex[i]));
            }
            BAR();
#pragma unroll
            for (int i = 0; i < 16; ++i) __builtin_amdgcn_raw_buffer_store_b128(__builtin_bit_cast(u32x4, ex[i] + SUMS(i)), sP, POFF(i), 0, 16);
            WAIT_V(0);
            if (ln == 0) __hip_atomic_store(flag, 1u, RLX_AGENT);
        }
        asm volatile(".Lk1_end_a:");
        return;
    }

    {
        unsigned long long gq[2][7];
#pragma unroll
        for (int c = 0; c < 2; ++c)
#pragma unroll
            for (int q = 0; q < 7; ++q) gq[c][q] = __hip_atomic_load(carry + (q + pm >= 7 ? q + pm - 7 : 0) * 1024 + CHAN(c, er), RLX_AGENT);
        f32x2 h[2];
        {
            f32x2 a16[2], run[2]; f32x4 E[2][6], F0[2]; f32x2 F1[2];
#pragma unroll
            for (int c = 0; c < 2; ++c) {
                a16[c] = *(LAS f32x2*)(ctab + c * (16 * 96) + 24);
#pragma unroll
                for (int i = 0; i < 6; ++i) E[c][i] = *(LAS f32x4*)(etab + c * (16 * 144) + i * 16);
                F0[c] = *(LAS f32x4*)(etab + c * (16 * 144) + wr * 32); F1[c] = *(LAS f32x2*)(etab + c * (16 * 144) + wr * 32 + 16);
                run[c] = (f32x2){0.f, 0.f};
            }
#pragma unroll
            for (int w = 0; w < 3; ++w)
                if (wr > w) {
#pragma unroll
                    for (int i = 0; i < 4; ++i)
#pragma unroll
                        for (int c = 0; c < 2; ++c) run[c] = cfma(a16[c], run[c], (f32x2){E[c][2 * w + (i >> 1)][(i & 1) * 2], E[c][2 * w + (i >> 1)][(i & 1) * 2 + 1]});
                }
#pragma unroll
            for (int c = 0; c < 2; ++c) {
                const f32x2 r1 = cfma(a16[c], run[c], (f32x2){F0[c][0], F0[c][1]}), r2 = cfma(a16[c], r1, (f32x2){F0[c][2], F0[c][3]}), r3 = cfma(a16[c], r2, F1[c]);
                h[c] = sel4(eq, run[c], r1, r2, r3);
            }
        }
        f32x4 ur[4], ui[4];
#pragma unroll
        for (int m = 0; m < 4; ++m) { ur[m] = park[m * 128]; ui[m] = park[m * 128 + 64]; }
        const f32x4 cs1 = *(LAS f32x4*)(ctab + 16 * 96);

        f32x2 a256[2], aq[2];
#pragma unroll
        for (int c = 0; c < 2; ++c) {
            LAS char* ct = ctab + c * (16 * 96);
            a256[c] = *(LAS f32x2*)(ct + 80); aq[c] = cmul(*(LAS f32x2*)(ct + 16 + eq * 8), *(LAS f32x2*)(ct + 48 + wr * 8));
        }
        unsigned mx = 0;
#pragma unroll
        for (int q = 0; q < 7; ++q) mx = max(mx, max((unsigned)gq[0][q], (unsigned)gq[1][q]));
        if (pm > 0 && __any(mx == 0xFFFFFFFFu)) {
            unsigned spins = 0;
            do {
                mx = 0;
#pragma unroll
                for (int c = 0; c < 2; ++c)
#pragma unroll
                    for (int q = 0; q < 7; ++q) {
                        gq[c][q] = __hip_atomic_load(carry + (q + pm >= 7 ? q + pm - 7 : 0) * 1024 + CHAN(c, er), RLX_AGENT);
                        mx = max(mx, (unsigned)gq[c][q]);
                    }
            } while (__any(mx == 0xFFFFFFFFu) && ++spins < (1u << 18));
        }
        f32x2 cg[2] = {{0.f, 0.f}, {0.f, 0.f}};
#pragma unroll
        for (int q = 0; q < 7; ++q) {
            const unsigned keep = q + pm >= 7 ? 0xFFFFFFFFu : 0u;
#pragma unroll
            for (int c = 0; c < 2; ++c) cg[c] = cfma(a256[c], cg[c], (f32x2){__uint_as_float((unsigned)gq[c][q] & keep), __uint_as_float((unsigned)(gq[c][q] >> 32) & keep)});
        }
        h[0] = cfma(aq[0], cg[0], h[0]); h[1] = cfma(aq[1], cg[1], h[1]);
        const unsigned ls = IMG_S(er, eq);
        const f32x2 b1 = {cs1[0], cs1[1]}, bf = {cs1[2], cs1[3]};
#pragma unroll
        for (int hh = 0; hh < 2; ++hh) {
            float vr[8], vi[8], wr8[8], wi8[8];
#pragma unroll
            for (int e = 0; e < 8; ++e) {
                const int m = 2 * hh + (e >> 2), q = e & 3;
                h[0] = cfma(a1, h[0], (f32x2){acc[m][7][q], acc[m][8][q]});
                h[1] = cfma(b1, h[1], (f32x2){ur[m][q], ui[m][q]});
                const float g2 = bflo(gt[m][q]);
                const f32x2 s0 = cmul(cf, h[0]), s1 = cmul(bf, h[1]);
                vr[e] = s0.x * g2; vi[e] = -s0.y * g2; wr8[e] = s1.x * g2; wi8[e] = -s1.y * g2;
            }
            ST16(ls + 40 * A2S + hh * 512, PK8(vr)); ST16(ls + 40 * A2S + hh * 512 + 128, PK8(wr8));
            ST16(ls + hh * 512, PK8(vi)); ST16(ls + hh * 512 + 128, PK8(wi8));
        }
    }
    int ty = threadIdx.x; asm volatile("" : "+v"(ty) :: "memory");
    const int ln = ty & 63, fr2 = ty & 15, fq2 = (ty >> 4) & 3;
    const int wn = wid & 1, kk = wid >> 1;
    LAS char* rH = lds + kk * 8192 + (ln >> 5) * 4096 + (ln & 31) * 8;
    LAS char* rC = lds + 16384 + (wn * 64 + fr2) * 128 + (((4 * kk + fq2) ^ (fr2 & 6)) * 16);
    LAS char* rWb = lds + 16384 + kk * 8192 + (ln >> 5) * 4096 + wn * 1024 + ((ln >> 2) & 7) * 128 + (ln & 3) * 8;
    const int rsw = (ln >> 3) & 3;
    f32x4 ac2[4][8];
    s16x4 HX[8][2], HY[8][2], WX[4][2], WY[4][2];
    int sl = 0;

#define RD1(RD, i, H, W) do { \
        if ((i) < 8) { \
            if (RD == 1) W[(i) >> 1][(i) & 1] = tr4(rWb + sl + ((((i) >> 1) ^ rsw) * 32) + ((i) & 1) * 2048); \
            else if (((i) & 1) == 0) { const s16x8 v_ = *(LAS s16x8*)(rC + sl + ((i) >> 1) * 2048); \
                W[(i) >> 1][0] = __builtin_shufflevector(v_, v_, 0, 1, 2, 3); W[(i) >> 1][1] = __builtin_shufflevector(v_, v_, 4, 5, 6, 7); } \
        } else H[((i) - 8) >> 1][(i) & 1] = tr4(rH + sl + (((i) - 8) >> 1) * 256 + ((i) & 1) * 2048); \
    } while (0)
#define MM(i, HC, WC) mfma_acc(ac2[(i) & 3][(i) >> 2], cat8(WC[(i) & 3][0], WC[(i) & 3][1]), cat8(HC[(i) >> 2][0], HC[(i) >> 2][1]))
#define STEP(RD, HC, WC, HN, WN) do { \
        _Pragma("unroll") for (int i = 0; i < 32; ++i) { MM(i, HC, WC); if (i < 24) RD1(RD, i, HN, WN); } \
        sl = sl == 4 * 32768 ? 0 : sl + 32768; LGKM0(); \
    } while (0)

    LGKM0();
    BAR();
#pragma unroll
    for (int i = 0; i < 24; ++i) RD1(1, i, HX, WX);
    sl = 32768; LGKM0();
    BAR();
#pragma unroll
    for (int i = 0; i < 32; ++i) {
        asm volatile("v_mfma_f32_16x16x32_bf16 %0, %1, %2, 0" : "=&v"(ac2[i & 3][i >> 2]) : "v"(cat8(WX[i & 3][0], WX[i & 3][1])), "v"(cat8(HX[i >> 2][0], HX[i >> 2][1])) : "memory");
        if (i < 24) RD1(1, i, HY, WY);
    }
    sl = 2 * 32768; LGKM0();
    BAR(); STEP(1, HY, WY, HX, WX);
    const int nf = late ? 15 : 11;
#pragma nounroll
    for (int i = 1; i < nf; ++i) {
        if (i == 3) WAIT_V(0);
        BAR(); STEP(1, HX, WX, HY, WY); BAR(); STEP(1, HY, WY, HX, WX);
    }
    BAR(); STEP(1, HX, WX, HY, WY); BAR(); STEP(0, HY, WY, HX, WX);
#pragma nounroll
    for (int i = late ? -SHIFT / 2 : SHIFT / 2; i < 7; ++i) { BAR(); STEP(0, HX, WX, HY, WY); BAR(); STEP(0, HY, WY, HX, WX); }
    BAR(); STEP(0, HX, WX, HY, WY);
    {
        const int sw = rs0 + wid;
        LAS char* dmp = lds + (sw >= 5 ? sw - 5 : sw) * 32768 + fq2 * 256;
#define DUMP(i) *(LAS f32x4*)(dmp + (i) * 1024 + ((fr2 ^ (4 * ((i) & 3) + fq2)) * 16)) = ac2[(i) & 3][(i) >> 2]
#pragma unroll
        for (int i = 0; i < 32; ++i) { MM(i, HY, WY); if (i >= 12) DUMP(i - 12); }
        asm volatile("s_nop 15" ::: "memory");
#pragma unroll
        for (int i = 20; i < 32; ++i) DUMP(i);
    }
    WAIT_L0();
    BAR();
    asm volatile(".Lk1_end_b:");
}

extern "C" void kernel_launch(void* const* d_in, const int* in_sizes, int n_in, void* d_out, int out_size, void* d_ws, size_t ws_size,
                              hipStream_t stream) {
    const u16* x = (const u16*)d_in[0];
    const u16* lre = (const u16*)d_in[1];
    const u16* lim = (const u16*)d_in[2];
    const u16* Bre = (const u16*)d_in[3];
    const u16* Bim = (const u16*)d_in[4];
    const u16* Cre = (const u16*)d_in[5];
    const u16* Cim = (const u16*)d_in[6];
    const u16* logdt = (const u16*)d_in[7];
    const u16* dskip = (const u16*)d_in[8];
    const u16* Wg = (const u16*)d_in[10];
    const u16* Wu = (const u16*)d_in[11];
    const u16* Wd = (const u16*)d_in[12];
    const u16* gw = (const u16*)d_in[13];
    char* ws = (char*)d_ws;
    (void)hipFuncSetAttribute((const void*)k1_fused, hipFuncAttributeMaxDynamicSharedMemorySize, K2_LDS);
    k1_fused<<<256, 512, K2_LDS, stream>>>(x, Wg, Wu, Bre, Bim, lre, lim, logdt, gw, Cre, Cim, Wd, dskip, ws, (float*)d_out);
}
```

```cpp
#include <hip/hip_runtime.h>
#include <hip/hip_bf16.h>


typedef unsigned short u16;
typedef __bf16 bf16x8 __attribute__((ext_vector_type(8)));
typedef __bf16 bf16x2 __attribute__((ext_vector_type(2)));
typedef short s16x4 __attribute__((ext_vector_type(4)));
typedef short s16x8 __attribute__((ext_vector_type(8)));
typedef float f32x4 __attribute__((ext_vector_type(4)));
typedef float f32x2 __attribute__((ext_vector_type(2)));
typedef unsigned u32x4 __attribute__((ext_vector_type(4)));
typedef unsigned u32x2 __attribute__((ext_vector_type(2)));
#define LAS __attribute__((address_space(3)))
#define GAS __attribute__((address_space(1)))
typedef GAS unsigned gu32;
typedef GAS unsigned long long gu64;
typedef __attribute__((address_space(4))) const unsigned long long* kptr;
#define RLX_AGENT __ATOMIC_RELAXED, __HIP_MEMORY_SCOPE_AGENT

constexpr int SEQ = 2048, DM = 1024, NP = 1024, FF = 3584;
constexpr int KSTEPS2 = (2 * NP + FF) / 64;

constexpr unsigned WS_CARRY = 0;
constexpr unsigned WS_G = 7 * 8192;
constexpr unsigned WS_RDY = 65536;
constexpr unsigned WS_FLAG2 = WS_RDY + 1024;
constexpr unsigned WS_HQ = WS_FLAG2 + 16384;
constexpr unsigned WS_HA = WS_HQ + 1024;
constexpr unsigned WS_HR = WS_HA + 1024;
constexpr unsigned WS_SYNC_BYTES = WS_HR + 2048;
constexpr size_t WS_A2 = 1 << 20;
constexpr unsigned A2S = 262144 + 128;
constexpr size_t WS_A2_BYTES = (size_t)KSTEPS2 * A2S;

__device__ __forceinline__ float bflo(unsigned v) { return __uint_as_float(v << 16); }
__device__ __forceinline__ float bfhi(unsigned v) { return __uint_as_float(v & 0xffff0000u); }
__device__ __forceinline__ float rbf(float f) { return (float)(__bf16)f; }
__device__ __forceinline__ unsigned pk2(float a, float b) {
    bf16x2 v; v.x = (__bf16)a; v.y = (__bf16)b; return __builtin_bit_cast(unsigned, v);
}
__device__ __forceinline__ float relu(float v) { return __int_as_float(max(__float_as_int(v), 0)); }
__device__ __forceinline__ float dot2(unsigned a, unsigned b, float c) {
    return __builtin_amdgcn_fdot2_f32_bf16(__builtin_bit_cast(bf16x2, a), __builtin_bit_cast(bf16x2, b), c, false);
}

#define WAIT_V(n) asm volatile("s_waitcnt vmcnt(" #n ")" ::: "memory")
#define WAIT_L0() asm volatile("s_waitcnt lgkmcnt(0)" ::: "memory")
#define LGKM0() __builtin_amdgcn_s_waitcnt(0xC07F)
#define BAR() do { asm volatile("" ::: "memory"); __builtin_amdgcn_s_barrier(); asm volatile("" ::: "memory"); } while (0)
#define SCHED() __builtin_amdgcn_sched_barrier(0)
#define SRD(p, bytes) __builtin_amdgcn_make_buffer_rsrc((void*)(p), (short)0, (int)(bytes), 0x00020000)

__device__ __forceinline__ void dma16(unsigned m0v, unsigned voff, __amdgpu_buffer_rsrc_t srd, unsigned soff) {
    asm volatile("s_mov_b32 m0, %0\n\ts_nop 0\n\tbuffer_load_dwordx4 %1, %2, %3 offen lds" ::"s"(m0v), "v"(voff), "s"(srd), "s"(soff) : "m0", "memory");
}
__device__ __forceinline__ bf16x8 lds_b128(LAS char* p) { return __builtin_bit_cast(bf16x8, *(LAS s16x8*)p); }
__device__ __forceinline__ bf16x8 lds_tr(LAS char* p0, LAS char* p1) {
    s16x4 a = __builtin_amdgcn_ds_read_tr16_b64_v4i16((LAS s16x4*)p0);
    s16x4 b = __builtin_amdgcn_ds_read_tr16_b64_v4i16((LAS s16x4*)p1);
    return __builtin_bit_cast(bf16x8, __builtin_shufflevector(a, b, 0, 1, 2, 3, 4, 5, 6, 7));
}
__device__ __forceinline__ f32x2 cmul(f32x2 a, f32x2 b) { return (f32x2){__builtin_fmaf(a.x, b.x, -(a.y * b.y)), __builtin_fmaf(a.x, b.y, a.y * b.x)}; }
__device__ __forceinline__ f32x2 cfma(f32x2 a, f32x2 b, f32x2 c) {
    return (f32x2){__builtin_fmaf(a.x, b.x, __builtin_fmaf(-a.y, b.y, c.x)), __builtin_fmaf(a.x, b.y, __builtin_fmaf(a.y, b.x, c.y))};
}
__device__ __forceinline__ f32x2 sel4(int i, f32x2 a, f32x2 b, f32x2 c, f32x2 d) {
    const bool o = i & 1, u = i & 2;
    const float lx = o ? b.x : a.x, ly = o ? b.y : a.y, hx = o ? d.x : c.x, hy = o ? d.y : c.y;
    return (f32x2){u ? hx : lx, u ? hy : ly};
}

constexpr int K2_LDS = 5 * 32768;
constexpr int SHIFT = 2;
constexpr int PF_D0 = 4;

__device__ __forceinline__ void dma4c(unsigned m0v, unsigned voff, __amdgpu_buffer_rsrc_t srd, unsigned soff) {
    asm volatile("s_mov_b32 m0, %0\n\ts_nop 0\n\tbuffer_load_dwordx4 %1, %2, %3 offen sc1 lds\n\tbuffer_load_dwordx4 %1, %2, %3 offen offset:1024 sc1 lds\n\t"
                 "buffer_load_dwordx4 %1, %2, %3 offen offset:2048 sc1 lds\n\tbuffer_load_dwordx4 %1, %2, %3 offen offset:3072 sc1 lds" ::"s"(m0v), "v"(voff), "s"(srd), "s"(soff) : "m0", "memory");
}
__device__ __forceinline__ void dma4(unsigned m0v, unsigned voff, __amdgpu_buffer_rsrc_t srd, unsigned s0, unsigned s1, unsigned s2, unsigned s3) {
    asm volatile("s_mov_b32 m0, %0\n\ts_nop 0\n\tbuffer_load_dwordx4 %1, %2, %3 offen lds\n\tbuffer_load_dwordx4 %1, %2, %4 offen offset:1024 lds\n\t"
                 "buffer_load_dwordx4 %1, %2, %5 offen offset:2048 lds\n\tbuffer_load_dwordx4 %1, %2, %6 offen offset:3072 lds" ::"s"(m0v), "v"(voff), "s"(srd), "s"(s0), "s"(s1), "s"(s2), "s"(s3) : "m0", "memory");
}
__device__ __forceinline__ void mfma_acc(f32x4& c, bf16x8 a, bf16x8 b) {
    asm volatile("v_mfma_f32_16x16x32_bf16 %0, %1, %2, %0" : "+v"(c) : "v"(a), "v"(b) : "memory");
}
__device__ __forceinline__ void mfma_acc2(f32x4& c, bf16x8 a0, bf16x8 b0, bf16x8 a1, bf16x8 b1) {
    asm volatile("v_mfma_f32_16x16x32_bf16 %0, %1, %2, %0\n\tv_mfma_f32_16x16x32_bf16 %0, %3, %4, %0" : "+v"(c) : "v"(a0), "v"(b0), "v"(a1), "v"(b1) : "memory");
}
__device__ __forceinline__ s16x4 tr4(LAS char* p) { return __builtin_amdgcn_ds_read_tr16_b64_v4i16((LAS s16x4*)p); }
__device__ __forceinline__ bf16x8 cat8(s16x4 a, s16x4 b) { return __builtin_bit_cast(bf16x8, __builtin_shufflevector(a, b, 0, 1, 2, 3, 4, 5, 6, 7)); }

constexpr int K1_ASB = 40960;
constexpr int K1_W = 81920;
constexpr int K1_WBUF = 28704, K1_UOFF = 14352;
constexpr int K1_ETAB = K1_W + 2 * K1_WBUF;
constexpr int K1_CTAB = K1_ETAB + 32 * 144;
constexpr int K1_SINK = K1_CTAB + 32 * 96;
constexpr int K1_GTAB = K1_SINK + 1024;
constexpr int K1_HS = K1_GTAB + 1024;

__global__ __launch_bounds__(512, 2) void k1_fused(const u16* __restrict__ x, const u16* __restrict__ Wg, const u16* __restrict__ Wu,
                                                       const u16* __restrict__ Bre, const u16* __restrict__ Bim, const u16* __restrict__ lre,
                                                       const u16* __restrict__ lim, const u16* __restrict__ logdt, const u16* __restrict__ gw,
                                                       const u16* __restrict__ Cre, const u16* __restrict__ Cim, const u16* __restrict__ Wd,
                                                       const u16* __restrict__ dskip, char* __restrict__ ws, float* __restrict__ out) {
    extern __shared__ __attribute__((aligned(16))) char smem[];
    LAS char* lds = (LAS char*)smem;
    const unsigned ldsb = (unsigned)(size_t)lds;
    const int tid = threadIdx.x, lane = tid & 63;
    const int wid = __builtin_amdgcn_readfirstlane(tid >> 6);
    const int wr = wid & 3, wc = wid >> 2;
    const int bid = blockIdx.x, idx = bid >> 3, j = (bid & 7) * 4 + (idx & 3), pm = idx >> 2;
    const int fr = lane & 15, fq = lane >> 4;
#define ROLE(b_, i_) const int xc = (b_) & 7, late = (xc & 1), mb2 = (xc >> 2) * 8 + ((i_) >> 2), nblk = ((xc >> 1) & 1) * 4 + ((i_) & 3), tile = mb2 * 8 + nblk
    const char* code0; unsigned codeA, codeB, codeM;
    asm volatile("s_getpc_b64 %0\n.Lk1_ref:\n\ts_mov_b32 %1, .Lk1_end_a-.Lk1_ref\n\ts_mov_b32 %2, .Lk1_end_b-.Lk1_ref\n\ts_mov_b32 %3, .Lk1_mid-.Lk1_ref"
                 : "=s"(code0), "=s"(codeA), "=s"(codeB), "=s"(codeM));
    const __amdgpu_buffer_rsrc_t sI = SRD(code0, (codeA > codeB ? codeA : codeB) + 192);
    const unsigned mSink = ldsb + K1_SINK;
    const bool pf = wid == 7;
    if (pf) {
        const __amdgpu_buffer_rsrc_t sJ = SRD(code0, codeM + 1024);
        dma16(mSink, (unsigned)(lane * 128), sJ, 0u); dma16(mSink, (unsigned)(lane * 128), sJ, 8192u);
    }

    const int ch = 32 * j + 8 * (fq & 1) + 4 * (fr >> 3) + 16 * ((fr >> 2) & 1) + (fr & 3);
    const int grow = pm * 256 + j * 8 + wid;
    u32x4 xa, xb, ga, gb; unsigned plr, pli, pld;
    {
        const u16* xr = x + (size_t)grow * DM;
        const unsigned l16 = lane * 16, c2 = ch * 2;
        asm volatile("s_nop 4\n\tglobal_load_dwordx4 %0, %7, %9\n\tglobal_load_dwordx4 %1, %7, %9 offset:1024\n\t"
                     "global_load_dwordx4 %2, %7, %10\n\tglobal_load_dwordx4 %3, %7, %10 offset:1024\n\t"
                     "global_load_ushort %4, %8, %11\n\tglobal_load_ushort %5, %8, %12\n\tglobal_load_ushort %6, %8, %13"
                     : "=&v"(xa), "=&v"(xb), "=&v"(ga), "=&v"(gb), "=&v"(plr), "=&v"(pli), "=&v"(pld)
                     : "v"(l16), "v"(c2), "s"(xr), "s"(gw), "s"(lre), "s"(lim), "s"(logdt) : "memory");
    }

    const int q4 = wid + 28, q5 = wid + 35;
    const __amdgpu_buffer_rsrc_t sX = SRD(x, SEQ * DM * 2), sG = SRD(Wg, DM * FF * 2), sU = SRD(Wu, DM * FF * 2);
    const __amdgpu_buffer_rsrc_t s4 = SRD(q4 < 32 ? x : (q4 & 2) ? Bim : Bre, q4 < 32 ? SEQ * DM * 2 : NP * DM * 2);
    const __amdgpu_buffer_rsrc_t s5 = SRD((q5 & 2) ? Bim : Bre, q5 < 40 ? NP * DM * 2 : 0);
    const int l3 = lane >> 3;
    const unsigned voffX = (unsigned)(((l3 >> 2) * 16 + (l3 & 3)) * 2048 + (((lane & 7) ^ (l3 & 6)) * 16));
    unsigned voffW;
    { const int rho = (tid * 4682) >> 16, c = tid - rho * 14, k = (rho & 0x13) | ((rho & 4) << 1) | ((rho & 8) >> 1);
      voffW = (unsigned)(k * (FF * 2) + c * 16); }
#define XROW(q) (pm * 256 + ((q) >> 3) * 64 + (((q) >> 2) & 1) * 8 + ((q) & 1) * 32 + (((q) >> 1) & 1) * 4)
#define BROW(q) (32 * j + 8 * (((q) >> 2) & 1) + 4 * ((q) & 1))
    constexpr unsigned WST = 64 * FF * 2, WSH = 32 * FF * 2;
    const unsigned so0 = (unsigned)(XROW(wid) * 2048), so1 = (unsigned)(XROW(wid + 7) * 2048), so2 = (unsigned)(XROW(wid + 14) * 2048), so3 = (unsigned)(XROW(wid + 21) * 2048),
                   so4 = (unsigned)((q4 < 32 ? XROW(q4) : BROW(q4)) * 2048), so5 = (unsigned)(BROW(q5) * 2048);
    const unsigned soW = (unsigned)(j * 224), mb0 = ldsb + wid * 1024;
    const unsigned m50 = q5 < 40 ? mb0 + 35 * 1024 : mSink, m51 = q5 < 40 ? mb0 + 35 * 1024 + K1_ASB : mSink;
#define ST_X(i, ko, b) dma16(mb0 + (b) * K1_ASB + (i) * 7 * 1024, voffX, sX, ((i) == 0 ? so0 : (i) == 1 ? so1 : (i) == 2 ? so2 : so3) + (ko))
#define ST_S4(ko, b) dma16(mb0 + (b) * K1_ASB + 28 * 1024, voffX, s4, so4 + (ko))
#define ST_S5(ko, b) dma16((b) ? m51 : m50, voffX, s5, so5 + (ko))
#define ST_G(h, t, b) dma16(mb0 + K1_W + (b) * K1_WBUF + (h) * 7168, voffW, sG, soW + (t) * WST + (h) * WSH)
#define ST_U(h, t, b) dma16(mb0 + K1_W + (b) * K1_WBUF + K1_UOFF + (h) * 7168, voffW, sU, soW + (t) * WST + (h) * WSH)
    if (!pf) { ST_X(0, 0, 0); ST_X(1, 0, 0); ST_X(2, 0, 0); ST_X(3, 0, 0); ST_S4(0, 0); ST_S5(0, 0); ST_G(0, 0, 0); ST_G(1, 0, 0); ST_U(0, 0, 0); ST_U(1, 0, 0); }

    const int ch0 = (fq ^ (fr & 6)) * 16;
    LAS char* rA0 = lds + wr * 8192 + fr * 128 + ch0;
    LAS char* rA1 = lds + wr * 8192 + fr * 128 + (ch0 ^ 64);
    LAS char* rS0 = lds + 32768 + wc * 4096 + fr * 128 + ch0;
    LAS char* rS1 = lds + 32768 + wc * 4096 + fr * 128 + (ch0 ^ 64);
    const int trow = (16 * (fq >> 1) + 4 * (fq & 1) + ((lane & 15) >> 2)) * 224;
    LAS char* rW = lds + K1_W + trow + wc * 96 + (lane & 3) * 8;
    LAS char* rM = lds + K1_W + trow + 192 + wc * 16 + (lane & 1) * 8 + ((lane >> 1) & 1) * K1_UOFF;

    f32x4 acc[4][9];
#pragma unroll
    for (int m = 0; m < 4; ++m)
#pragma unroll
        for (int n = 0; n < 9; ++n) { acc[m][n] = (f32x4){0.f, 0.f, 0.f, 0.f}; asm volatile("" : "+v"(acc[m][n])); }
    bf16x8 Af[4][2], Bf[3][2];

#define RD_A(m, b) do { Af[m][0] = lds_b128(rA0 + (b) * K1_ASB + (m) * 2048); Af[m][1] = lds_b128(rA1 + (b) * K1_ASB + (m) * 2048); } while (0)
#define RD_S(slot, ri, b) do { Bf[slot][0] = lds_b128(rS0 + (b) * K1_ASB + (ri) * 2048); Bf[slot][1] = lds_b128(rS1 + (b) * K1_ASB + (ri) * 2048); } while (0)
#define RD_W(slot, base, off) do { Bf[slot][0] = lds_tr((base) + (off), (base) + (off) + 1792); Bf[slot][1] = lds_tr((base) + (off) + 7168, (base) + (off) + 7168 + 1792); } while (0)
#define SEC1(b, d0, d1) do { RD_S(0, 0, b); RD_S(1, 1, b); d0; RD_W(2, rW, (b) * K1_WBUF); RD_A(0, b); d1; RD_A(1, b); RD_A(2, b); RD_A(3, b); } while (0)
#define SEC2(b, d0, d1, d2, d3) do { d0; RD_W(0, rW, (b) * K1_WBUF + 32); d1; RD_W(1, rW, (b) * K1_WBUF + 64); d2; RD_W(2, rM, (b) * K1_WBUF); d3; } while (0)
#define SEC3(b, d0, d1, d2, d3) do { d0; RD_W(0, rW, (b) * K1_WBUF + K1_UOFF); d1; RD_W(1, rW, (b) * K1_WBUF + K1_UOFF + 32); d2; RD_W(2, rW, (b) * K1_WBUF + K1_UOFF + 64); d3; } while (0)
#define GO(n0, n1, n2) do { asm volatile("" ::: "memory"); LGKM0(); BAR(); SCHED(); \
        _Pragma("unroll") for (int m = 0; m < 4; ++m) { \
            mfma_acc2(acc[m][n0], Af[m][0], Bf[0][0], Af[m][1], Bf[0][1]); mfma_acc2(acc[m][n1], Af[m][0], Bf[1][0], Af[m][1], Bf[1][1]); \
            mfma_acc2(acc[m][n2], Af[m][0], Bf[2][0], Af[m][1], Bf[2][1]); } \
        SCHED(); } while (0)

#define RELANE() int tz = threadIdx.x; asm volatile("" : "+v"(tz)); const int er = tz & 15, eq = (tz >> 4) & 3
    asm volatile("" : "+v"(rA0), "+v"(rA1), "+v"(rS0), "+v"(rS1), "+v"(rW), "+v"(rM));
    WAIT_V(10);
    if (pf) WAIT_V(0);
    asm volatile("" : "+v"(xa), "+v"(xb), "+v"(ga), "+v"(gb), "+v"(plr), "+v"(pli), "+v"(pld) :: "memory");
    {
        float s = 0.f, s2 = 0.f;
#pragma unroll
        for (int e = 0; e < 4; ++e) { s = dot2(xa[e], ga[e], s); s2 = dot2(xb[e], gb[e], s2); }
        s += s2;
#define DPP_ADD(ctrl, rmask) s += __int_as_float(__builtin_amdgcn_update_dpp(0, __float_as_int(s), ctrl, rmask, 0xf, true))
        DPP_ADD(0x111, 0xf); DPP_ADD(0x112, 0xf); DPP_ADD(0x114, 0xf); DPP_ADD(0x118, 0xf); DPP_ADD(0x142, 0xa); DPP_ADD(0x143, 0xc);
        const float g = rbf(__builtin_amdgcn_rcpf(1.f + __builtin_amdgcn_exp2f(-1.4426950408889634f * rbf(s))));
        if (lane == 63) __hip_atomic_store((gu32*)(ws + WS_G) + grow, pk2(g, 1.f - g), RLX_AGENT);
    }
    if (!pf) {
        BAR();
        ST_X(0, 128, 1); ST_X(1, 128, 1); ST_X(2, 128, 1); ST_X(3, 128, 1); ST_S4(128, 1); ST_S5(128, 1); ST_G(0, 1, 1); ST_G(1, 1, 1);
        WAIT_V(11);
        if (wc == 1) BAR();
#pragma nounroll
        for (int t = 0; t < 16; t += 2) {
            asm volatile("" : "+s"(t));
            const unsigned ko = t == 14 ? 0x80000000u : (unsigned)(t + 2) * 128u;
            BAR(); SEC1(0, ST_U(0, t + 1, 1), ST_U(1, t + 1, 1)); WAIT_V(10);
            GO(7, 8, 0);
            BAR(); SEC2(0, ST_X(0, ko, 0), ST_X(1, ko, 0), ST_X(2, ko, 0), ST_X(3, ko, 0));
            GO(1, 2, 6);
            BAR(); SEC3(0, ST_G(0, t + 2, 0), ST_G(1, t + 2, 0), ST_S4(ko, 0), ST_S5(ko, 0)); WAIT_V(10);
            GO(3, 4, 5);
            BAR(); SEC1(1, ST_U(0, t + 2, 0), ST_U(1, t + 2, 0)); WAIT_V(10);
            GO(7, 8, 0);
            BAR(); SEC2(1, ST_X(0, ko + 128, 1), ST_X(1, ko + 128, 1), ST_X(2, ko + 128, 1), ST_X(3, ko + 128, 1));
            GO(1, 2, 6);
            BAR(); SEC3(1, ST_G(0, t + 3, 1), ST_G(1, t + 3, 1), ST_S4(ko + 128, 1), ST_S5(ko + 128, 1)); WAIT_V(10);
            GO(3, 4, 5);
        }
    }
    int w7 = wid; asm volatile("" : "+s"(w7));
    if (w7 == 7) {
        {
            const float lr = bflo(plr), li = bflo(pli), dt = rbf(expf(bflo(pld)));
            const float e = expf(dt * lr); float sn, cs; sincosf(dt * li, &sn, &cs);
            const f32x2 a1 = {e * cs, e * sn};
            const float nr = a1.x - 1.f, den = lr * lr + li * li;
            f32x2 a16 = cmul(a1, a1); a16 = cmul(a16, a16); a16 = cmul(a16, a16); a16 = cmul(a16, a16);
            const f32x2 a32 = cmul(a16, a16), a48 = cmul(a32, a16), a64 = cmul(a32, a32), a128 = cmul(a64, a64), a192 = cmul(a128, a64), a256 = cmul(a128, a128);
            if (lane < 32) {
                LAS f32x4* ct = (LAS f32x4*)(lds + K1_CTAB + lane * 96);
                ct[0] = (f32x4){a1.x, a1.y, 2.f * ((nr * lr + a1.y * li) / den), 2.f * ((a1.y * lr - nr * li) / den)};
                ct[1] = (f32x4){1.f, 0.f, a16.x, a16.y}; ct[2] = (f32x4){a32.x, a32.y, a48.x, a48.y};
                ct[3] = (f32x4){1.f, 0.f, a64.x, a64.y}; ct[4] = (f32x4){a128.x, a128.y, a192.x, a192.y};
                ct[5] = (f32x4){a256.x, a256.y, 0.f, 0.f};
            }
        }
        const int pb = (bid >> 3) | ((bid & 7) << 5);
        const __amdgpu_buffer_rsrc_t sPW = SRD((pb & 1) ? Wg : Wu, DM * FF * 2), sPD = SRD((pb & 1) ? Wd : (pb & 2) ? Cim : Cre, (pb & 1) ? DM * FF * 2 : DM * NP * 2);
        const __amdgpu_buffer_rsrc_t sPB = SRD((const char*)((pb & 1) ? Bre : Bim) + ((bid & 7) * 128 + (idx >> 1) * 8) * 2048, 16384);
        const __amdgpu_buffer_rsrc_t sH = SRD(ws, WS_SYNC_BYTES);
        const unsigned l128 = lane * 128, OUT = 0x80000000u;
        const unsigned l7 = (unsigned)(lane * 37) >> 8, vpw = (l7 * (FF * 2 - 7 * 128) + l128) | (unsigned)((62 - lane) & 0x80000000);
        const unsigned pwx = (unsigned)((idx >> 1) * 9 * FF * 2 + (bid & 7) * 896);
#define PF_W(r) dma16(mSink, vpw, sPW, pwx + (unsigned)(r) * (144 * FF * 2))
#define PF_B(r) dma16(mSink, l128, sPB, (unsigned)(r) * 8192)
        const unsigned pds = (pb & 1) ? (1u << 20) : (1u << 19), pd0 = (unsigned)(((pb & 1) ? pb >> 1 : pb >> 2) * 8192);
#define PF_D(r) dma16(mSink, l128, sPD, pd0 + (unsigned)(r) * pds)
#define RFL(v) ((unsigned)__builtin_amdgcn_readfirstlane(v))
#define HDMA(m, vo, srd, so) asm volatile("s_nop 4\n\ts_mov_b32 m0, %0\n\ts_nop 0\n\tbuffer_load_dwordx4 %1, %2, %3 offen sc1 lds" ::"s"((unsigned)(m)), "v"(vo), "s"(srd), "s"((unsigned)(so)) : "m0", "memory")
#define HLD(op, v, vo) asm volatile("s_nop 4\n\t" op " %0, %1, %2, 0 offen sc1\n\ts_waitcnt vmcnt(0)" : "=v"(v) : "v"(vo), "s"(sH) : "memory")
#define HST32(off, v) do { __builtin_amdgcn_raw_buffer_store_b32((unsigned)(v), sH, off, 0, 16); asm volatile("" ::: "memory"); } while (0)
#define HST64(off, lo_, hi2_) do { __builtin_amdgcn_raw_buffer_store_b64((u32x2){(unsigned)(lo_), (unsigned)(hi2_)}, sH, off, 0, 16); asm volatile("" ::: "memory"); } while (0)
#define REPLY(q_) do { _Pragma("unroll") for (int i = 0; i < 4; ++i) HST64(WS_HR + l16 * 2 + i * 8, (q_)[i], tag); } while (0)
#define LANE0(off) (l16 == 0 ? (unsigned)(off) : OUT)
#define ALLANS(a_) __all((a_)[0] == tag && (a_)[1] == tag && (a_)[2] == tag && (a_)[3] == tag)
#define ATX(cond, ...) do { int t_ = t; asm volatile("" : "+s"(t_)); if (cond) { unsigned l16 = l128; asm volatile("" : "+v"(l16)); l16 >>= 3; __VA_ARGS__ } } while (0)
#define AT(v, ...) ATX(t_ == (v), __VA_ARGS__)
#define AT2(v, ...) ATX((unsigned)(t_ - (v)) <= 2u, __VA_ARGS__)
        unsigned ask = 0, tag = 0, done = 0;
        const int boss = bid == 0 ? 0 : 64;
        BAR();
        HDMA(ldsb + K1_HS + 1024, l128 >> 3, sH, WS_HR + (bid >> 7) * 1024);
        HDMA(ldsb + K1_HS, l128 >> 3, sH, bid == 0 ? WS_HA : OUT);
        PF_W(1);
        BAR();
#pragma nounroll
        for (int t = 0; t < 16; t += 2) {
            asm volatile("" : "+s"(t));
            BAR(); SEC1(0, , );
            AT(10,
                HDMA(mSink, l128, sI, codeM); HDMA(mSink, l128, sI, codeM + 8192); HDMA(mSink, l128, sI, codeM + 16384););
            AT(12, HDMA(ldsb + K1_HS, l16, sH, WS_HA););
            GO(7, 8, 0);
            BAR(); SEC2(0, , , , );
            AT(0,
                ROLE(bid, idx);
                HST64((pm < 7 ? WS_CARRY + pm * 8192 + j * 256 : OUT) + (l16 < 32 * 16 ? l16 >> 1 : OUT >> 1), ~0u, ~0u);
                HST32(l16 == 4 * 16 ? WS_RDY + (pm * 32 + j) * 4 : (late ? OUT : WS_FLAG2 + tile * 128) + (l16 < 4 * 16 ? l16 * 2 : OUT >> 1), ~0u););
            AT(2,
                const __amdgpu_buffer_rsrc_t sA = SRD(ws + WS_A2, WS_A2_BYTES), sO = SRD(out, SEQ * DM * 4);
                const unsigned vo = (l16 >> 4) * A2S, vo2 = l16 << 13;
                HDMA(mSink, vo, sA, A2S - 128); HDMA(mSink, vo, sA, 65 * A2S - 128); HDMA(mSink, vo2, sO, 0u););
            AT2(4 + boss,
                WAIT_V(2);
                const u32x4 q = *(LAS u32x4*)(lds + K1_HS + l16), qp = *(LAS u32x4*)(lds + K1_GTAB + l16);
                if (t_ == 4 || __any(q[0] != qp[0] || q[1] != qp[1] || q[2] != qp[2] || q[3] != qp[3])) REPLY(q);
                *(LAS u32x4*)(lds + K1_GTAB + l16) = q;);
            AT2(6 + (int)done * 64, HDMA(ldsb + K1_HS + 1024, l16, sH, WS_HR + (bid >> 7) * 1024););
            GO(1, 2, 6);
            BAR(); SEC3(0, , PF_W((t >> 1) + 2), PF_D(t - PF_D0), );
            GO(3, 4, 5);
            BAR(); SEC1(1, , );
            AT2(2 + boss, HDMA(ldsb + K1_HS, l16, sH, WS_HQ););
            AT(14, WAIT_V(0););
            GO(7, 8, 0);
            BAR(); SEC2(1, , , , );
            AT(0,
                WAIT_V(5);
                ask = RFL(*(LAS unsigned*)(lds + K1_HS + 1024 + (bid & 127) * 8)) + 1u;
                const u32x4 old = *(LAS u32x4*)(lds + K1_HS + l16);
                HST32(LANE0(WS_HQ + bid * 4), ask);
                tag = RFL(old[0]) + 1u;
                while (__any(old[0] == tag || old[1] == tag || old[2] == tag || old[3] == tag)) ++tag;);
            AT2(6 + (int)done * 64,
                WAIT_V(2);
                const u32x2 r = *(LAS u32x2*)(lds + K1_HS + 1024 + (bid & 127) * 8);
                if (RFL(r[0]) == ask) { tag = RFL(r[1]); done = 1u; HST32(LANE0(WS_HA + bid * 4), tag); });
            AT(12,
                WAIT_V(2);
                u32x4 ans = *(LAS u32x4*)(lds + K1_HS + l16);
                if (!(done && ALLANS(ans))) {
                    unsigned spins = 0;
                    _Pragma("nounroll") do {
                        if (bid == 0) { u32x4 q; HLD("buffer_load_dwordx4", q, WS_HQ + l16); REPLY(q); }
                        if (!done) {
                            u32x2 r; HLD("buffer_load_dwordx2", r, WS_HR + bid * 8);
                            if (RFL(r[0]) == ask) { tag = RFL(r[1]); done = 1u; HST32(LANE0(WS_HA + bid * 4), tag); }
                        }
                        HLD("buffer_load_dwordx4", ans, WS_HA + l16);
                    } while (!(done && ALLANS(ans)) && ++spins < (1u << 18));
                }
                HDMA(ldsb + K1_GTAB, l16, sH, WS_G + pm * 1024););
            GO(1, 2, 6);
            BAR(); SEC3(1, , PF_B(t >> 1), PF_D(t + 1 - PF_D0), );
            GO(3, 4, 5);
        }
    }
    asm volatile(".Lk1_mid:\n\ts_nop 15" ::: "memory"); SCHED();
    if (wc == 0) BAR();
    RELANE();
    kptr ka = (kptr)__builtin_amdgcn_kernarg_segment_ptr(); asm volatile("" : "+s"(ka));
#define KARG(T, i) ((T)ka[i])
    char* const ws2 = KARG(char*, 13);

    LAS char* ctab = lds + K1_CTAB + (wc * 16 + er) * 96;
    LAS char* etab = lds + K1_ETAB + (wc * 16 + er) * 144;
    const f32x4 cst = *(LAS f32x4*)ctab;
    const f32x2 a1 = {cst[0], cst[1]}, cf = {cst[2], cst[3]};
    u32x4 gt[4];
#pragma unroll
    for (int m = 0; m < 4; ++m) gt[m] = *(LAS u32x4*)(lds + K1_GTAB + wr * 256 + eq * 64 + m * 16);
    const __amdgpu_buffer_rsrc_t sA2 = SRD(ws2 + WS_A2, WS_A2_BYTES);
#define ST16(voff, v) __builtin_amdgcn_raw_buffer_store_b128(v, sA2, voff, 0, 16)
#define PK8(v) (u32x4){pk2(v[0], v[1]), pk2(v[2], v[3]), pk2(v[4], v[5]), pk2(v[6], v[7])}
    const int mblk = pm * 2 + (wr >> 1);
#define IMG_Q(r, q) (unsigned)(((q) >> 1) * 256 + ((q) & 1) * 16 + ((r) & 3) * 32 + mblk * 16384 + (wr & 1) * 1024)
#define IMG_T(r, q) (IMG_Q(r, q) + (unsigned)((((r) >> 2) & 1) * 2048 + ((r) >> 3) * 128 + (j >> 2) * A2S + (j & 3) * 4096))
#define IMG_M(r, q) (IMG_Q(r, q) + (unsigned)((((r) >> 2) & 1) * 2048 + ((r) >> 3) * 512 + (24 + (j >> 2)) * A2S + (j & 3) * 4096))
#define IMG_S(r, q) (IMG_Q(r, q) + (unsigned)((((r) >> 2) & 1) * 4096 + ((r) >> 3) * 2048 + (32 + (j >> 1)) * A2S + (j & 1) * 8192))
    const unsigned lo = IMG_T(er, eq);
#define GLU_HALF(n, hh, o) do { float v[8]; \
        _Pragma("unroll") for (int e = 0; e < 8; ++e) { const int m = 2 * (hh) + (e >> 2), q = e & 3; v[e] = relu(acc[m][n][q]) * (acc[m][3 + (n)][q] * bfhi(gt[m][q])); } \
        o = PK8(v); } while (0)
#define GLU_TILE(n) do { u32x4 o_; GLU_HALF(n, 0, o_); ST16(lo + (wc * 48 + 8 * (n)) * A2S, o_); GLU_HALF(n, 1, o_); ST16(lo + (wc * 48 + 8 * (n)) * A2S + 512, o_); } while (0)
    GLU_TILE(0);
    GLU_TILE(1);
    asm volatile("" ::: "memory");
    {
        f32x2 h = {acc[0][7][0], acc[0][8][0]};
#pragma unroll
        for (int e = 1; e < 16; ++e) h = cfma(a1, h, (f32x2){acc[e >> 2][7][e & 3], acc[e >> 2][8][e & 3]});
        *(LAS f32x2*)(etab + (wr * 4 + eq) * 8) = h;
    }
    WAIT_V(0); WAIT_L0();
    BAR();
    gu32* rdy = (gu32*)(ws2 + WS_RDY);
    if (tz == 192) __hip_atomic_store(rdy + pm * 32 + j, 1u, RLX_AGENT);
    gu64* carry = (gu64*)(ws2 + WS_CARRY);
#define CHAN(c, r) (32 * j + 8 * (c) + 4 * ((r) >> 3) + 16 * (((r) >> 2) & 1) + ((r) & 3))
    if (wid < 2 && pm < 7) {
        const f32x2 a16 = *(LAS f32x2*)(lds + K1_CTAB + (wid * 16 + er) * 96 + 24);
        f32x4 E[8];
#pragma unroll
        for (int i = 0; i < 8; ++i) E[i] = *(LAS f32x4*)(lds + K1_ETAB + (wid * 16 + er) * 144 + i * 16);
        f32x2 run = {E[0][0], E[0][1]};
#pragma unroll
        for (int i = 1; i < 16; ++i) run = cfma(a16, run, (f32x2){E[i >> 1][(i & 1) * 2], E[i >> 1][(i & 1) * 2 + 1]});
        unsigned ux = __float_as_uint(run.x), uy = __float_as_uint(run.y);
        if (ux == 0xFFFFFFFFu) ux = 0x7FC00000u;
        if (eq == 0) __hip_atomic_store(carry + pm * 1024 + CHAN(wid, er), ((unsigned long long)uy << 32) | ux, RLX_AGENT);
    }
    GLU_TILE(2);
    {
        float v[8];
        const bool lowh = er < 8;
#pragma unroll
        for (int e = 0; e < 8; ++e) {
            const int m = e >> 2, q = e & 3;
            const float v0 = lowh ? relu(acc[m][6][q]) : acc[m][6][q] * bfhi(gt[m][q]);
            const float v1 = lowh ? relu(acc[2 + m][6][q]) : acc[2 + m][6][q] * bfhi(gt[2 + m][q]);
            const float rcv = __int_as_float(__builtin_amdgcn_mov_dpp(__float_as_int(lowh ? v1 : v0), 0x128, 0xf, 0xf, true));
            v[e] = (lowh ? v0 : v1) * rcv;
        }
        ST16(IMG_M(er, eq) + wc * 128, PK8(v));
    }
    LAS f32x4* park = (LAS f32x4*)(lds + 3 * 32768 + wr * 8192 + (tz & 63) * 16);
    if (wc == 1) {
#pragma unroll
        for (int m = 0; m < 4; ++m) { park[m * 128] = acc[m][7]; park[m * 128 + 64] = acc[m][8]; }
    }
    WAIT_L0();
    BAR();

    ROLE(bid, idx);
    const int n0 = nblk * 128;
    const int rs0 = late ? (48 + SHIFT) % 5 : (40 - SHIFT) % 5;

    if (wc == 1) {
        int ty = threadIdx.x; asm volatile("" : "+v"(ty) :: "memory");
        const int ln = ty & 63, e3 = ln >> 3;
#define LEVEL(lvl) do { if (ty == 256) __hip_atomic_store(rdy + pm * 32 + j, (unsigned)(lvl), RLX_AGENT); } while (0)
        const __amdgpu_buffer_rsrc_t sC = SRD(late ? KARG(const u16*, 10) : KARG(const u16*, 9), DM * NP * 2), sW = SRD(KARG(const u16*, 11), FF * DM * 2);
        const unsigned voffA = (unsigned)(ln * 16);
        const unsigned voffC = (unsigned)(e3 * 2048 + (((ln & 7) ^ (e3 & 6)) * 16));
        const unsigned voffD = (unsigned)(((e3 >> 2) * 8 + (e3 & 3)) * 2048 + ((((ln >> 1) & 3) ^ (e3 >> 1)) * 32) + (ln & 1) * 16);
        const unsigned soA = (unsigned)((late ? 0 : 48) * A2S + mb2 * 16384 + wr * 4096);
        const unsigned soC = (unsigned)((n0 + 32 * wr) * 2048);
        const unsigned soD = (unsigned)(((late ? 0 : 48) + 112 * wr) * 2048 + n0 * 2);
        const unsigned mB = ldsb + wr * 4096;
#define A4(img, sl) dma4c(mB + (sl), voffA, sA2, soA + (img) * A2S)
#define D4(row, sl) do { const unsigned b_ = soD + (row) * 2048; \
            asm volatile("s_mov_b32 m0, %0\n\ts_nop 0\n\tbuffer_load_dwordx4 %1, %2, %3 offen lds\n\tbuffer_load_dwordx4 %1, %2, %4 offen offset:2048 lds\n\t" \
                         "buffer_load_dwordx4 %1, %2, %5 offen offset:3072 lds\n\ts_mov_b32 m0, %6\n\ts_nop 0\n\tbuffer_load_dwordx4 %1, %2, %3 offen offset:128 lds" \
                         ::"s"(mB + (sl) + 16384), "v"(voffD), "s"(sW), "s"(b_), "s"(b_ + 8192 - 2048), "s"(b_ + 8192 + 128 - 3072), "s"(mB + (sl) + 16384 + 1024 - 128) : "m0", "memory"); } while (0)
#define C4(srd, p, sl) do { const unsigned b_ = soC + (p) * 128; dma4(mB + (sl) + 16384, voffC, srd, b_, b_ + 16384 - 1024, b_ + 32768 - 2048, b_ + 49152 - 3072); } while (0)
#define POLL(v, p) asm volatile("global_load_dword %0, %1, off sc1\n\ts_waitcnt vmcnt(0)" : "=v"(v) : "v"(p) : "memory")
        gu32* rl = rdy + (mb2 >> 1) * 32 + (ln & 31);
#define XP(k_) (((((k_) & 3) << 1) | (((k_) >> 2) & 1)) ^ (xc & 1))
        unsigned fv;
        D4(448 * XP(0), 0); D4(448 * XP(1), 32768); D4(448 * XP(2), 2 * 32768);
        POLL(fv, rl);
        {
            const int other = ((ln >> 2) ^ xc) & 1;
            unsigned spins = 0;
            while (!__all((int)fv >= 1 || other) && ++spins < (1u << 18)) { __builtin_amdgcn_s_sleep(4); POLL(fv, rl); }
        }
        A4(XP(0), 0); A4(XP(1), 32768); A4(XP(2), 2 * 32768);
        WAIT_V(8); BAR();
        int k = 3, sl = 3 * 32768;
#define NEXT_SLOT() sl = sl == 4 * 32768 ? 0 : sl + 32768
#define STAGE_W() do { const int q_ = k < 16 ? (k & 3) | ((k & 4) << 1) | ((k & 8) >> 1) : k, x_ = XP(q_), row_ = 448 * x_ + ((q_ >> 3) == 3 ? 96 : 16 * (q_ >> 3)); \
            A4((q_ & ~7) + x_, sl); D4(row_, sl); ++k; NEXT_SLOT(); } while (0)
#define LSYNC() do { WAIT_V(16); BAR(); } while (0)
        const __amdgpu_buffer_rsrc_t sR = SRD(KARG(const u16*, 9), DM * NP * 2);
        STAGE_W(); WAIT_V(12); BAR();
        const int nw = late ? 32 : 24;
#pragma nounroll
        while (k < nw) {
            asm volatile("s_bitcmp1_b32 0x2040, %2\n\ts_cbranch_scc0 1f\n\tglobal_load_dword %0, %1, off sc1\n1:" : "+v"(fv) : "v"(rl), "s"(k) : "scc", "memory");
            if ((0x10500u >> k) & 1) {
                if (k == 10) LEVEL(2);
                else if (!__all((int)fv >= (k >> 3))) {
                    unsigned spins = 0, w_;
                    do { __builtin_amdgcn_s_sleep(1); POLL(w_, rl); } while (!__all((int)w_ >= (k >> 3)) && ++spins < (1u << 18));
                }
            }
            STAGE_W(); LSYNC();
        }
        const int np = late ? 16 + SHIFT : 16 - SHIFT;
#pragma nounroll
        for (int p = 0; p < np; ++p) {
            const bool ov = p >= 16;
            A4(ov ? 72 - SHIFT + p : k, sl); C4(ov ? sR : sC, ov ? p - SHIFT : p, sl);
            ++k; NEXT_SLOT(); LSYNC();
        }
        const int tb = mb2 * 128 + (wr >> 1) * 64 + (wr & 1) * 8 + (ln >> 5) * 16, dcol = n0 + (ln & 31) * 4;
#define ROW(i) (tb + ((i) >> 3) * 32 + ((i) & 7))
        gu32* flag = (gu32*)(ws2 + WS_FLAG2 + tile * 128 + wr * 32);
        const __amdgpu_buffer_rsrc_t sP = SRD(KARG(float*, 14), SEQ * DM * 4);
#define POFF(i) ((ROW(i) * DM + dcol) * 4)
        const int wn1 = (ln >> 4) & 1, s0 = rs0 + wn1, s1 = rs0 + 2 + wn1;
        const int inp = wr * 8192 + ((ln >> 2) & 3) * 1024 + (ln & 3) * 256, sw = (ln & 15) ^ ((ln >> 5) * 8);
        LAS char* q0 = lds + (s0 >= 5 ? s0 - 5 : s0) * 32768 + inp;
        LAS char* q1 = lds + (s1 >= 5 ? s1 - 5 : s1) * 32768 + inp;
#define SUMS(i) (*(LAS f32x4*)(q0 + ((i) >> 3) * 4096 + ((sw ^ ((i) & 7)) * 16)) + *(LAS f32x4*)(q1 + ((i) >> 3) * 4096 + ((sw ^ ((i) & 7)) * 16)))
        f32x4 ex[16];
        if (late) {
            WAIT_V(8); BAR();
            POLL(fv, flag); BAR();
            for (unsigned spins = 0; !__all((int)fv >= 1) && spins < (1u << 18); ++spins) { __builtin_amdgcn_s_sleep(1); POLL(fv, flag); }
#pragma unroll
            for (int i = 0; i < 16; ++i) ex[i] = __builtin_bit_cast(f32x4, __builtin_amdgcn_raw_buffer_load_b128(sP, POFF(i), 0, 16));
            BAR();
#pragma unroll
            for (int i = 0; i < 16; ++i) __builtin_amdgcn_raw_buffer_store_b128(__builtin_bit_cast(u32x4, ex[i] + SUMS(i)), sP, POFF(i), 0, 16);
        } else {
            const __amdgpu_buffer_rsrc_t sXk = SRD(KARG(const u16*, 0), SEQ * DM * 2), sDk = SRD(KARG(const u16*, 12), DM * 2), sGk = SRD(ws2 + WS_G, 8192);
            WAIT_V(8); BAR();
            u32x2 dk = __builtin_amdgcn_raw_buffer_load_b64(sDk, dcol * 2, 0, 0), xv[16]; u32x4 gwd[4];
#pragma unroll
            for (int i = 0; i < 4; ++i) gwd[i] = __builtin_amdgcn_raw_buffer_load_b128(sGk, ROW(4 * i) * 4, 0, 16);
#pragma unroll
            for (int i = 0; i < 16; ++i) xv[i] = __builtin_amdgcn_raw_buffer_load_b64(sXk, (tb * DM + dcol) * 2 + (i & 1) * 2048, (((i) >> 3) * 32 + ((i) & 6)) * 2048, 0);
            WAIT_V(21); BAR();
            asm volatile("" : "+v"(dk), "+v"(gwd[0]), "+v"(gwd[1]), "+v"(gwd[2]), "+v"(gwd[3]));
#pragma unroll
            for (int i = 0; i < 16; ++i) {
                const float g = bflo(gwd[i >> 2][i & 3]);
                ex[i] = (f32x4){g * rbf(bflo(xv[i][0]) * bflo(dk[0])), g * rbf(bfhi(xv[i][0]) * bfhi(dk[0])),
                                g * rbf(bflo(xv[i][1]) * bflo(dk[1])), g * rbf(bfhi(xv[i][1]) * bfhi(dk[1]))};
                asm volatile("" : "+v"(ex[i]));
            }
            BAR();
#pragma unroll
            for (int i = 0; i < 16; ++i) __builtin_amdgcn_raw_buffer_store_b128(__builtin_bit_cast(u32x4, ex[i] + SUMS(i)), sP, POFF(i), 0, 16);
            WAIT_V(0);
            if (ln == 0) __hip_atomic_store(flag, 1u, RLX_AGENT);
        }
        asm volatile(".Lk1_end_a:");
        return;
    }

    {
        unsigned long long gq[2][7];
#pragma unroll
        for (int c = 0; c < 2; ++c)
#pragma unroll
            for (int q = 0; q < 7; ++q) gq[c][q] = __hip_atomic_load(carry + (q + pm >= 7 ? q + pm - 7 : 0) * 1024 + CHAN(c, er), RLX_AGENT);
        f32x2 h[2];
        {
            f32x2 a16[2], run[2]; f32x4 E[2][6], F0[2]; f32x2 F1[2];
#pragma unroll
            for (int c = 0; c < 2; ++c) {
                a16[c] = *(LAS f32x2*)(ctab + c * (16 * 96) + 24);
#pragma unroll
                for (int i = 0; i < 6; ++i) E[c][i] = *(LAS f32x4*)(etab + c * (16 * 144) + i * 16);
                F0[c] = *(LAS f32x4*)(etab + c * (16 * 144) + wr * 32); F1[c] = *(LAS f32x2*)(etab + c * (16 * 144) + wr * 32 + 16);
                run[c] = (f32x2){0.f, 0.f};
            }
#pragma unroll
            for (int w = 0; w < 3; ++w)
                if (wr > w) {
#pragma unroll
                    for (int i = 0; i < 4; ++i)
#pragma unroll
                        for (int c = 0; c < 2; ++c) run[c] = cfma(a16[c], run[c], (f32x2){E[c][2 * w + (i >> 1)][(i & 1) * 2], E[c][2 * w + (i >> 1)][(i & 1) * 2 + 1]});
                }
#pragma unroll
            for (int c = 0; c < 2; ++c) {
                const f32x2 r1 = cfma(a16[c], run[c], (f32x2){F0[c][0], F0[c][1]}), r2 = cfma(a16[c], r1, (f32x2){F0[c][2], F0[c][3]}), r3 = cfma(a16[c], r2, F1[c]);
                h[c] = sel4(eq, run[c], r1, r2, r3);
            }
        }
        f32x4 ur[4], ui[4];
#pragma unroll
        for (int m = 0; m < 4; ++m) { ur[m] = park[m * 128]; ui[m] = park[m * 128 + 64]; }
        const f32x4 cs1 = *(LAS f32x4*)(ctab + 16 * 96);

        f32x2 a256[2], aq[2];
#pragma unroll
        for (int c = 0; c < 2; ++c) {
            LAS char* ct = ctab + c * (16 * 96);
            a256[c] = *(LAS f32x2*)(ct + 80); aq[c] = cmul(*(LAS f32x2*)(ct + 16 + eq * 8), *(LAS f32x2*)(ct + 48 + wr * 8));
        }
        unsigned mx = 0;
#pragma unroll
        for (int q = 0; q < 7; ++q) mx = max(mx, max((unsigned)gq[0][q], (unsigned)gq[1][q]));
        if (pm > 0 && __any(mx == 0xFFFFFFFFu)) {
            unsigned spins = 0;
            do {
                mx = 0;
#pragma unroll
                for (int c = 0; c < 2; ++c)
#pragma unroll
                    for (int q = 0; q < 7; ++q) {
                        gq[c][q] = __hip_atomic_load(carry + (q + pm >= 7 ? q + pm - 7 : 0) * 1024 + CHAN(c, er), RLX_AGENT);
                        mx = max(mx, (unsigned)gq[c][q]);
                    }
            } while (__any(mx == 0xFFFFFFFFu) && ++spins < (1u << 18));
        }
        f32x2 cg[2] = {{0.f, 0.f}, {0.f, 0.f}};
#pragma unroll
        for (int q = 0; q < 7; ++q) {
            const unsigned keep = q + pm >= 7 ? 0xFFFFFFFFu : 0u;
#pragma unroll
            for (int c = 0; c < 2; ++c) cg[c] = cfma(a256[c], cg[c], (f32x2){__uint_as_float((unsigned)gq[c][q] & keep), __uint_as_float((unsigned)(gq[c][q] >> 32) & keep)});
        }
        h[0] = cfma(aq[0], cg[0], h[0]); h[1] = cfma(aq[1], cg[1], h[1]);
        const unsigned ls = IMG_S(er, eq);
        const f32x2 b1 = {cs1[0], cs1[1]}, bf = {cs1[2], cs1[3]};
#pragma unroll
        for (int hh = 0; hh < 2; ++hh) {
            float vr[8], vi[8], wr8[8], wi8[8];
#pragma unroll
            for (int e = 0; e < 8; ++e) {
                const int m = 2 * hh + (e >> 2), q = e & 3;
                h[0] = cfma(a1, h[0], (f32x2){acc[m][7][q], acc[m][8][q]});
                h[1] = cfma(b1, h[1], (f32x2){ur[m][q], ui[m][q]});
                const float g2 = bflo(gt[m][q]);
                const f32x2 s0 = cmul(cf, h[0]), s1 = cmul(bf, h[1]);
                vr[e] = s0.x * g2; vi[e] = -s0.y * g2; wr8[e] = s1.x * g2; wi8[e] = -s1.y * g2;
            }
            ST16(ls + 40 * A2S + hh * 512, PK8(vr)); ST16(ls + 40 * A2S + hh * 512 + 128, PK8(wr8));
            ST16(ls + hh * 512, PK8(vi)); ST16(ls + hh * 512 + 128, PK8(wi8));
        }
    }
    int ty = threadIdx.x; asm volatile("" : "+v"(ty) :: "memory");
    const int ln = ty & 63, fr2 = ty & 15, fq2 = (ty >> 4) & 3;
    const int wn = wid & 1, kk = wid >> 1;
    LAS char* rH = lds + kk * 8192 + (ln >> 5) * 4096 + (ln & 31) * 8;
    LAS char* rC = lds + 16384 + (wn * 64 + fr2) * 128 + (((4 * kk + fq2) ^ (fr2 & 6)) * 16);
    LAS char* rWb = lds + 16384 + kk * 8192 + (ln >> 5) * 4096 + wn * 1024 + ((ln >> 2) & 7) * 128 + (ln & 3) * 8;
    const int rsw = (ln >> 3) & 3;
    f32x4 ac2[4][8];
    s16x4 HX[8][2], HY[8][2], WX[4][2], WY[4][2];
    int sl = 0;

#define RD1(RD, i, H, W) do { \
        if ((i) < 8) { \
            if (RD == 1) W[(i) >> 1][(i) & 1] = tr4(rWb + sl + ((((i) >> 1) ^ rsw) * 32) + ((i) & 1) * 2048); \
            else if (((i) & 1) == 0) { const s16x8 v_ = *(LAS s16x8*)(rC + sl + ((i) >> 1) * 2048); \
                W[(i) >> 1][0] = __builtin_shufflevector(v_, v_, 0, 1, 2, 3); W[(i) >> 1][1] = __builtin_shufflevector(v_, v_, 4, 5, 6, 7); } \
        } else H[((i) - 8) >> 1][(i) & 1] = tr4(rH + sl + (((i) - 8) >> 1) * 256 + ((i) & 1) * 2048); \
    } while (0)
#define MM(i, HC, WC) mfma_acc(ac2[(i) & 3][(i) >> 2], cat8(WC[(i) & 3][0], WC[(i) & 3][1]), cat8(HC[(i) >> 2][0], HC[(i) >> 2][1]))
#define STEP(RD, HC, WC, HN, WN) do { \
        _Pragma("unroll") for (int i = 0; i < 32; ++i) { MM(i, HC, WC); if (i < 24) RD1(RD, i, HN, WN); } \
        sl = sl == 4 * 32768 ? 0 : sl + 32768; LGKM0(); \
    } while (0)

    LGKM0();
    BAR();
#pragma unroll
    for (int i = 0; i < 24; ++i) RD1(1, i, HX, WX);
    sl = 32768; LGKM0();
    BAR();
#pragma unroll
    for (int i = 0; i < 32; ++i) {
        asm volatile("v_mfma_f32_16x16x32_bf16 %0, %1, %2, 0" : "=&v"(ac2[i & 3][i >> 2]) : "v"(cat8(WX[i & 3][0], WX[i & 3][1])), "v"(cat8(HX[i >> 2][0], HX[i >> 2][1])) : "memory");
        if (i < 24) RD1(1, i, HY, WY);
    }
    sl = 2 * 32768; LGKM0();
    BAR(); STEP(1, HY, WY, HX, WX);
    const int nf = late ? 15 : 11;
#pragma nounroll
    for (int i = 1; i < nf; ++i) {
        if (i == 3) WAIT_V(0);
        BAR(); STEP(1, HX, WX, HY, WY); BAR(); STEP(1, HY, WY, HX, WX);
    }
    BAR(); STEP(1, HX, WX, HY, WY); BAR(); STEP(0, HY, WY, HX, WX);
#pragma nounroll
    for (int i = late ? -SHIFT / 2 : SHIFT / 2; i < 7; ++i) { BAR(); STEP(0, HX, WX, HY, WY); BAR(); STEP(0, HY, WY, HX, WX); }
    BAR(); STEP(0, HX, WX, HY, WY);
    {
        const int sw = rs0 + wid;
        LAS char* dmp = lds + (sw >= 5 ? sw - 5 : sw) * 32768 + fq2 * 256;
#define DUMP(i) *(LAS f32x4*)(dmp + (i) * 1024 + ((fr2 ^ (4 * ((i) & 3) + fq2)) * 16)) = ac2[(i) & 3][(i) >> 2]
#pragma unroll
        for (int i = 0; i < 32; ++i) { MM(i, HY, WY); if (i >= 12) DUMP(i - 12); }
        asm volatile("s_nop 15" ::: "memory");
#pragma unroll
        for (int i = 20; i < 32; ++i) DUMP(i);
    }
    WAIT_L0();
    BAR();
    asm volatile(".Lk1_end_b:");
}

extern "C" void kernel_launch(void* const* d_in, const int* in_sizes, int n_in, void* d_out, int out_size, void* d_ws, size_t ws_size,
                              hipStream_t stream) {
    const u16* x = (const u16*)d_in[0];
    const u16* lre = (const u16*)d_in[1];
    const u16* lim = (const u16*)d_in[2];
    const u16* Bre = (const u16*)d_in[3];
    const u16* Bim = (const u16*)d_in[4];
    const u16* Cre = (const u16*)d_in[5];
    const u16* Cim = (const u16*)d_in[6];
    const u16* logdt = (const u16*)d_in[7];
    const u16* dskip = (const u16*)d_in[8];
    const u16* Wg = (const u16*)d_in[10];
    const u16* Wu = (const u16*)d_in[11];
    const u16* Wd = (const u16*)d_in[12];
    const u16* gw = (const u16*)d_in[13];
    char* ws = (char*)d_ws;
    (void)hipFuncSetAttribute((const void*)k1_fused, hipFuncAttributeMaxDynamicSharedMemorySize, K2_LDS);
    k1_fused<<<256, 512, K2_LDS, stream>>>(x, Wg, Wu, Bre, Bim, lre, lim, logdt, gw, Cre, Cim, Wd, dskip, ws, (float*)d_out);
}
```

```cpp
#include <hip/hip_runtime.h>
#include <hip/hip_bf16.h>


typedef unsigned short u16;
typedef __bf16 bf16x8 __attribute__((ext_vector_type(8)));
typedef __bf16 bf16x2 __attribute__((ext_vector_type(2)));
typedef short s16x4 __attribute__((ext_vector_type(4)));
typedef short s16x8 __attribute__((ext_vector_type(8)));
typedef float f32x4 __attribute__((ext_vector_type(4)));
typedef float f32x2 __attribute__((ext_vector_type(2)));
typedef unsigned u32x4 __attribute__((ext_vector_type(4)));
typedef unsigned u32x2 __attribute__((ext_vector_type(2)));
#define LAS __attribute__((address_space(3)))
#define GAS __attribute__((address_space(1)))
typedef GAS unsigned gu32;
typedef GAS unsigned long long gu64;
typedef __attribute__((address_space(4))) const unsigned long long* kptr;
#define RLX_AGENT __ATOMIC_RELAXED, __HIP_MEMORY_SCOPE_AGENT

constexpr int SEQ = 2048, DM = 1024, NP = 1024, FF = 3584;
constexpr int KSTEPS2 = (2 * NP + FF) / 64;

constexpr unsigned WS_CARRY = 0;
constexpr unsigned WS_G = 7 * 8192;
constexpr unsigned WS_RDY = 65536;
constexpr unsigned WS_FLAG2 = WS_RDY + 1024;
constexpr unsigned WS_HQ = WS_FLAG2 + 16384;
constexpr unsigned WS_HA = WS_HQ + 1024;
constexpr unsigned WS_HR = WS_HA + 1024;
constexpr unsigned WS_SYNC_BYTES = WS_HR + 2048;
constexpr size_t WS_A2 = 1 << 20;
constexpr unsigned A2S = 262144 + 128;
constexpr size_t WS_A2_BYTES = (size_t)KSTEPS2 * A2S;

__device__ __forceinline__ float bflo(unsigned v) { return __uint_as_float(v << 16); }
__device__ __forceinline__ float bfhi(unsigned v) { return __uint_as_float(v & 0xffff0000u); }
__device__ __forceinline__ float rbf(float f) { return (float)(__bf16)f; }
__device__ __forceinline__ unsigned pk2(float a, float b) {
    bf16x2 v; v.x = (__bf16)a; v.y = (__bf16)b; return __builtin_bit_cast(unsigned, v);
}
__device__ __forceinline__ float relu(float v) { return __int_as_float(max(__float_as_int(v), 0)); }
__device__ __forceinline__ float dot2(unsigned a, unsigned b, float c) {
    return __builtin_amdgcn_fdot2_f32_bf16(__builtin_bit_cast(bf16x2, a), __builtin_bit_cast(bf16x2, b), c, false);
}

#define WAIT_V(n) asm volatile("s_waitcnt vmcnt(" #n ")" ::: "memory")
#define WAIT_L0() asm volatile("s_waitcnt lgkmcnt(0)" ::: "memory")
#define LGKM0() __builtin_amdgcn_s_waitcnt(0xC07F)
#define BAR() do { asm volatile("" ::: "memory"); __builtin_amdgcn_s_barrier(); asm volatile("" ::: "memory"); } while (0)
#define SCHED() __builtin_amdgcn_sched_barrier(0)
#define SRD(p, bytes) __builtin_amdgcn_make_buffer_rsrc((void*)(p), (short)0, (int)(bytes), 0x00020000)

__device__ __forceinline__ void dma16(unsigned m0v, unsigned voff, __amdgpu_buffer_rsrc_t srd, unsigned soff) {
    asm volatile("s_mov_b32 m0, %0\n\ts_nop 0\n\tbuffer_load_dwordx4 %1, %2, %3 offen lds" ::"s"(m0v), "v"(voff), "s"(srd), "s"(soff) : "m0", "memory");
}
__device__ __forceinline__ bf16x8 lds_b128(LAS char* p) { return __builtin_bit_cast(bf16x8, *(LAS s16x8*)p); }
__device__ __forceinline__ bf16x8 lds_tr(LAS char* p0, LAS char* p1) {
    s16x4 a = __builtin_amdgcn_ds_read_tr16_b64_v4i16((LAS s16x4*)p0);
    s16x4 b = __builtin_amdgcn_ds_read_tr16_b64_v4i16((LAS s16x4*)p1);
    return __builtin_bit_cast(bf16x8, __builtin_shufflevector(a, b, 0, 1, 2, 3, 4, 5, 6, 7));
}
__device__ __forceinline__ f32x2 cmul(f32x2 a, f32x2 b) { return (f32x2){__builtin_fmaf(a.x, b.x, -(a.y * b.y)), __builtin_fmaf(a.x, b.y, a.y * b.x)}; }
__device__ __forceinline__ f32x2 cfma(f32x2 a, f32x2 b, f32x2 c) {
    return (f32x2){__builtin_fmaf(a.x, b.x, __builtin_fmaf(-a.y, b.y, c.x)), __builtin_fmaf(a.x, b.y, __builtin_fmaf(a.y, b.x, c.y))};
}
__device__ __forceinline__ f32x2 sel4(int i, f32x2 a, f32x2 b, f32x2 c, f32x2 d) {
    const bool o = i & 1, u = i & 2;
    const float lx = o ? b.x : a.x, ly = o ? b.y : a.y, hx = o ? d.x : c.x, hy = o ? d.y : c.y;
    return (f32x2){u ? hx : lx, u ? hy : ly};
}

constexpr int K2_LDS = 5 * 32768;
constexpr int SHIFT = 2;
constexpr int PF_D0 = 4;

__device__ __forceinline__ void dma4c(unsigned m0v, unsigned voff, __amdgpu_buffer_rsrc_t srd, unsigned soff) {
    asm volatile("s_mov_b32 m0, %0\n\ts_nop 0\n\tbuffer_load_dwordx4 %1, %2, %3 offen sc1 lds\n\tbuffer_load_dwordx4 %1, %2, %3 offen offset:1024 sc1 lds\n\t"
                 "buffer_load_dwordx4 %1, %2, %3 offen offset:2048 sc1 lds\n\tbuffer_load_dwordx4 %1, %2, %3 offen offset:3072 sc1 lds" ::"s"(m0v), "v"(voff), "s"(srd), "s"(soff) : "m0", "memory");
}
__device__ __forceinline__ void dma4(unsigned m0v, unsigned voff, __amdgpu_buffer_rsrc_t srd, unsigned s0, unsigned s1, unsigned s2, unsigned s3) {
    asm volatile("s_mov_b32 m0, %0\n\ts_nop 0\n\tbuffer_load_dwordx4 %1, %2, %3 offen lds\n\tbuffer_load_dwordx4 %1, %2, %4 offen offset:1024 lds\n\t"
                 "buffer_load_dwordx4 %1, %2, %5 offen offset:2048 lds\n\tbuffer_load_dwordx4 %1, %2, %6 offen offset:3072 lds" ::"s"(m0v), "v"(voff), "s"(srd), "s"(s0), "s"(s1), "s"(s2), "s"(s3) : "m0", "memory");
}
__device__ __forceinline__ void mfma_acc(f32x4& c, bf16x8 a, bf16x8 b) {
    asm volatile("v_mfma_f32_16x16x32_bf16 %0, %1, %2, %0" : "+v"(c) : "v"(a), "v"(b) : "memory");
}
__device__ __forceinline__ void mfma_acc2(f32x4& c, bf16x8 a0, bf16x8 b0, bf16x8 a1, bf16x8 b1) {
    asm volatile("v_mfma_f32_16x16x32_bf16 %0, %1, %2, %0\n\tv_mfma_f32_16x16x32_bf16 %0, %3, %4, %0" : "+v"(c) : "v"(a0), "v"(b0), "v"(a1), "v"(b1) : "memory");
}
__device__ __forceinline__ s16x4 tr4(LAS char* p) { return __builtin_amdgcn_ds_read_tr16_b64_v4i16((LAS s16x4*)p); }
__device__ __forceinline__ bf16x8 cat8(s16x4 a, s16x4 b) { return __builtin_bit_cast(bf16x8, __builtin_shufflevector(a, b, 0, 1, 2, 3, 4, 5, 6, 7)); }

constexpr int K1_ASB = 40960;
constexpr int K1_W = 81920;
constexpr int K1_WBUF = 28704, K1_UOFF = 14352;
constexpr int K1_ETAB = K1_W + 2 * K1_WBUF;
constexpr int K1_CTAB = K1_ETAB + 32 * 144;
constexpr int K1_SINK = K1_CTAB + 32 * 96;
constexpr int K1_GTAB = K1_SINK + 1024;
constexpr int K1_HS = K1_GTAB + 1024;

__global__ __launch_bounds__(512, 2) void k1_fused(const u16* __restrict__ x, const u16* __restrict__ Wg, const u16* __restrict__ Wu,
                                                       const u16* __restrict__ Bre, const u16* __restrict__ Bim, const u16* __restrict__ lre,
                                                       const u16* __restrict__ lim, const u16* __restrict__ logdt, const u16* __restrict__ gw,
                                                       const u16* __restrict__ Cre, const u16* __restrict__ Cim, const u16* __restrict__ Wd,
                                                       const u16* __restrict__ dskip, char* __restrict__ ws, float* __restrict__ out) {
    extern __shared__ __attribute__((aligned(16))) char smem[];
    LAS char* lds = (LAS char*)smem;
    const unsigned ldsb = (unsigned)(size_t)lds;
    const int tid = threadIdx.x, lane = tid & 63;
    const int wid = __builtin_amdgcn_readfirstlane(tid >> 6);
    const int wr = wid & 3, wc = wid >> 2;
    const int bid = blockIdx.x, idx = bid >> 3, j = (bid & 7) * 4 + (idx & 3), pm = idx >> 2;
    const int fr = lane & 15, fq = lane >> 4;
#define ROLE(b_, i_) const int xc = (b_) & 7, late = (xc & 1), mb2 = (xc >> 2) * 8 + ((i_) >> 2), nblk = ((xc >> 1) & 1) * 4 + ((i_) & 3), tile = mb2 * 8 + nblk
    const char* code0; unsigned codeA, codeB, codeM;
    asm volatile("s_getpc_b64 %0\n.Lk1_ref:\n\ts_mov_b32 %1, .Lk1_end_a-.Lk1_ref\n\ts_mov_b32 %2, .Lk1_end_b-.Lk1_ref\n\ts_mov_b32 %3, .Lk1_mid-.Lk1_ref"
                 : "=s"(code0), "=s"(codeA), "=s"(codeB), "=s"(codeM));
    const __amdgpu_buffer_rsrc_t sI = SRD(code0, (codeA > codeB ? codeA : codeB) + 192);
    const unsigned mSink = ldsb + K1_SINK;
    const bool pf = wid == 7;
    if (pf) { dma16(mSink, (unsigned)(lane * 128), sI, 0u); dma16(mSink, (unsigned)(lane * 128), sI, 8192u); }

    const int ch = 32 * j + 8 * (fq & 1) + 4 * (fr >> 3) + 16 * ((fr >> 2) & 1) + (fr & 3);
    const int grow = pm * 256 + j * 8 + wid;
    u32x4 xa, xb, ga, gb; unsigned plr, pli, pld;
    {
        const u16* xr = x + (size_t)grow * DM;
        const unsigned l16 = lane * 16, c2 = ch * 2;
        asm volatile("s_nop 4\n\tglobal_load_dwordx4 %0, %7, %9\n\tglobal_load_dwordx4 %1, %7, %9 offset:1024\n\t"
                     "global_load_dwordx4 %2, %7, %10\n\tglobal_load_dwordx4 %3, %7, %10 offset:1024\n\t"
                     "global_load_ushort %4, %8, %11\n\tglobal_load_ushort %5, %8, %12\n\tglobal_load_ushort %6, %8, %13"
                     : "=&v"(xa), "=&v"(xb), "=&v"(ga), "=&v"(gb), "=&v"(plr), "=&v"(pli), "=&v"(pld)
                     : "v"(l16), "v"(c2), "s"(xr), "s"(gw), "s"(lre), "s"(lim), "s"(logdt) : "memory");
    }

    const int q4 = wid + 28, q5 = wid + 35;
    const __amdgpu_buffer_rsrc_t sX = SRD(x, SEQ * DM * 2), sG = SRD(Wg, DM * FF * 2), sU = SRD(Wu, DM * FF * 2);
    const __amdgpu_buffer_rsrc_t s4 = SRD(q4 < 32 ? x : (q4 & 2) ? Bim : Bre, q4 < 32 ? SEQ * DM * 2 : NP * DM * 2);
    const __amdgpu_buffer_rsrc_t s5 = SRD((q5 & 2) ? Bim : Bre, q5 < 40 ? NP * DM * 2 : 0);
    const int l3 = lane >> 3;
    const unsigned voffX = (unsigned)(((l3 >> 2) * 16 + (l3 & 3)) * 2048 + (((lane & 7) ^ (l3 & 6)) * 16));
    unsigned voffW;
    { const int rho = (tid * 4682) >> 16, c = tid - rho * 14, k = (rho & 0x13) | ((rho & 4) << 1) | ((rho & 8) >> 1);
      voffW = (unsigned)(k * (FF * 2) + c * 16); }
#define XROW(q) (pm * 256 + ((q) >> 3) * 64 + (((q) >> 2) & 1) * 8 + ((q) & 1) * 32 + (((q) >> 1) & 1) * 4)
#define BROW(q) (32 * j + 8 * (((q) >> 2) & 1) + 4 * ((q) & 1))
    constexpr unsigned WST = 64 * FF * 2, WSH = 32 * FF * 2;
    const unsigned so0 = (unsigned)(XROW(wid) * 2048), so1 = (unsigned)(XROW(wid + 7) * 2048), so2 = (unsigned)(XROW(wid + 14) * 2048), so3 = (unsigned)(XROW(wid + 21) * 2048),
                   so4 = (unsigned)((q4 < 32 ? XROW(q4) : BROW(q4)) * 2048), so5 = (unsigned)(BROW(q5) * 2048);
    const unsigned soW = (unsigned)(j * 224), mb0 = ldsb + wid * 1024;
    const unsigned m50 = q5 < 40 ? mb0 + 35 * 1024 : mSink, m51 = q5 < 40 ? mb0 + 35 * 1024 + K1_ASB : mSink;
#define ST_X(i, ko, b) dma16(mb0 + (b) * K1_ASB + (i) * 7 * 1024, voffX, sX, ((i) == 0 ? so0 : (i) == 1 ? so1 : (i) == 2 ? so2 : so3) + (ko))
#define ST_S4(ko, b) dma16(mb0 + (b) * K1_ASB + 28 * 1024, voffX, s4, so4 + (ko))
#define ST_S5(ko, b) dma16((b) ? m51 : m50, voffX, s5, so5 + (ko))
#define ST_G(h, t, b) dma16(mb0 + K1_W + (b) * K1_WBUF + (h) * 7168, voffW, sG, soW + (t) * WST + (h) * WSH)
#define ST_U(h, t, b) dma16(mb0 + K1_W + (b) * K1_WBUF + K1_UOFF + (h) * 7168, voffW, sU, soW + (t) * WST + (h) * WSH)
    if (!pf) { ST_X(0, 0, 0); ST_X(1, 0, 0); ST_X(2, 0, 0); ST_X(3, 0, 0); ST_S4(0, 0); ST_S5(0, 0); ST_G(0, 0, 0); ST_G(1, 0, 0); ST_U(0, 0, 0); ST_U(1, 0, 0); }

    const int ch0 = (fq ^ (fr & 6)) * 16;
    LAS char* rA0 = lds + wr * 8192 + fr * 128 + ch0;
    LAS char* rA1 = lds + wr * 8192 + fr * 128 + (ch0 ^ 64);
    LAS char* rS0 = lds + 32768 + wc * 4096 + fr * 128 + ch0;
    LAS char* rS1 = lds + 32768 + wc * 4096 + fr * 128 + (ch0 ^ 64);
    const int trow = (16 * (fq >> 1) + 4 * (fq & 1) + ((lane & 15) >> 2)) * 224;
    LAS char* rW = lds + K1_W + trow + wc * 96 + (lane & 3) * 8;
    LAS char* rM = lds + K1_W + trow + 192 + wc * 16 + (lane & 1) * 8 + ((lane >> 1) & 1) * K1_UOFF;

    f32x4 acc[4][9];
#pragma unroll
    for (int m = 0; m < 4; ++m)
#pragma unroll
        for (int n = 0; n < 9; ++n) { acc[m][n] = (f32x4){0.f, 0.f, 0.f, 0.f}; asm volatile("" : "+v"(acc[m][n])); }
    bf16x8 Af[4][2], Bf[3][2];

#define RD_A(m, b) do { Af[m][0] = lds_b128(rA0 + (b) * K1_ASB + (m) * 2048); Af[m][1] = lds_b128(rA1 + (b) * K1_ASB + (m) * 2048); } while (0)
#define RD_S(slot, ri, b) do { Bf[slot][0] = lds_b128(rS0 + (b) * K1_ASB + (ri) * 2048); Bf[slot][1] = lds_b128(rS1 + (b) * K1_ASB + (ri) * 2048); } while (0)
#define RD_W(slot, base, off) do { Bf[slot][0] = lds_tr((base) + (off), (base) + (off) + 1792); Bf[slot][1] = lds_tr((base) + (off) + 7168, (base) + (off) + 7168 + 1792); } while (0)
#define SEC1(b, d0, d1) do { RD_S(0, 0, b); RD_S(1, 1, b); d0; RD_W(2, rW, (b) * K1_WBUF); RD_A(0, b); d1; RD_A(1, b); RD_A(2, b); RD_A(3, b); } while (0)
#define SEC2(b, d0, d1, d2, d3) do { d0; RD_W(0, rW, (b) * K1_WBUF + 32); d1; RD_W(1, rW, (b) * K1_WBUF + 64); d2; RD_W(2, rM, (b) * K1_WBUF); d3; } while (0)
#define SEC3(b, d0, d1, d2, d3) do { d0; RD_W(0, rW, (b) * K1_WBUF + K1_UOFF); d1; RD_W(1, rW, (b) * K1_WBUF + K1_UOFF + 32); d2; RD_W(2, rW, (b) * K1_WBUF + K1_UOFF + 64); d3; } while (0)
#define GO(n0, n1, n2) do { asm volatile("" ::: "memory"); LGKM0(); BAR(); SCHED(); \
        _Pragma("unroll") for (int m = 0; m < 4; ++m) { \
            mfma_acc2(acc[m][n0], Af[m][0], Bf[0][0], Af[m][1], Bf[0][1]); mfma_acc2(acc[m][n1], Af[m][0], Bf[1][0], Af[m][1], Bf[1][1]); \
            mfma_acc2(acc[m][n2], Af[m][0], Bf[2][0], Af[m][1], Bf[2][1]); } \
        SCHED(); } while (0)

#define RELANE() int tz = threadIdx.x; asm volatile("" : "+v"(tz)); const int er = tz & 15, eq = (tz >> 4) & 3
    asm volatile("" : "+v"(rA0), "+v"(rA1), "+v"(rS0), "+v"(rS1), "+v"(rW), "+v"(rM));
    WAIT_V(10);
    if (pf) WAIT_V(0);
    asm volatile("" : "+v"(xa), "+v"(xb), "+v"(ga), "+v"(gb), "+v"(plr), "+v"(pli), "+v"(pld) :: "memory");
    {
        float s = 0.f, s2 = 0.f;
#pragma unroll
        for (int e = 0; e < 4; ++e) { s = dot2(xa[e], ga[e], s); s2 = dot2(xb[e], gb[e], s2); }
        s += s2;
#define DPP_ADD(ctrl, rmask) s += __int_as_float(__builtin_amdgcn_update_dpp(0, __float_as_int(s), ctrl, rmask, 0xf, true))
        DPP_ADD(0x111, 0xf); DPP_ADD(0x112, 0xf); DPP_ADD(0x114, 0xf); DPP_ADD(0x118, 0xf); DPP_ADD(0x142, 0xa); DPP_ADD(0x143, 0xc);
        const float g = rbf(__builtin_amdgcn_rcpf(1.f + __builtin_amdgcn_exp2f(-1.4426950408889634f * rbf(s))));
        if (lane == 63) __hip_atomic_store((gu32*)(ws + WS_G) + grow, pk2(g, 1.f - g), RLX_AGENT);
    }
    if (!pf) {
        BAR();
        ST_X(0, 128, 1); ST_X(1, 128, 1); ST_X(2, 128, 1); ST_X(3, 128, 1); ST_S4(128, 1); ST_S5(128, 1); ST_G(0, 1, 1); ST_G(1, 1, 1);
        WAIT_V(11);
        if (wc == 1) BAR();
#pragma nounroll
        for (int t = 0; t < 16; t += 2) {
            asm volatile("" : "+s"(t));
            const unsigned ko = t == 14 ? 0x80000000u : (unsigned)(t + 2) * 128u;
            BAR(); SEC1(0, ST_U(0, t + 1, 1), ST_U(1, t + 1, 1)); WAIT_V(10);
            GO(7, 8, 0);
            BAR(); SEC2(0, ST_X(0, ko, 0), ST_X(1, ko, 0), ST_X(2, ko, 0), ST_X(3, ko, 0));
            GO(1, 2, 6);
            BAR(); SEC3(0, ST_G(0, t + 2, 0), ST_G(1, t + 2, 0), ST_S4(ko, 0), ST_S5(ko, 0)); WAIT_V(10);
            GO(3, 4, 5);
            BAR(); SEC1(1, ST_U(0, t + 2, 0), ST_U(1, t + 2, 0)); WAIT_V(10);
            GO(7, 8, 0);
            BAR(); SEC2(1, ST_X(0, ko + 128, 1), ST_X(1, ko + 128, 1), ST_X(2, ko + 128, 1), ST_X(3, ko + 128, 1));
            GO(1, 2, 6);
            BAR(); SEC3(1, ST_G(0, t + 3, 1), ST_G(1, t + 3, 1), ST_S4(ko + 128, 1), ST_S5(ko + 128, 1)); WAIT_V(10);
            GO(3, 4, 5);
        }
    }
    int w7 = wid; asm volatile("" : "+s"(w7));
    if (w7 == 7) {
        {
            const float lr = bflo(plr), li = bflo(pli), dt = rbf(expf(bflo(pld)));
            const float e = expf(dt * lr); float sn, cs; sincosf(dt * li, &sn, &cs);
            const f32x2 a1 = {e * cs, e * sn};
            const float nr = a1.x - 1.f, den = lr * lr + li * li;
            f32x2 a16 = cmul(a1, a1); a16 = cmul(a16, a16); a16 = cmul(a16, a16); a16 = cmul(a16, a16);
            const f32x2 a32 = cmul(a16, a16), a48 = cmul(a32, a16), a64 = cmul(a32, a32), a128 = cmul(a64, a64), a192 = cmul(a128, a64), a256 = cmul(a128, a128);
            if (lane < 32) {
                LAS f32x4* ct = (LAS f32x4*)(lds + K1_CTAB + lane * 96);
                ct[0] = (f32x4){a1.x, a1.y, 2.f * ((nr * lr + a1.y * li) / den), 2.f * ((a1.y * lr - nr * li) / den)};
                ct[1] = (f32x4){1.f, 0.f, a16.x, a16.y}; ct[2] = (f32x4){a32.x, a32.y, a48.x, a48.y};
                ct[3] = (f32x4){1.f, 0.f, a64.x, a64.y}; ct[4] = (f32x4){a128.x, a128.y, a192.x, a192.y};
                ct[5] = (f32x4){a256.x, a256.y, 0.f, 0.f};
            }
        }
        const int pb = (bid >> 3) | ((bid & 7) << 5);
        const __amdgpu_buffer_rsrc_t sPW = SRD((pb & 1) ? Wg : Wu, DM * FF * 2), sPD = SRD((pb & 1) ? Wd : (pb & 2) ? Cim : Cre, (pb & 1) ? DM * FF * 2 : DM * NP * 2);
        const __amdgpu_buffer_rsrc_t sH = SRD(ws, WS_SYNC_BYTES);
        const unsigned l128 = lane * 128, OUT = 0x80000000u;
        const unsigned pds = (pb & 1) ? (1u << 20) : (1u << 19), pw0 = (unsigned)((pb >> 1) * 8192), pd0 = (unsigned)(((pb & 1) ? pb >> 1 : pb >> 2) * 8192);
#define PF_W(r) dma16(mSink, l128, sPW, pw0 + (unsigned)(r) * (1u << 20))
#define PF_D(r) dma16(mSink, l128, sPD, pd0 + (unsigned)(r) * pds)
#define RFL(v) ((unsigned)__builtin_amdgcn_readfirstlane(v))
#define HDMA(m, vo, srd, so) asm volatile("s_nop 4\n\ts_mov_b32 m0, %0\n\ts_nop 0\n\tbuffer_load_dwordx4 %1, %2, %3 offen sc1 lds" ::"s"((unsigned)(m)), "v"(vo), "s"(srd), "s"((unsigned)(so)) : "m0", "memory")
#define HLD(op, v, vo) asm volatile("s_nop 4\n\t" op " %0, %1, %2, 0 offen sc1\n\ts_waitcnt vmcnt(0)" : "=v"(v) : "v"(vo), "s"(sH) : "memory")
#define HST32(off, v) do { __builtin_amdgcn_raw_buffer_store_b32((unsigned)(v), sH, off, 0, 16); asm volatile("" ::: "memory"); } while (0)
#define HST64(off, lo_, hi2_) do { __builtin_amdgcn_raw_buffer_store_b64((u32x2){(unsigned)(lo_), (unsigned)(hi2_)}, sH, off, 0, 16); asm volatile("" ::: "memory"); } while (0)
#define REPLY(q_) do { _Pragma("unroll") for (int i = 0; i < 4; ++i) HST64(WS_HR + l16 * 2 + i * 8, (q_)[i], tag); } while (0)
#define LANE0(off) (l16 == 0 ? (unsigned)(off) : OUT)
#define ALLANS(a_) __all((a_)[0] == tag && (a_)[1] == tag && (a_)[2] == tag && (a_)[3] == tag)
#define ATX(cond, ...) do { int t_ = t; asm volatile("" : "+s"(t_)); if (cond) { unsigned l16 = l128; asm volatile("" : "+v"(l16)); l16 >>= 3; __VA_ARGS__ } } while (0)
#define AT(v, ...) ATX(t_ == (v), __VA_ARGS__)
#define AT2(v, ...) ATX((unsigned)(t_ - (v)) <= 2u, __VA_ARGS__)
        unsigned ask = 0, tag = 0, done = 0;
        const int boss = bid == 0 ? 0 : 64;
        BAR();
        HDMA(ldsb + K1_HS + 1024, l128 >> 3, sH, WS_HR + (bid >> 7) * 1024);
        HDMA(ldsb + K1_HS, l128 >> 3, sH, bid == 0 ? WS_HA : OUT);
        PF_W(1);
        BAR();
#pragma nounroll
        for (int t = 0; t < 16; t += 2) {
            asm volatile("" : "+s"(t));
            BAR(); SEC1(0, , );
            AT(10,
                HDMA(mSink, l128, sI, codeM); HDMA(mSink, l128, sI, codeM + 8192); HDMA(mSink, l128, sI, codeM + 16384););
            AT(12, HDMA(ldsb + K1_HS, l16, sH, WS_HA););
            GO(7, 8, 0);
            BAR(); SEC2(0, , , , );
            AT(0,
                ROLE(bid, idx);
                HST64((pm < 7 ? WS_CARRY + pm * 8192 + j * 256 : OUT) + (l16 < 32 * 16 ? l16 >> 1 : OUT >> 1), ~0u, ~0u);
                HST32(l16 == 4 * 16 ? WS_RDY + (pm * 32 + j) * 4 : (late ? OUT : WS_FLAG2 + tile * 128) + (l16 < 4 * 16 ? l16 * 2 : OUT >> 1), ~0u););
            AT(2,
                const __amdgpu_buffer_rsrc_t sA = SRD(ws + WS_A2, WS_A2_BYTES), sO = SRD(out, SEQ * DM * 4);
                const unsigned vo = (l16 >> 4) * A2S, vo2 = l16 << 13;
                HDMA(mSink, vo, sA, A2S - 128); HDMA(mSink, vo, sA, 65 * A2S - 128); HDMA(mSink, vo2, sO, 0u););
            AT2(4 + boss,
                WAIT_V(2);
                const u32x4 q = *(LAS u32x4*)(lds + K1_HS + l16), qp = *(LAS u32x4*)(lds + K1_GTAB + l16);
                if (t_ == 4 || __any(q[0] != qp[0] || q[1] != qp[1] || q[2] != qp[2] || q[3] != qp[3])) REPLY(q);
                *(LAS u32x4*)(lds + K1_GTAB + l16) = q;);
            AT2(6 + (int)done * 64, HDMA(ldsb + K1_HS + 1024, l16, sH, WS_HR + (bid >> 7) * 1024););
            GO(1, 2, 6);
            BAR(); SEC3(0, , PF_W(t + 2), PF_D(t - PF_D0), );
            GO(3, 4, 5);
            BAR(); SEC1(1, , );
            AT2(2 + boss, HDMA(ldsb + K1_HS, l16, sH, WS_HQ););
            AT(14, WAIT_V(0););
            GO(7, 8, 0);
            BAR(); SEC2(1, , , , );
            AT(0,
                WAIT_V(5);
                ask = RFL(*(LAS unsigned*)(lds + K1_HS + 1024 + (bid & 127) * 8)) + 1u;
                const u32x4 old = *(LAS u32x4*)(lds + K1_HS + l16);
                HST32(LANE0(WS_HQ + bid * 4), ask);
                tag = RFL(old[0]) + 1u;
                while (__any(old[0] == tag || old[1] == tag || old[2] == tag || old[3] == tag)) ++tag;);
            AT2(6 + (int)done * 64,
                WAIT_V(2);
                const u32x2 r = *(LAS u32x2*)(lds + K1_HS + 1024 + (bid & 127) * 8);
                if (RFL(r[0]) == ask) { tag = RFL(r[1]); done = 1u; HST32(LANE0(WS_HA + bid * 4), tag); });
            AT(12,
                WAIT_V(2);
                u32x4 ans = *(LAS u32x4*)(lds + K1_HS + l16);
                if (!(done && ALLANS(ans))) {
                    unsigned spins = 0;
                    _Pragma("nounroll") do {
                        if (bid == 0) { u32x4 q; HLD("buffer_load_dwordx4", q, WS_HQ + l16); REPLY(q); }
                        if (!done) {
                            u32x2 r; HLD("buffer_load_dwordx2", r, WS_HR + bid * 8);
                            if (RFL(r[0]) == ask) { tag = RFL(r[1]); done = 1u; HST32(LANE0(WS_HA + bid * 4), tag); }
                        }
                        HLD("buffer_load_dwordx4", ans, WS_HA + l16);
                    } while (!(done && ALLANS(ans)) && ++spins < (1u << 18));
                }
                HDMA(ldsb + K1_GTAB, l16, sH, WS_G + pm * 1024););
            GO(1, 2, 6);
            BAR(); SEC3(1, , PF_W(t + 3), PF_D(t + 1 - PF_D0), );
            GO(3, 4, 5);
        }
    }
    asm volatile(".Lk1_mid:\n\ts_nop 15" ::: "memory"); SCHED();
    if (wc == 0) BAR();
    RELANE();
    kptr ka = (kptr)__builtin_amdgcn_kernarg_segment_ptr(); asm volatile("" : "+s"(ka));
#define KARG(T, i) ((T)ka[i])
    char* const ws2 = KARG(char*, 13);

    LAS char* ctab = lds + K1_CTAB + (wc * 16 + er) * 96;
    LAS char* etab = lds + K1_ETAB + (wc * 16 + er) * 144;
    const f32x4 cst = *(LAS f32x4*)ctab;
    const f32x2 a1 = {cst[0], cst[1]}, cf = {cst[2], cst[3]};
    u32x4 gt[4];
#pragma unroll
    for (int m = 0; m < 4; ++m) gt[m] = *(LAS u32x4*)(lds + K1_GTAB + wr * 256 + eq * 64 + m * 16);
    const __amdgpu_buffer_rsrc_t sA2 = SRD(ws2 + WS_A2, WS_A2_BYTES);
#define ST16(voff, v) __builtin_amdgcn_raw_buffer_store_b128(v, sA2, voff, 0, 16)
#define PK8(v) (u32x4){pk2(v[0], v[1]), pk2(v[2], v[3]), pk2(v[4], v[5]), pk2(v[6], v[7])}
    const int mblk = pm * 2 + (wr >> 1);
#define IMG_Q(r, q) (unsigned)(((q) >> 1) * 256 + ((q) & 1) * 16 + ((r) & 3) * 32 + mblk * 16384 + (wr & 1) * 1024)
#define IMG_T(r, q) (IMG_Q(r, q) + (unsigned)((((r) >> 2) & 1) * 2048 + ((r) >> 3) * 128 + (j >> 2) * A2S + (j & 3) * 4096))
#define IMG_M(r, q) (IMG_Q(r, q) + (unsigned)((((r) >> 2) & 1) * 2048 + ((r) >> 3) * 512 + (24 + (j >> 2)) * A2S + (j & 3) * 4096))
#define IMG_S(r, q) (IMG_Q(r, q) + (unsigned)((((r) >> 2) & 1) * 4096 + ((r) >> 3) * 2048 + (32 + (j >> 1)) * A2S + (j & 1) * 8192))
    const unsigned lo = IMG_T(er, eq);
#define GLU_HALF(n, hh, o) do { float v[8]; \
        _Pragma("unroll") for (int e = 0; e < 8; ++e) { const int m = 2 * (hh) + (e >> 2), q = e & 3; v[e] = relu(acc[m][n][q]) * (acc[m][3 + (n)][q] * bfhi(gt[m][q])); } \
        o = PK8(v); } while (0)
#define GLU_TILE(n) do { u32x4 o_; GLU_HALF(n, 0, o_); ST16(lo + (wc * 48 + 8 * (n)) * A2S, o_); GLU_HALF(n, 1, o_); ST16(lo + (wc * 48 + 8 * (n)) * A2S + 512, o_); } while (0)
    GLU_TILE(0);
    GLU_TILE(1);
    asm volatile("" ::: "memory");
    {
        f32x2 h = {acc[0][7][0], acc[0][8][0]};
#pragma unroll
        for (int e = 1; e < 16; ++e) h = cfma(a1, h, (f32x2){acc[e >> 2][7][e & 3], acc[e >> 2][8][e & 3]});
        *(LAS f32x2*)(etab + (wr * 4 + eq) * 8) = h;
    }
    WAIT_V(0); WAIT_L0();
    BAR();
    gu32* rdy = (gu32*)(ws2 + WS_RDY);
    if (tz == 192) __hip_atomic_store(rdy + pm * 32 + j, 1u, RLX_AGENT);
    gu64* carry = (gu64*)(ws2 + WS_CARRY);
#define CHAN(c, r) (32 * j + 8 * (c) + 4 * ((r) >> 3) + 16 * (((r) >> 2) & 1) + ((r) & 3))
    if (wid < 2 && pm < 7) {
        const f32x2 a16 = *(LAS f32x2*)(lds + K1_CTAB + (wid * 16 + er) * 96 + 24);
        f32x4 E[8];
#pragma unroll
        for (int i = 0; i < 8; ++i) E[i] = *(LAS f32x4*)(lds + K1_ETAB + (wid * 16 + er) * 144 + i * 16);
        f32x2 run = {E[0][0], E[0][1]};
#pragma unroll
        for (int i = 1; i < 16; ++i) run = cfma(a16, run, (f32x2){E[i >> 1][(i & 1) * 2], E[i >> 1][(i & 1) * 2 + 1]});
        unsigned ux = __float_as_uint(run.x), uy = __float_as_uint(run.y);
        if (ux == 0xFFFFFFFFu) ux = 0x7FC00000u;
        if (eq == 0) __hip_atomic_store(carry + pm * 1024 + CHAN(wid, er), ((unsigned long long)uy << 32) | ux, RLX_AGENT);
    }
    GLU_TILE(2);
    {
        float v[8];
        const bool lowh = er < 8;
#pragma unroll
        for (int e = 0; e < 8; ++e) {
            const int m = e >> 2, q = e & 3;
            const float v0 = lowh ? relu(acc[m][6][q]) : acc[m][6][q] * bfhi(gt[m][q]);
            const float v1 = lowh ? relu(acc[2 + m][6][q]) : acc[2 + m][6][q] * bfhi(gt[2 + m][q]);
            const float rcv = __int_as_float(__builtin_amdgcn_mov_dpp(__float_as_int(lowh ? v1 : v0), 0x128, 0xf, 0xf, true));
            v[e] = (lowh ? v0 : v1) * rcv;
        }
        ST16(IMG_M(er, eq) + wc * 128, PK8(v));
    }
    LAS f32x4* park = (LAS f32x4*)(lds + 3 * 32768 + wr * 8192 + (tz & 63) * 16);
    if (wc == 1) {
#pragma unroll
        for (int m = 0; m < 4; ++m) { park[m * 128] = acc[m][7]; park[m * 128 + 64] = acc[m][8]; }
    }
    WAIT_L0();
    BAR();

    ROLE(bid, idx);
    const int n0 = nblk * 128;
    const int rs0 = late ? (48 + SHIFT) % 5 : (40 - SHIFT) % 5;

    if (wc == 1) {
        int ty = threadIdx.x; asm volatile("" : "+v"(ty) :: "memory");
        const int ln = ty & 63, e3 = ln >> 3;
#define LEVEL(lvl) do { if (ty == 256) __hip_atomic_store(rdy + pm * 32 + j, (unsigned)(lvl), RLX_AGENT); } while (0)
        const __amdgpu_buffer_rsrc_t sC = SRD(late ? KARG(const u16*, 10) : KARG(const u16*, 9), DM * NP * 2), sW = SRD(KARG(const u16*, 11), FF * DM * 2);
        const unsigned voffA = (unsigned)(ln * 16);
        const unsigned voffC = (unsigned)(e3 * 2048 + (((ln & 7) ^ (e3 & 6)) * 16));
        const unsigned voffD = (unsigned)(((e3 >> 2) * 8 + (e3 & 3)) * 2048 + ((((ln >> 1) & 3) ^ (e3 >> 1)) * 32) + (ln & 1) * 16);
        const unsigned soA = (unsigned)((late ? 0 : 48) * A2S + mb2 * 16384 + wr * 4096);
        const unsigned soC = (unsigned)((n0 + 32 * wr) * 2048);
        const unsigned soD = (unsigned)(((late ? 0 : 48) + 112 * wr) * 2048 + n0 * 2);
        const unsigned mB = ldsb + wr * 4096;
#define A4(img, sl) dma4c(mB + (sl), voffA, sA2, soA + (img) * A2S)
#define D4(row, sl) do { const unsigned b_ = soD + (row) * 2048; \
            asm volatile("s_mov_b32 m0, %0\n\ts_nop 0\n\tbuffer_load_dwordx4 %1, %2, %3 offen lds\n\tbuffer_load_dwordx4 %1, %2, %4 offen offset:2048 lds\n\t" \
                         "buffer_load_dwordx4 %1, %2, %5 offen offset:3072 lds\n\ts_mov_b32 m0, %6\n\ts_nop 0\n\tbuffer_load_dwordx4 %1, %2, %3 offen offset:128 lds" \
                         ::"s"(mB + (sl) + 16384), "v"(voffD), "s"(sW), "s"(b_), "s"(b_ + 8192 - 2048), "s"(b_ + 8192 + 128 - 3072), "s"(mB + (sl) + 16384 + 1024 - 128) : "m0", "memory"); } while (0)
#define C4(srd, p, sl) do { const unsigned b_ = soC + (p) * 128; dma4(mB + (sl) + 16384, voffC, srd, b_, b_ + 16384 - 1024, b_ + 32768 - 2048, b_ + 49152 - 3072); } while (0)
#define POLL(v, p) asm volatile("global_load_dword %0, %1, off sc1\n\ts_waitcnt vmcnt(0)" : "=v"(v) : "v"(p) : "memory")
#define ASK(v, p) asm volatile("global_load_dword %0, %1, off sc1" : "=v"(v) : "v"(p) : "memory")
#define NEED(v, p, lvl) do { asm volatile("" : "+v"(v) :: "memory"); if (!__all((int)(v) >= (lvl))) { unsigned spins_ = 0, w_; \
            do { __builtin_amdgcn_s_sleep(1); POLL(w_, p); } while (!__all((int)w_ >= (lvl)) && ++spins_ < (1u << 18)); } } while (0)
        gu32* rl = rdy + (mb2 >> 1) * 32 + (ln & 31);
#define XP(k_) (((((k_) & 3) << 1) | (((k_) >> 2) & 1)) ^ (xc & 1))
        unsigned fv;
        D4(448 * XP(0), 0); D4(448 * XP(1), 32768); D4(448 * XP(2), 2 * 32768);
        POLL(fv, rl);
        {
            const int other = ((ln >> 2) ^ xc) & 1;
            unsigned spins = 0;
            while (!__all((int)fv >= 1 || other) && ++spins < (1u << 18)) { __builtin_amdgcn_s_sleep(4); POLL(fv, rl); }
        }
        A4(XP(0), 0); A4(XP(1), 32768); A4(XP(2), 2 * 32768);
        WAIT_V(8); BAR();
        int k = 3, sl = 3 * 32768;
#define NEXT_SLOT() sl = sl == 4 * 32768 ? 0 : sl + 32768
#define STAGE_W() do { const int q_ = k < 16 ? (k & 3) | ((k & 4) << 1) | ((k & 8) >> 1) : k, x_ = XP(q_), row_ = 448 * x_ + ((q_ >> 3) == 3 ? 96 : 16 * (q_ >> 3)); \
            A4((q_ & ~7) + x_, sl); D4(row_, sl); ++k; NEXT_SLOT(); } while (0)
#define LSYNC() do { WAIT_V(16); BAR(); } while (0)
        STAGE_W(); WAIT_V(12); BAR();
        STAGE_W(); LSYNC(); STAGE_W(); LSYNC();
        ASK(fv, rl);
        STAGE_W(); LSYNC(); STAGE_W(); LSYNC();
        NEED(fv, rl, 1);
        STAGE_W(); LSYNC(); STAGE_W(); LSYNC();
        LEVEL(2);
#pragma nounroll
        while (k < 13) { STAGE_W(); LSYNC(); }
        ASK(fv, rl);
        STAGE_W(); LSYNC(); STAGE_W(); LSYNC(); STAGE_W(); LSYNC();
        NEED(fv, rl, 2);
        const int nw = late ? 32 : 24;
#pragma nounroll
        while (k < nw) { STAGE_W(); LSYNC(); }
        const int np = late ? 16 : 16 - SHIFT;
#pragma nounroll
        for (int p = 0; p < np; ++p) { A4(k, sl); C4(sC, p, sl); ++k; NEXT_SLOT(); LSYNC(); }
        if (late) {
            const __amdgpu_buffer_rsrc_t sR = SRD(KARG(const u16*, 9), DM * NP * 2);
#pragma nounroll
            for (int p = 16 - SHIFT; p < 16; ++p) { A4(72 + p, sl); C4(sR, p, sl); NEXT_SLOT(); LSYNC(); }
        }
        const int tb = mb2 * 128 + (wr >> 1) * 64 + (wr & 1) * 8 + (ln >> 5) * 16, dcol = n0 + (ln & 31) * 4;
#define ROW(i) (tb + ((i) >> 3) * 32 + ((i) & 7))
        gu32* flag = (gu32*)(ws2 + WS_FLAG2 + tile * 128 + wr * 32);
        const __amdgpu_buffer_rsrc_t sP = SRD(KARG(float*, 14), SEQ * DM * 4);
#define POFF(i) ((ROW(i) * DM + dcol) * 4)
        const int wn1 = (ln >> 4) & 1, s0 = rs0 + wn1, s1 = rs0 + 2 + wn1;
        const int inp = wr * 8192 + ((ln >> 2) & 3) * 1024 + (ln & 3) * 256, sw = (ln & 15) ^ ((ln >> 5) * 8);
        LAS char* q0 = lds + (s0 >= 5 ? s0 - 5 : s0) * 32768 + inp;
        LAS char* q1 = lds + (s1 >= 5 ? s1 - 5 : s1) * 32768 + inp;
#define SUMS(i) (*(LAS f32x4*)(q0 + ((i) >> 3) * 4096 + ((sw ^ ((i) & 7)) * 16)) + *(LAS f32x4*)(q1 + ((i) >> 3) * 4096 + ((sw ^ ((i) & 7)) * 16)))
        f32x4 ex[16];
        if (late) {
            WAIT_V(8); BAR();
            ASK(fv, flag);
            WAIT_V(0); BAR();
            NEED(fv, flag, 1);
#pragma unroll
            for (int i = 0; i < 16; ++i) ex[i] = __builtin_bit_cast(f32x4, __builtin_amdgcn_raw_buffer_load_b128(sP, POFF(i), 0, 16));
            BAR();
#pragma unroll
            for (int i = 0; i < 16; ++i) __builtin_amdgcn_raw_buffer_store_b128(__builtin_bit_cast(u32x4, ex[i] + SUMS(i)), sP, POFF(i), 0, 16);
        } else {
            const __amdgpu_buffer_rsrc_t sXk = SRD(KARG(const u16*, 0), SEQ * DM * 2), sDk = SRD(KARG(const u16*, 12), DM * 2), sGk = SRD(ws2 + WS_G, 8192);
            WAIT_V(8); BAR();
            u32x2 dk = __builtin_amdgcn_raw_buffer_load_b64(sDk, dcol * 2, 0, 0), xv[16]; u32x4 gwd[4];
#pragma unroll
            for (int i = 0; i < 4; ++i) gwd[i] = __builtin_amdgcn_raw_buffer_load_b128(sGk, ROW(4 * i) * 4, 0, 16);
#pragma unroll
            for (int i = 0; i < 16; ++i) xv[i] = __builtin_amdgcn_raw_buffer_load_b64(sXk, (tb * DM + dcol) * 2 + (i & 1) * 2048, (((i) >> 3) * 32 + ((i) & 6)) * 2048, 0);
            WAIT_V(21); BAR();
            asm volatile("" : "+v"(dk), "+v"(gwd[0]), "+v"(gwd[1]), "+v"(gwd[2]), "+v"(gwd[3]));
#pragma unroll
            for (int i = 0; i < 16; ++i) {
                const float g = bflo(gwd[i >> 2][i & 3]);
                ex[i] = (f32x4){g * rbf(bflo(xv[i][0]) * bflo(dk[0])), g * rbf(bfhi(xv[i][0]) * bfhi(dk[0])),
                                g * rbf(bflo(xv[i][1]) * bflo(dk[1])), g * rbf(bfhi(xv[i][1]) * bfhi(dk[1]))};
                asm volatile("" : "+v"(ex[i]));
            }
            BAR();
#pragma unroll
            for (int i = 0; i < 16; ++i) __builtin_amdgcn_raw_buffer_store_b128(__builtin_bit_cast(u32x4, ex[i] + SUMS(i)), sP, POFF(i), 0, 16);
            WAIT_V(0);
            if (ln == 0) __hip_atomic_store(flag, 1u, RLX_AGENT);
        }
        asm volatile(".Lk1_end_a:");
        return;
    }

    {
        unsigned long long gq[2][7];
#pragma unroll
        for (int c = 0; c < 2; ++c)
#pragma unroll
            for (int q = 0; q < 7; ++q) gq[c][q] = __hip_atomic_load(carry + (q + pm >= 7 ? q + pm - 7 : 0) * 1024 + CHAN(c, er), RLX_AGENT);
        f32x2 h[2];
        {
            f32x2 a16[2], run[2]; f32x4 E[2][6], F0[2]; f32x2 F1[2];
#pragma unroll
            for (int c = 0; c < 2; ++c) {
                a16[c] = *(LAS f32x2*)(ctab + c * (16 * 96) + 24);
#pragma unroll
                for (int i = 0; i < 6; ++i) E[c][i] = *(LAS f32x4*)(etab + c * (16 * 144) + i * 16);
                F0[c] = *(LAS f32x4*)(etab + c * (16 * 144) + wr * 32); F1[c] = *(LAS f32x2*)(etab + c * (16 * 144) + wr * 32 + 16);
                run[c] = (f32x2){0.f, 0.f};
            }
#pragma unroll
            for (int w = 0; w < 3; ++w)
                if (wr > w) {
#pragma unroll
                    for (int i = 0; i < 4; ++i)
#pragma unroll
                        for (int c = 0; c < 2; ++c) run[c] = cfma(a16[c], run[c], (f32x2){E[c][2 * w + (i >> 1)][(i & 1) * 2], E[c][2 * w + (i >> 1)][(i & 1) * 2 + 1]});
                }
#pragma unroll
            for (int c = 0; c < 2; ++c) {
                const f32x2 r1 = cfma(a16[c], run[c], (f32x2){F0[c][0], F0[c][1]}), r2 = cfma(a16[c], r1, (f32x2){F0[c][2], F0[c][3]}), r3 = cfma(a16[c], r2, F1[c]);
                h[c] = sel4(eq, run[c], r1, r2, r3);
            }
        }
        f32x4 ur[4], ui[4];
#pragma unroll
        for (int m = 0; m < 4; ++m) { ur[m] = park[m * 128]; ui[m] = park[m * 128 + 64]; }
        const f32x4 cs1 = *(LAS f32x4*)(ctab + 16 * 96);

        f32x2 a256[2], aq[2];
#pragma unroll
        for (int c = 0; c < 2; ++c) {
            LAS char* ct = ctab + c * (16 * 96);
            a256[c] = *(LAS f32x2*)(ct + 80); aq[c] = cmul(*(LAS f32x2*)(ct + 16 + eq * 8), *(LAS f32x2*)(ct + 48 + wr * 8));
        }
        unsigned mx = 0;
#pragma unroll
        for (int q = 0; q < 7; ++q) mx = max(mx, max((unsigned)gq[0][q], (unsigned)gq[1][q]));
        if (pm > 0 && __any(mx == 0xFFFFFFFFu)) {
            unsigned spins = 0;
            do {
                mx = 0;
#pragma unroll
                for (int c = 0; c < 2; ++c)
#pragma unroll
                    for (int q = 0; q < 7; ++q) {
                        gq[c][q] = __hip_atomic_load(carry + (q + pm >= 7 ? q + pm - 7 : 0) * 1024 + CHAN(c, er), RLX_AGENT);
                        mx = max(mx, (unsigned)gq[c][q]);
                    }
            } while (__any(mx == 0xFFFFFFFFu) && ++spins < (1u << 18));
        }
        f32x2 cg[2] = {{0.f, 0.f}, {0.f, 0.f}};
#pragma unroll
        for (int q = 0; q < 7; ++q) {
            const unsigned keep = q + pm >= 7 ? 0xFFFFFFFFu : 0u;
#pragma unroll
            for (int c = 0; c < 2; ++c) cg[c] = cfma(a256[c], cg[c], (f32x2){__uint_as_float((unsigned)gq[c][q] & keep), __uint_as_float((unsigned)(gq[c][q] >> 32) & keep)});
        }
        h[0] = cfma(aq[0], cg[0], h[0]); h[1] = cfma(aq[1], cg[1], h[1]);
        const unsigned ls = IMG_S(er, eq);
        const f32x2 b1 = {cs1[0], cs1[1]}, bf = {cs1[2], cs1[3]};
#pragma unroll
        for (int hh = 0; hh < 2; ++hh) {
            float vr[8], vi[8], wr8[8], wi8[8];
#pragma unroll
            for (int e = 0; e < 8; ++e) {
                const int m = 2 * hh + (e >> 2), q = e & 3;
                h[0] = cfma(a1, h[0], (f32x2){acc[m][7][q], acc[m][8][q]});
                h[1] = cfma(b1, h[1], (f32x2){ur[m][q], ui[m][q]});
                const float g2 = bflo(gt[m][q]);
                const f32x2 s0 = cmul(cf, h[0]), s1 = cmul(bf, h[1]);
                vr[e] = s0.x * g2; vi[e] = -s0.y * g2; wr8[e] = s1.x * g2; wi8[e] = -s1.y * g2;
            }
            ST16(ls + 40 * A2S + hh * 512, PK8(vr)); ST16(ls + 40 * A2S + hh * 512 + 128, PK8(wr8));
            ST16(ls + hh * 512, PK8(vi)); ST16(ls + hh * 512 + 128, PK8(wi8));
        }
    }
    int ty = threadIdx.x; asm volatile("" : "+v"(ty) :: "memory");
    const int ln = ty & 63, fr2 = ty & 15, fq2 = (ty >> 4) & 3;
    const int wn = wid & 1, kk = wid >> 1;
    LAS char* rH = lds + kk * 8192 + (ln >> 5) * 4096 + (ln & 31) * 8;
    LAS char* rC = lds + 16384 + (wn * 64 + fr2) * 128 + (((4 * kk + fq2) ^ (fr2 & 6)) * 16);
    LAS char* rWb = lds + 16384 + kk * 8192 + (ln >> 5) * 4096 + wn * 1024 + ((ln >> 2) & 7) * 128 + (ln & 3) * 8;
    const int rsw = (ln >> 3) & 3;
    f32x4 ac2[4][8];
    s16x4 HX[8][2], HY[8][2], WX[4][2], WY[4][2];
    int sl = 0;

#define RD1(RD, i, H, W) do { \
        if ((i) < 8) { \
            if (RD == 1) W[(i) >> 1][(i) & 1] = tr4(rWb + sl + ((((i) >> 1) ^ rsw) * 32) + ((i) & 1) * 2048); \
            else if (((i) & 1) == 0) { const s16x8 v_ = *(LAS s16x8*)(rC + sl + ((i) >> 1) * 2048); \
                W[(i) >> 1][0] = __builtin_shufflevector(v_, v_, 0, 1, 2, 3); W[(i) >> 1][1] = __builtin_shufflevector(v_, v_, 4, 5, 6, 7); } \
        } else H[((i) - 8) >> 1][(i) & 1] = tr4(rH + sl + (((i) - 8) >> 1) * 256 + ((i) & 1) * 2048); \
    } while (0)
#define MM(i, HC, WC) mfma_acc(ac2[(i) & 3][(i) >> 2], cat8(WC[(i) & 3][0], WC[(i) & 3][1]), cat8(HC[(i) >> 2][0], HC[(i) >> 2][1]))
#define STEP(RD, HC, WC, HN, WN) do { \
        _Pragma("unroll") for (int i = 0; i < 32; ++i) { MM(i, HC, WC); if (i < 24) RD1(RD, i, HN, WN); } \
        sl = sl == 4 * 32768 ? 0 : sl + 32768; LGKM0(); \
    } while (0)

    LGKM0();
    BAR();
#pragma unroll
    for (int i = 0; i < 24; ++i) RD1(1, i, HX, WX);
    sl = 32768; LGKM0();
    BAR();
#pragma unroll
    for (int i = 0; i < 32; ++i) {
        asm volatile("v_mfma_f32_16x16x32_bf16 %0, %1, %2, 0" : "=&v"(ac2[i & 3][i >> 2]) : "v"(cat8(WX[i & 3][0], WX[i & 3][1])), "v"(cat8(HX[i >> 2][0], HX[i >> 2][1])) : "memory");
        if (i < 24) RD1(1, i, HY, WY);
    }
    sl = 2 * 32768; LGKM0();
    BAR(); STEP(1, HY, WY, HX, WX);
    const int nf = late ? 15 : 11;
#pragma nounroll
    for (int i = 1; i < nf; ++i) {
        if (i == 3) WAIT_V(0);
        BAR(); STEP(1, HX, WX, HY, WY); BAR(); STEP(1, HY, WY, HX, WX);
    }
    BAR(); STEP(1, HX, WX, HY, WY); BAR(); STEP(0, HY, WY, HX, WX);
#pragma nounroll
    for (int i = late ? -SHIFT / 2 : SHIFT / 2; i < 7; ++i) { BAR(); STEP(0, HX, WX, HY, WY); BAR(); STEP(0, HY, WY, HX, WX); }
    BAR(); STEP(0, HX, WX, HY, WY);
    {
        const int sw = rs0 + wid;
        LAS char* dmp = lds + (sw >= 5 ? sw - 5 : sw) * 32768 + fq2 * 256;
#define DUMP(i) *(LAS f32x4*)(dmp + (i) * 1024 + ((fr2 ^ (4 * ((i) & 3) + fq2)) * 16)) = ac2[(i) & 3][(i) >> 2]
#pragma unroll
        for (int i = 0; i < 32; ++i) { MM(i, HY, WY); if (i >= 12) DUMP(i - 12); }
        asm volatile("s_nop 15" ::: "memory");
#pragma unroll
        for (int i = 20; i < 32; ++i) DUMP(i);
    }
    WAIT_L0();
    BAR();
    asm volatile(".Lk1_end_b:");
}

extern "C" void kernel_launch(void* const* d_in, const int* in_sizes, int n_in, void* d_out, int out_size, void* d_ws, size_t ws_size,
                              hipStream_t stream) {
    const u16* x = (const u16*)d_in[0];
    const u16* lre = (const u16*)d_in[1];
    const u16* lim = (const u16*)d_in[2];
    const u16* Bre = (const u16*)d_in[3];
    const u16* Bim = (const u16*)d_in[4];
    const u16* Cre = (const u16*)d_in[5];
    const u16* Cim = (const u16*)d_in[6];
    const u16* logdt = (const u16*)d_in[7];
    const u16* dskip = (const u16*)d_in[8];
    const u16* Wg = (const u16*)d_in[10];
    const u16* Wu = (const u16*)d_in[11];
    const u16* Wd = (const u16*)d_in[12];
    const u16* gw = (const u16*)d_in[13];
    char* ws = (char*)d_ws;
    (void)hipFuncSetAttribute((const void*)k1_fused, hipFuncAttributeMaxDynamicSharedMemorySize, K2_LDS);
    k1_fused<<<256, 512, K2_LDS, stream>>>(x, Wg, Wu, Bre, Bim, lre, lim, logdt, gw, Cre, Cim, Wd, dskip, ws, (float*)d_out);
}
```

```cpp
#include <hip/hip_runtime.h>
#include <hip/hip_bf16.h>

typedef unsigned short u16;
typedef __bf16 bf16x8 __attribute__((ext_vector_type(8)));
typedef __bf16 bf16x2 __attribute__((ext_vector_type(2)));
typedef short s16x4 __attribute__((ext_vector_type(4)));
typedef short s16x8 __attribute__((ext_vector_type(8)));
typedef float f32x4 __attribute__((ext_vector_type(4)));
typedef float f32x2 __attribute__((ext_vector_type(2)));
typedef unsigned u32x4 __attribute__((ext_vector_type(4)));
typedef unsigned u32x2 __attribute__((ext_vector_type(2)));
#define LAS __attribute__((address_space(3)))
#define GAS __attribute__((address_space(1)))
typedef GAS unsigned gu32;
typedef GAS unsigned long long gu64;
typedef __attribute__((address_space(4))) const unsigned long long* kptr;
#define RLX_AGENT __ATOMIC_RELAXED, __HIP_MEMORY_SCOPE_AGENT

constexpr int SEQ = 2048, DM = 1024, NP = 1024, FF = 3584;
constexpr int KSTEPS2 = (2 * NP + FF) / 64;

constexpr unsigned WS_CARRY = 0;
constexpr unsigned WS_G = 7 * 8192;
constexpr unsigned WS_RDY = 65536;
constexpr unsigned WS_FLAG2 = WS_RDY + 1024;
constexpr unsigned WS_HQ = WS_FLAG2 + 16384;
constexpr unsigned WS_HA = WS_HQ + 1024;
constexpr unsigned WS_HR = WS_HA + 1024;
constexpr unsigned WS_SYNC_BYTES = WS_HR + 2048;
constexpr size_t WS_A2 = 1 << 20;
constexpr unsigned A2S = 262144 + 128;
constexpr size_t WS_A2_BYTES = (size_t)KSTEPS2 * A2S;

__device__ __forceinline__ float bflo(unsigned v) { return __uint_as_float(v << 16); }
__device__ __forceinline__ float bfhi(unsigned v) { return __uint_as_float(v & 0xffff0000u); }
__device__ __forceinline__ float rbf(float f) { return (float)(__bf16)f; }
__device__ __forceinline__ unsigned pk2(float a, float b) {
    bf16x2 v; v.x = (__bf16)a; v.y = (__bf16)b; return __builtin_bit_cast(unsigned, v);
}
__device__ __forceinline__ float relu(float v) { return __int_as_float(max(__float_as_int(v), 0)); }
__device__ __forceinline__ float dot2(unsigned a, unsigned b, float c) {
    return __builtin_amdgcn_fdot2_f32_bf16(__builtin_bit_cast(bf16x2, a), __builtin_bit_cast(bf16x2, b), c, false);
}

constexpr size_t WS_DBG = 200u << 20;
#define STAMP(kid, i)
#define STAMP_TAKE(v)
#define STAMP_PUT(kid, i, v)

#define FSTAMP(i)
#define FDUMP()
#define WAIT_V(n) asm volatile("s_waitcnt vmcnt(" #n ")" ::: "memory")
#define WAIT_L0() asm volatile("s_waitcnt lgkmcnt(0)" ::: "memory")
#define LGKM0() __builtin_amdgcn_s_waitcnt(0xC07F)
#define BAR() __builtin_amdgcn_s_barrier()
#define SCHED() __builtin_amdgcn_sched_barrier(0)
#define SRD(p, bytes) __builtin_amdgcn_make_buffer_rsrc((void*)(p), (short)0, (int)(bytes), 0x00020000)

__device__ __forceinline__ void dma16(unsigned m0v, unsigned voff, __amdgpu_buffer_rsrc_t srd, unsigned soff) {
    asm volatile("s_mov_b32 m0, %0\n\ts_nop 0\n\tbuffer_load_dwordx4 %1, %2, %3 offen lds" ::"s"(m0v), "v"(voff), "s"(srd), "s"(soff) : "m0", "memory");
}
__device__ __forceinline__ bf16x8 lds_b128(LAS char* p) { return __builtin_bit_cast(bf16x8, *(LAS s16x8*)p); }
__device__ __forceinline__ bf16x8 lds_tr(LAS char* p0, LAS char* p1) {
    s16x4 a = __builtin_amdgcn_ds_read_tr16_b64_v4i16((LAS s16x4*)p0);
    s16x4 b = __builtin_amdgcn_ds_read_tr16_b64_v4i16((LAS s16x4*)p1);
    return __builtin_bit_cast(bf16x8, __builtin_shufflevector(a, b, 0, 1, 2, 3, 4, 5, 6, 7));
}
#define MFMA(a, b, c) __builtin_amdgcn_mfma_f32_16x16x32_bf16(a, b, c, 0, 0, 0)
__device__ __forceinline__ f32x2 cmul(f32x2 a, f32x2 b) { return (f32x2){__builtin_fmaf(a.x, b.x, -(a.y * b.y)), __builtin_fmaf(a.x, b.y, a.y * b.x)}; }
__device__ __forceinline__ f32x2 cfma(f32x2 a, f32x2 b, f32x2 c) {
    return (f32x2){__builtin_fmaf(a.x, b.x, __builtin_fmaf(-a.y, b.y, c.x)), __builtin_fmaf(a.x, b.y, __builtin_fmaf(a.y, b.x, c.y))};
}
__device__ __forceinline__ f32x2 sel4(int i, f32x2 a, f32x2 b, f32x2 c, f32x2 d) {
    const bool o = i & 1, u = i & 2;
    const float lx = o ? b.x : a.x, ly = o ? b.y : a.y, hx = o ? d.x : c.x, hy = o ? d.y : c.y;
    return (f32x2){u ? hx : lx, u ? hy : ly};
}

#define STPOL 16
#define PF_W0 1
#define PF_D0 4
#define OUTPOL 16

#define K2_SPLIT 40
#define MAP 0
#define ABL_NOLOAD 0
#define ABL_NOREAD 0
#define ABL_NOMFMA 0
#define CODE_T 10
#define ILV 1
#define SHIFT 2
#define GATE2 1
#define PRO 0
#define MORD 3
#define DEEP 0
#define THIN 1
#define M04 1
#define ASKPOS 0
#define PSLEEP 4
#define WSWEEP 0
#define BPF 1
#define ROLL 0
#define XLIN 0
#define WFIRST 0
#define EW 0
#define CODE2 63
#define LATE_FLIP 0
#define DIST 5
constexpr int K2_LDS = 5 * 32768;

__device__ __forceinline__ void dma16c(unsigned m0v, unsigned voff, __amdgpu_buffer_rsrc_t srd, unsigned soff) {
    asm volatile("s_mov_b32 m0, %0\n\ts_nop 0\n\tbuffer_load_dwordx4 %1, %2, %3 offen sc1 lds" ::"s"(m0v), "v"(voff), "s"(srd), "s"(soff) : "m0", "memory");
}
#define BPOL ""
#define APOL " sc1"
__device__ __forceinline__ void dma4c(unsigned m0v, unsigned voff, __amdgpu_buffer_rsrc_t srd, unsigned soff) {
    asm volatile("s_mov_b32 m0, %0\n\ts_nop 0\n\tbuffer_load_dwordx4 %1, %2, %3 offen" APOL " lds\n\tbuffer_load_dwordx4 %1, %2, %3 offen offset:1024" APOL " lds\n\t"
                 "buffer_load_dwordx4 %1, %2, %3 offen offset:2048" APOL " lds\n\tbuffer_load_dwordx4 %1, %2, %3 offen offset:3072" APOL " lds" ::"s"(m0v), "v"(voff), "s"(srd), "s"(soff) : "m0", "memory");
}
__device__ __forceinline__ void dma4(unsigned m0v, unsigned voff, __amdgpu_buffer_rsrc_t srd, unsigned s0, unsigned s1, unsigned s2, unsigned s3) {
    asm volatile("s_mov_b32 m0, %0\n\ts_nop 0\n\tbuffer_load_dwordx4 %1, %2, %3 offen lds\n\tbuffer_load_dwordx4 %1, %2, %4 offen offset:1024 lds\n\t"
                 "buffer_load_dwordx4 %1, %2, %5 offen offset:2048 lds\n\tbuffer_load_dwordx4 %1, %2, %6 offen offset:3072 lds" ::"s"(m0v), "v"(voff), "s"(srd), "s"(s0), "s"(s1), "s"(s2), "s"(s3) : "m0", "memory");
}
__device__ __forceinline__ void mfma_acc(f32x4& c, bf16x8 a, bf16x8 b) {
    asm volatile("v_mfma_f32_16x16x32_bf16 %0, %1, %2, %0" : "+v"(c) : "v"(a), "v"(b) : "memory");
}
__device__ __forceinline__ void mfma_acc2(f32x4& c, bf16x8 a0, bf16x8 b0, bf16x8 a1, bf16x8 b1) {
    asm volatile("v_mfma_f32_16x16x32_bf16 %0, %1, %2, %0\n\tv_mfma_f32_16x16x32_bf16 %0, %3, %4, %0" : "+v"(c) : "v"(a0), "v"(b0), "v"(a1), "v"(b1) : "memory");
}
__device__ __forceinline__ s16x4 tr4(LAS char* p) { return __builtin_amdgcn_ds_read_tr16_b64_v4i16((LAS s16x4*)p); }
__device__ __forceinline__ bf16x8 cat8(s16x4 a, s16x4 b) { return __builtin_bit_cast(bf16x8, __builtin_shufflevector(a, b, 0, 1, 2, 3, 4, 5, 6, 7)); }

constexpr int K1_ASB = 40960;
constexpr int K1_W = 81920;
constexpr int K1_WBUF = 28704, K1_UOFF = 14352;
constexpr int K1_ETAB = K1_W + 2 * K1_WBUF;
constexpr int K1_CTAB = K1_ETAB + 32 * 144;
constexpr int K1_SINK = K1_CTAB + 32 * 96;
constexpr int K1_GTAB = K1_SINK + 1024;
constexpr int K1_HS = K1_GTAB + 1024;

__global__ __launch_bounds__(512, 2) void k1_fused(const u16* __restrict__ x, const u16* __restrict__ Wg, const u16* __restrict__ Wu,
                                                       const u16* __restrict__ Bre, const u16* __restrict__ Bim, const u16* __restrict__ lre,
                                                       const u16* __restrict__ lim, const u16* __restrict__ logdt, const u16* __restrict__ gw,
                                                       const u16* __restrict__ Cre, const u16* __restrict__ Cim, const u16* __restrict__ Wd,
                                                       const u16* __restrict__ dskip, char* __restrict__ ws, float* __restrict__ out) {
    extern __shared__ __attribute__((aligned(16))) char smem[];
    LAS char* lds = (LAS char*)smem;
    const unsigned ldsb = (unsigned)(size_t)lds;
    const int tid = threadIdx.x, lane = tid & 63;
    const int wid = __builtin_amdgcn_readfirstlane(tid >> 6);
    const int wr = wid & 3, wc = wid >> 2;
    const int bid = blockIdx.x, idx = bid >> 3, j = (bid & 7) * 4 + (idx & 3), pm = idx >> 2;
    const int fr = lane & 15, fq = lane >> 4;
#define ROLE(b_, i_) const int xc = (b_) & 7, late = (xc & 1) ^ LATE_FLIP, mb2 = (xc >> 2) * 8 + ((i_) >> 2), nblk = ((xc >> 1) & 1) * 4 + ((i_) & 3), tile = mb2 * 8 + nblk
    STAMP_TAKE(st0);
#define LT(n)
#define LITE_DECL()
#define LT1()
    FSTAMP(0);

    const char* code0; unsigned codeA, codeB;
    asm volatile("s_getpc_b64 %0\n.Lk1_ref:\n\ts_mov_b32 %1, .Lk1_end_a-.Lk1_ref\n\ts_mov_b32 %2, .Lk1_end_b-.Lk1_ref" : "=s"(code0), "=s"(codeA), "=s"(codeB));
    const __amdgpu_buffer_rsrc_t sI = SRD(code0, (codeA > codeB ? codeA : codeB) + 192);
    const unsigned mSink = ldsb + K1_SINK;
    const bool pf = wid == 7;
    if (pf) { dma16(mSink, (unsigned)(lane * 128), sI, 0u); dma16(mSink, (unsigned)(lane * 128) | (unsigned)((CODE2 - lane) & 0x80000000), sI, 8192u); }

    const int ch = 32 * j + 8 * (fq & 1) + 4 * (fr >> 3) + 16 * ((fr >> 2) & 1) + (fr & 3);
    const int grow = pm * 256 + j * 8 + wid;
    u32x4 xa, xb, ga, gb; unsigned plr, pli, pld;
    {
        const u16* xr = x + (size_t)grow * DM;
        const unsigned l16 = lane * 16, c2 = ch * 2;
        asm volatile("s_nop 4\n\tglobal_load_dwordx4 %0, %7, %9\n\tglobal_load_dwordx4 %1, %7, %9 offset:1024\n\t"
                     "global_load_dwordx4 %2, %7, %10\n\tglobal_load_dwordx4 %3, %7, %10 offset:1024\n\t"
                     "global_load_ushort %4, %8, %11\n\tglobal_load_ushort %5, %8, %12\n\tglobal_load_ushort %6, %8, %13"
                     : "=&v"(xa), "=&v"(xb), "=&v"(ga), "=&v"(gb), "=&v"(plr), "=&v"(pli), "=&v"(pld)
                     : "v"(l16), "v"(c2), "s"(xr), "s"(gw), "s"(lre), "s"(lim), "s"(logdt) : "memory");
    }

    const int q4 = wid + 28, q5 = wid + 35;
    const __amdgpu_buffer_rsrc_t sX = SRD(x, SEQ * DM * 2), sG = SRD(Wg, DM * FF * 2), sU = SRD(Wu, DM * FF * 2);
    const __amdgpu_buffer_rsrc_t s4 = SRD(q4 < 32 ? x : (q4 & 2) ? Bim : Bre, q4 < 32 ? SEQ * DM * 2 : NP * DM * 2);
    const __amdgpu_buffer_rsrc_t s5 = SRD((q5 & 2) ? Bim : Bre, q5 < 40 ? NP * DM * 2 : 0);
    const int l3 = lane >> 3;
    const unsigned voffX = (unsigned)(((l3 >> 2) * 16 + (l3 & 3)) * 2048 + (((lane & 7) ^ (l3 & 6)) * 16));
    unsigned voffW;
    { const int rho = (tid * 4682) >> 16, c = tid - rho * 14, k = (rho & 0x13) | ((rho & 4) << 1) | ((rho & 8) >> 1);
      voffW = (unsigned)(k * (FF * 2) + c * 16); }
#define XROW(q) (pm * 256 + ((q) >> 3) * 64 + (((q) >> 2) & 1) * 8 + ((q) & 1) * 32 + (((q) >> 1) & 1) * 4)
#define BROW(q) (32 * j + 8 * (((q) >> 2) & 1) + 4 * ((q) & 1))
    constexpr unsigned WST = 64 * FF * 2, WSH = 32 * FF * 2;
    const unsigned so0 = (unsigned)(XROW(wid) * 2048), so1 = (unsigned)(XROW(wid + 7) * 2048), so2 = (unsigned)(XROW(wid + 14) * 2048), so3 = (unsigned)(XROW(wid + 21) * 2048),
                   so4 = (unsigned)((q4 < 32 ? XROW(q4) : BROW(q4)) * 2048), so5 = (unsigned)(BROW(q5) * 2048);
    const unsigned soW = (unsigned)(j * 224), mb0 = ldsb + wid * 1024;
    const unsigned m50 = q5 < 40 ? mb0 + 35 * 1024 : mSink, m51 = q5 < 40 ? mb0 + 35 * 1024 + K1_ASB : mSink;
#define ST_X(i, ko, b) dma16(mb0 + (b) * K1_ASB + (i) * 7 * 1024, voffX, sX, ((i) == 0 ? so0 : (i) == 1 ? so1 : (i) == 2 ? so2 : so3) + (ko))
#define ST_S4(ko, b) dma16(mb0 + (b) * K1_ASB + 28 * 1024, voffX, s4, so4 + (ko))
#define ST_S5(ko, b) dma16((b) ? m51 : m50, voffX, s5, so5 + (ko))
#define ST_G(h, t, b) dma16(mb0 + K1_W + (b) * K1_WBUF + (h) * 7168, voffW, sG, soW + (t) * WST + (h) * WSH)
#define ST_U(h, t, b) dma16(mb0 + K1_W + (b) * K1_WBUF + K1_UOFF + (h) * 7168, voffW, sU, soW + (t) * WST + (h) * WSH)
    if (!pf) { ST_X(0, 0, 0); ST_X(1, 0, 0); ST_X(2, 0, 0); ST_X(3, 0, 0); ST_S4(0, 0); ST_S5(0, 0); ST_G(0, 0, 0); ST_G(1, 0, 0); ST_U(0, 0, 0); ST_U(1, 0, 0); }
    FSTAMP(1);

    const int ch0 = (fq ^ (fr & 6)) * 16;
    LAS char* rA0 = lds + wr * 8192 + fr * 128 + ch0;
    LAS char* rA1 = lds + wr * 8192 + fr * 128 + (ch0 ^ 64);
    LAS char* rS0 = lds + 32768 + wc * 4096 + fr * 128 + ch0;
    LAS char* rS1 = lds + 32768 + wc * 4096 + fr * 128 + (ch0 ^ 64);
    const int trow = (16 * (fq >> 1) + 4 * (fq & 1) + ((lane & 15) >> 2)) * 224;
    LAS char* rW = lds + K1_W + trow + wc * 96 + (lane & 3) * 8;
    LAS char* rM = lds + K1_W + trow + 192 + wc * 16 + (lane & 1) * 8 + ((lane >> 1) & 1) * K1_UOFF;

    f32x4 acc[4][9];
#pragma unroll
    for (int m = 0; m < 4; ++m)
#pragma unroll
        for (int n = 0; n < 9; ++n) { acc[m][n] = (f32x4){0.f, 0.f, 0.f, 0.f}; asm volatile("" : "+v"(acc[m][n])); }
    bf16x8 Af[4][2], Bf[3][2];

#define RD_A(m, b) do { Af[m][0] = lds_b128(rA0 + (b) * K1_ASB + (m) * 2048); Af[m][1] = lds_b128(rA1 + (b) * K1_ASB + (m) * 2048); } while (0)
#define RD_S(slot, ri, b) do { Bf[slot][0] = lds_b128(rS0 + (b) * K1_ASB + (ri) * 2048); Bf[slot][1] = lds_b128(rS1 + (b) * K1_ASB + (ri) * 2048); } while (0)
#define RD_W(slot, base, off) do { Bf[slot][0] = lds_tr((base) + (off), (base) + (off) + 1792); Bf[slot][1] = lds_tr((base) + (off) + 7168, (base) + (off) + 7168 + 1792); } while (0)
#define SEC1(b, d0, d1) do { RD_S(0, 0, b); RD_S(1, 1, b); d0; RD_W(2, rW, (b) * K1_WBUF); RD_A(0, b); d1; RD_A(1, b); RD_A(2, b); RD_A(3, b); } while (0)
#define SEC2(b, d0, d1, d2, d3) do { d0; RD_W(0, rW, (b) * K1_WBUF + 32); d1; RD_W(1, rW, (b) * K1_WBUF + 64); d2; RD_W(2, rM, (b) * K1_WBUF); d3; } while (0)
#define SEC3(b, d0, d1, d2, d3) do { d0; RD_W(0, rW, (b) * K1_WBUF + K1_UOFF); d1; RD_W(1, rW, (b) * K1_WBUF + K1_UOFF + 32); d2; RD_W(2, rW, (b) * K1_WBUF + K1_UOFF + 64); d3; } while (0)
#define GO(n0, n1, n2) do { asm volatile("" ::: "memory"); LGKM0(); BAR(); SCHED(); \
        _Pragma("unroll") for (int m = 0; m < 4; ++m) { \
            mfma_acc2(acc[m][n0], Af[m][0], Bf[0][0], Af[m][1], Bf[0][1]); mfma_acc2(acc[m][n1], Af[m][0], Bf[1][0], Af[m][1], Bf[1][1]); \
            mfma_acc2(acc[m][n2], Af[m][0], Bf[2][0], Af[m][1], Bf[2][1]); } \
        SCHED(); } while (0)

#define RELANE() int tz = threadIdx.x; asm volatile("" : "+v"(tz)); const int er = tz & 15, eq = (tz >> 4) & 3
    asm volatile("" : "+v"(rA0), "+v"(rA1), "+v"(rS0), "+v"(rS1), "+v"(rW), "+v"(rM));
    WAIT_V(10);
    if (pf) WAIT_V(0);
    asm volatile("" : "+v"(xa), "+v"(xb), "+v"(ga), "+v"(gb), "+v"(plr), "+v"(pli), "+v"(pld) :: "memory");
    FSTAMP(2);
    {
        float s = 0.f, s2 = 0.f;
#pragma unroll
        for (int e = 0; e < 4; ++e) { s = dot2(xa[e], ga[e], s); s2 = dot2(xb[e], gb[e], s2); }
        s += s2;
#define DPP_ADD(ctrl, rmask) s += __int_as_float(__builtin_amdgcn_update_dpp(0, __float_as_int(s), ctrl, rmask, 0xf, true))
        DPP_ADD(0x111, 0xf); DPP_ADD(0x112, 0xf); DPP_ADD(0x114, 0xf); DPP_ADD(0x118, 0xf); DPP_ADD(0x142, 0xa); DPP_ADD(0x143, 0xc);
        const float g = rbf(__builtin_amdgcn_rcpf(1.f + __builtin_amdgcn_exp2f(-1.4426950408889634f * rbf(s))));
        if (lane == 63) __hip_atomic_store((gu32*)(ws + WS_G) + grow, pk2(g, 1.f - g), RLX_AGENT);
    }
    FSTAMP(3);
    LITE_DECL();
    if (!pf) {
        BAR();
        ST_X(0, 128, 1); ST_X(1, 128, 1); ST_X(2, 128, 1); ST_X(3, 128, 1); ST_S4(128, 1); ST_S5(128, 1); ST_G(0, 1, 1); ST_G(1, 1, 1);
        WAIT_V(11);
        if (wc == 1) BAR();
        LT1();
        FSTAMP(6);
#pragma nounroll
        for (int t = 0; t < 16; t += 2) {
            asm volatile("" : "+s"(t));
            const unsigned ko = t == 14 ? 0x80000000u : (unsigned)(t + 2) * 128u;
            BAR(); FSTAMP(8 + t); SEC1(0, ST_U(0, t + 1, 1), ST_U(1, t + 1, 1)); WAIT_V(10);
            GO(7, 8, 0);
            BAR(); SEC2(0, ST_X(0, ko, 0), ST_X(1, ko, 0), ST_X(2, ko, 0), ST_X(3, ko, 0));
            GO(1, 2, 6);
            BAR(); SEC3(0, ST_G(0, t + 2, 0), ST_G(1, t + 2, 0), ST_S4(ko, 0), ST_S5(ko, 0)); WAIT_V(10);
            GO(3, 4, 5);
            BAR(); FSTAMP(9 + t); SEC1(1, ST_U(0, t + 2, 0), ST_U(1, t + 2, 0)); WAIT_V(10);
            GO(7, 8, 0);
            BAR(); SEC2(1, ST_X(0, ko + 128, 1), ST_X(1, ko + 128, 1), ST_X(2, ko + 128, 1), ST_X(3, ko + 128, 1));
            GO(1, 2, 6);
            BAR(); SEC3(1, ST_G(0, t + 3, 1), ST_G(1, t + 3, 1), ST_S4(ko + 128, 1), ST_S5(ko + 128, 1)); WAIT_V(10);
            GO(3, 4, 5);
        }
    }
    int w7 = wid; asm volatile("" : "+s"(w7));
    if (w7 == 7) {
        {
            const float lr = bflo(plr), li = bflo(pli), dt = rbf(expf(bflo(pld)));
            const float e = expf(dt * lr); float sn, cs; sincosf(dt * li, &sn, &cs);
            const f32x2 a1 = {e * cs, e * sn};
            const float nr = a1.x - 1.f, den = lr * lr + li * li;
            f32x2 a16 = cmul(a1, a1); a16 = cmul(a16, a16); a16 = cmul(a16, a16); a16 = cmul(a16, a16);
            const f32x2 a32 = cmul(a16, a16), a48 = cmul(a32, a16), a64 = cmul(a32, a32), a128 = cmul(a64, a64), a192 = cmul(a128, a64), a256 = cmul(a128, a128);
            if (lane < 32) {
                LAS f32x4* ct = (LAS f32x4*)(lds + K1_CTAB + lane * 96);
                ct[0] = (f32x4){a1.x, a1.y, 2.f * ((nr * lr + a1.y * li) / den), 2.f * ((a1.y * lr - nr * li) / den)};
                ct[1] = (f32x4){1.f, 0.f, a16.x, a16.y}; ct[2] = (f32x4){a32.x, a32.y, a48.x, a48.y};
                ct[3] = (f32x4){1.f, 0.f, a64.x, a64.y}; ct[4] = (f32x4){a128.x, a128.y, a192.x, a192.y};
                ct[5] = (f32x4){a256.x, a256.y, 0.f, 0.f};
            }
        }
        const int pb = (bid >> 3) | ((bid & 7) << 5);
        const __amdgpu_buffer_rsrc_t sPW = SRD((pb & 1) ? Wg : Wu, DM * FF * 2), sPD = SRD((pb & 1) ? Wd : (pb & 2) ? Cim : Cre, (pb & 1) ? DM * FF * 2 : DM * NP * 2);
        const __amdgpu_buffer_rsrc_t sH = SRD(ws, WS_SYNC_BYTES);
        const unsigned l128 = lane * 128, OUT = 0x80000000u;
        const unsigned pds = (pb & 1) ? (1u << 20) : (1u << 19), pw0 = (unsigned)((pb >> 1) * 8192), pd0 = (unsigned)(((pb & 1) ? pb >> 1 : pb >> 2) * 8192);
#define PF_W(r) dma16(mSink, l128, sPW, pw0 + (unsigned)(r) * (1u << 20))
#define PF_WA(t) PF_W((t) + 2)
#define PF_WB(t) PF_W((t) + 3)
#define PF_D(r) dma16(mSink, l128, sPD, pd0 + (unsigned)(r) * pds)
#define RFL(v) ((unsigned)__builtin_amdgcn_readfirstlane(v))
#define HDMA(m, vo, srd, so) asm volatile("s_nop 4\n\ts_mov_b32 m0, %0\n\ts_nop 0\n\tbuffer_load_dwordx4 %1, %2, %3 offen sc1 lds" ::"s"((unsigned)(m)), "v"(vo), "s"(srd), "s"((unsigned)(so)) : "m0", "memory")
#define HLD(op, v, vo) asm volatile("s_nop 4\n\t" op " %0, %1, %2, 0 offen sc1\n\ts_waitcnt vmcnt(0)" : "=v"(v) : "v"(vo), "s"(sH) : "memory")
#define HST32(off, v) do { __builtin_amdgcn_raw_buffer_store_b32((unsigned)(v), sH, off, 0, 16); asm volatile("" ::: "memory"); } while (0)
#define HST64(off, lo_, hi2_) do { __builtin_amdgcn_raw_buffer_store_b64((u32x2){(unsigned)(lo_), (unsigned)(hi2_)}, sH, off, 0, 16); asm volatile("" ::: "memory"); } while (0)
#define REPLY(q_) do { _Pragma("unroll") for (int i = 0; i < 4; ++i) HST64(WS_HR + l16 * 2 + i * 8, (q_)[i], tag); } while (0)
#define LANE0(off) (l16 == 0 ? (unsigned)(off) : OUT)
#define ALLANS(a_) __all((a_)[0] == tag && (a_)[1] == tag && (a_)[2] == tag && (a_)[3] == tag)
#define ATX(cond, ...) do { int t_ = t; asm volatile("" : "+s"(t_)); if (cond) { unsigned l16 = l128; asm volatile("" : "+v"(l16)); l16 >>= 3; __VA_ARGS__ } } while (0)
#define AT(v, ...) ATX(t_ == (v), __VA_ARGS__)
#define AT2(v, ...) ATX((unsigned)(t_ - (v)) <= 2u, __VA_ARGS__)
        unsigned ask = 0, tag = 0, done = 0;
        const int boss = bid == 0 ? 0 : 64;
        BAR();
        HDMA(ldsb + K1_HS + 1024, l128 >> 3, sH, WS_HR + (bid >> 7) * 1024);
        HDMA(ldsb + K1_HS, l128 >> 3, sH, bid == 0 ? WS_HA : OUT);
        PF_W(1);
        BAR();
        LT1();
        FSTAMP(6);
#pragma nounroll
        for (int t = 0; t < 16; t += 2) {
            asm volatile("" : "+s"(t));
            BAR(); FSTAMP(8 + t); SEC1(0, , );
            AT(10,
                HDMA(mSink, l128, sI, 10240); HDMA(mSink, l128, sI, 10240 + 8192); HDMA(mSink, l128, sI, 10240 + 16384););
            AT(12, HDMA(ldsb + K1_HS, l16, sH, WS_HA););
            GO(7, 8, 0);
            BAR(); SEC2(0, , , , );
            AT(0,
                ROLE(bid, idx);
                HST64((pm < 7 ? WS_CARRY + pm * 8192 + j * 256 : OUT) + (l16 < 32 * 16 ? l16 >> 1 : OUT >> 1), ~0u, ~0u);
                HST32(l16 == 4 * 16 ? WS_RDY + (pm * 32 + j) * 4 : (late ? OUT : WS_FLAG2 + tile * 128) + (l16 < 4 * 16 ? l16 * 2 : OUT >> 1), ~0u););
            AT(2,
                const __amdgpu_buffer_rsrc_t sA = SRD(ws + WS_A2, WS_A2_BYTES), sO = SRD(out, SEQ * DM * 4);
                const unsigned vo = (l16 >> 4) * A2S, vo2 = l16 << 13;
                HDMA(mSink, vo, sA, A2S - 128); HDMA(mSink, vo, sA, 65 * A2S - 128); HDMA(mSink, vo2, sO, 0u););
            AT2(4 + boss,
                WAIT_V(2);
                const u32x4 q = *(LAS u32x4*)(lds + K1_HS + l16), qp = *(LAS u32x4*)(lds + K1_GTAB + l16);
                if (t_ == 4 || __any(q[0] != qp[0] || q[1] != qp[1] || q[2] != qp[2] || q[3] != qp[3])) REPLY(q);
                *(LAS u32x4*)(lds + K1_GTAB + l16) = q;);
            AT2(6 + (int)done * 64, HDMA(ldsb + K1_HS + 1024, l16, sH, WS_HR + (bid >> 7) * 1024););
            GO(1, 2, 6);
            BAR(); SEC3(0, , PF_WA(t), PF_D(t - PF_D0), );
            GO(3, 4, 5);
            BAR(); FSTAMP(9 + t); SEC1(1, , );
            AT2(2 + boss, HDMA(ldsb + K1_HS, l16, sH, WS_HQ););
            AT(14, WAIT_V(0););
            GO(7, 8, 0);
            BAR(); SEC2(1, , , , );
            AT(0,
                if (XLIN == 2) WAIT_V(7); else if (BPF == 2) WAIT_V(6); else WAIT_V(5);
                ask = RFL(*(LAS unsigned*)(lds + K1_HS + 1024 + (bid & 127) * 8)) + 1u;
                const u32x4 old = *(LAS u32x4*)(lds + K1_HS + l16);
                HST32(LANE0(WS_HQ + bid * 4), ask);
                tag = RFL(old[0]) + 1u;
                while (__any(old[0] == tag || old[1] == tag || old[2] == tag || old[3] == tag)) ++tag;);
            AT2(6 + (int)done * 64,
                WAIT_V(2);
                const u32x2 r = *(LAS u32x2*)(lds + K1_HS + 1024 + (bid & 127) * 8);
                if (RFL(r[0]) == ask) { tag = RFL(r[1]); done = 1u; HST32(LANE0(WS_HA + bid * 4), tag); });
            AT(12,
                WAIT_V(2);
                u32x4 ans = *(LAS u32x4*)(lds + K1_HS + l16);
                if (!(done && ALLANS(ans))) {
                    unsigned spins = 0;
                    _Pragma("nounroll") do {
                        if (bid == 0) { u32x4 q; HLD("buffer_load_dwordx4", q, WS_HQ + l16); REPLY(q); }
                        if (!done) {
                            u32x2 r; HLD("buffer_load_dwordx2", r, WS_HR + bid * 8);
                            if (RFL(r[0]) == ask) { tag = RFL(r[1]); done = 1u; HST32(LANE0(WS_HA + bid * 4), tag); }
                        }
                        HLD("buffer_load_dwordx4", ans, WS_HA + l16);
                    } while (!(done && ALLANS(ans)) && ++spins < (1u << 18));
                }
                HDMA(ldsb + K1_GTAB, l16, sH, WS_G + pm * 1024););
            GO(1, 2, 6);
            BAR(); SEC3(1, , PF_WB(t), PF_D(t + 1 - PF_D0), );
            GO(3, 4, 5);
        }
    }
    asm volatile("s_nop 15" ::: "memory");
    if (wc == 0) BAR();
    RELANE();
    STAMP(0, 2);
    LT(2);
    FSTAMP(24);
    kptr ka = (kptr)__builtin_amdgcn_kernarg_segment_ptr(); asm volatile("" : "+s"(ka));
#define KARG(T, i) ((T)ka[i])
    char* const ws2 = KARG(char*, 13);

    LAS char* ctab = lds + K1_CTAB + (wc * 16 + er) * 96;
    LAS char* etab = lds + K1_ETAB + (wc * 16 + er) * 144;
    const f32x4 cst = *(LAS f32x4*)ctab;
    const f32x2 a1 = {cst[0], cst[1]}, cf = {cst[2], cst[3]};
    u32x4 gt[4];
#pragma unroll
    for (int m = 0; m < 4; ++m) gt[m] = *(LAS u32x4*)(lds + K1_GTAB + wr * 256 + eq * 64 + m * 16);
    FSTAMP(25);
    const __amdgpu_buffer_rsrc_t sA2 = SRD(ws2 + WS_A2, WS_A2_BYTES);
#define ST16(voff, v) __builtin_amdgcn_raw_buffer_store_b128(v, sA2, voff, 0, STPOL)
#define PK8(v) (u32x4){pk2(v[0], v[1]), pk2(v[2], v[3]), pk2(v[4], v[5]), pk2(v[6], v[7])}
    const int mblk = pm * 2 + (wr >> 1);
#define IMG_Q(r, q) (unsigned)(((q) >> 1) * 256 + ((q) & 1) * 16 + ((r) & 3) * 32 + mblk * 16384 + (wr & 1) * 1024)
#define IMG_T(r, q) (IMG_Q(r, q) + (unsigned)((((r) >> 2) & 1) * 2048 + ((r) >> 3) * 128 + (j >> 2) * A2S + (j & 3) * 4096))
#define IMG_M(r, q) (IMG_Q(r, q) + (unsigned)((((r) >> 2) & 1) * 2048 + ((r) >> 3) * 512 + (24 + (j >> 2)) * A2S + (j & 3) * 4096))
#define IMG_S(r, q) (IMG_Q(r, q) + (unsigned)((((r) >> 2) & 1) * 4096 + ((r) >> 3) * 2048 + (32 + (j >> 1)) * A2S + (j & 1) * 8192))
    const unsigned lo = IMG_T(er, eq);
#define GLU_HALF(n, hh, o) do { float v[8]; \
        _Pragma("unroll") for (int e = 0; e < 8; ++e) { const int m = 2 * (hh) + (e >> 2), q = e & 3; v[e] = relu(acc[m][n][q]) * (acc[m][3 + (n)][q] * bfhi(gt[m][q])); } \
        o = PK8(v); } while (0)
#define GLU_TILE(n) do { u32x4 o_; GLU_HALF(n, 0, o_); ST16(lo + (wc * 48 + 8 * (n)) * A2S, o_); GLU_HALF(n, 1, o_); ST16(lo + (wc * 48 + 8 * (n)) * A2S + 512, o_); } while (0)
    GLU_TILE(0);
    GLU_TILE(1);
    asm volatile("" ::: "memory");
    {
        f32x2 h = {acc[0][7][0], acc[0][8][0]};
#pragma unroll
        for (int e = 1; e < 16; ++e) h = cfma(a1, h, (f32x2){acc[e >> 2][7][e & 3], acc[e >> 2][8][e & 3]});
        *(LAS f32x2*)(etab + (wr * 4 + eq) * 8) = h;
    }
    WAIT_V(0); WAIT_L0();
    FSTAMP(26);
    BAR(); asm volatile("" ::: "memory");
    gu32* rdy = (gu32*)(ws2 + WS_RDY);
    if (tz == 192) __hip_atomic_store(rdy + pm * 32 + j, 1u, RLX_AGENT);
    gu64* carry = (gu64*)(ws2 + WS_CARRY);
#define CHAN(c, r) (32 * j + 8 * (c) + 4 * ((r) >> 3) + 16 * (((r) >> 2) & 1) + ((r) & 3))
    if (wid < 2 && pm < 7) {
        const f32x2 a16 = *(LAS f32x2*)(lds + K1_CTAB + (wid * 16 + er) * 96 + 24);
        f32x4 E[8];
#pragma unroll
        for (int i = 0; i < 8; ++i) E[i] = *(LAS f32x4*)(lds + K1_ETAB + (wid * 16 + er) * 144 + i * 16);
        f32x2 run = {E[0][0], E[0][1]};
#pragma unroll
        for (int i = 1; i < 16; ++i) run = cfma(a16, run, (f32x2){E[i >> 1][(i & 1) * 2], E[i >> 1][(i & 1) * 2 + 1]});
        unsigned ux = __float_as_uint(run.x), uy = __float_as_uint(run.y);
        if (ux == 0xFFFFFFFFu) ux = 0x7FC00000u;
        if (eq == 0) __hip_atomic_store(carry + pm * 1024 + CHAN(wid, er), ((unsigned long long)uy << 32) | ux, RLX_AGENT);
    }
    STAMP(0, 3);
    FSTAMP(27);
    GLU_TILE(2);
    {
        float v[8];
        const bool lowh = er < 8;
#pragma unroll
        for (int e = 0; e < 8; ++e) {
            const int m = e >> 2, q = e & 3;
            const float v0 = lowh ? relu(acc[m][6][q]) : acc[m][6][q] * bfhi(gt[m][q]);
            const float v1 = lowh ? relu(acc[2 + m][6][q]) : acc[2 + m][6][q] * bfhi(gt[2 + m][q]);
            const float rcv = __int_as_float(__builtin_amdgcn_mov_dpp(__float_as_int(lowh ? v1 : v0), 0x128, 0xf, 0xf, true));
            v[e] = (lowh ? v0 : v1) * rcv;
        }
        ST16(IMG_M(er, eq) + wc * 128, PK8(v));
    }
    LAS f32x4* park = (LAS f32x4*)(lds + 3 * 32768 + wr * 8192 + (tz & 63) * 16);
    if (wc == 1) {
#pragma unroll
        for (int m = 0; m < 4; ++m) { park[m * 128] = acc[m][7]; park[m * 128 + 64] = acc[m][8]; }
    }
    STAMP(0, 4);
    WAIT_L0();
    FSTAMP(28);
    BAR(); asm volatile("" ::: "memory");
    FSTAMP(29);

    ROLE(bid, idx);
    const int n0 = nblk * 128;
    const int rs0 = late ? (48 + SHIFT) % 5 : (40 - SHIFT) % 5;

    if (wc == 1) {
        int ty = threadIdx.x; asm volatile("" : "+v"(ty) :: "memory");
        const int ln = ty & 63, e3 = ln >> 3;
#define LEVEL(lvl) do { if (ty == 256) __hip_atomic_store(rdy + pm * 32 + j, (unsigned)(lvl), RLX_AGENT); } while (0)
        const __amdgpu_buffer_rsrc_t sC = SRD(late ? KARG(const u16*, 10) : KARG(const u16*, 9), DM * NP * 2), sW = SRD(KARG(const u16*, 11), FF * DM * 2);
        const unsigned voffA = (unsigned)(ln * 16);
        const unsigned voffC = (unsigned)(e3 * 2048 + (((ln & 7) ^ (e3 & 6)) * 16));
        const unsigned voffD = (unsigned)(((e3 >> 2) * 8 + (e3 & 3)) * 2048 + ((((ln >> 1) & 3) ^ (e3 >> 1)) * 32) + (ln & 1) * 16);
        const unsigned soA = (unsigned)((late ? 0 : 48) * A2S + mb2 * 16384 + wr * 4096);
        const unsigned soC = (unsigned)((n0 + 32 * wr) * 2048);
        const unsigned soD = (unsigned)(((late ? 0 : 48) + 112 * wr) * 2048 + n0 * 2);
        const unsigned mB = ldsb + wr * 4096;
#define DMA_A(i, k, sl) dma16c(mB + (sl) + (i) * 1024, voffA, sA2, soA + (k) * A2S + (i) * 1024)
#define DMA_C(srd, i, p, sl) dma16(mB + (sl) + 16384 + (i) * 1024, voffC, srd, soC + (p) * 128 + (i) * (8 * 2048))
#define DMA_D(i, row, sl) dma16(mB + (sl) + 16384 + (i) * 1024, voffD, sW, soD + (row) * 2048 + ((i) >> 1) * (4 * 2048) + ((i) & 1) * 128)
#define POLL(v, p) asm volatile("global_load_dword %0, %1, off sc1\n\ts_waitcnt vmcnt(0)" : "=v"(v) : "v"(p) : "memory")
#define ASK(v, p) asm volatile("global_load_dword %0, %1, off sc1" : "=v"(v) : "v"(p) : "memory")
#define NEED(v, p, lvl) do { asm volatile("" : "+v"(v) :: "memory"); if (!__all((int)(v) >= (lvl))) { unsigned spins_ = 0, w_; \
            do { __builtin_amdgcn_s_sleep(1); POLL(w_, p); } while (!__all((int)w_ >= (lvl)) && ++spins_ < (1u << 18)); } } while (0)
        gu32* rl = rdy + (mb2 >> 1) * 32 + (ln & 31);
#define XP(k_) (((((k_) & 3) << 1) | (((k_) >> 2) & 1)) ^ (xc & 1))
        unsigned fv;
#pragma unroll
        for (int p = 0; p < 3; ++p)
#pragma unroll
            for (int i = 0; i < 4; ++i) DMA_D(i, 448 * XP(p), p * 32768);
        POLL(fv, rl);
        {
            const int other = ((ln >> 2) ^ xc) & 1;
            unsigned spins = 0;
            while (!__all((int)fv >= 1 || (GATE2 && other)) && ++spins < (1u << 18)) { __builtin_amdgcn_s_sleep(PSLEEP); POLL(fv, rl); }
        }
        FSTAMP(31);
#pragma unroll
        for (int p = 0; p < 3; ++p)
#pragma unroll
            for (int i = 0; i < 4; ++i) DMA_A(i, XP(p), p * 32768);
        WAIT_V(8); FSTAMP(32); BAR(); FSTAMP(33); LT(3);
        int k = 3, sl = 3 * 32768;
#define A4(img, sl) dma4c(mB + (sl), voffA, sA2, soA + (img) * A2S)
#define D4(row, sl) do { const unsigned b_ = soD + (row) * 2048; \
            asm volatile("s_mov_b32 m0, %0\n\ts_nop 0\n\tbuffer_load_dwordx4 %1, %2, %3 offen" BPOL " lds\n\tbuffer_load_dwordx4 %1, %2, %4 offen offset:2048" BPOL " lds\n\t" \
                         "buffer_load_dwordx4 %1, %2, %5 offen offset:3072" BPOL " lds\n\ts_mov_b32 m0, %6\n\ts_nop 0\n\tbuffer_load_dwordx4 %1, %2, %3 offen offset:128" BPOL " lds" \
                         ::"s"(mB + (sl) + 16384), "v"(voffD), "s"(sW), "s"(b_), "s"(b_ + 8192 - 2048), "s"(b_ + 8192 + 128 - 3072), "s"(mB + (sl) + 16384 + 1024 - 128) : "m0", "memory"); } while (0)
#define C4(srd, p, sl) do { const unsigned b_ = soC + (p) * 128; dma4(mB + (sl) + 16384, voffC, srd, b_, b_ + 16384 - 1024, b_ + 32768 - 2048, b_ + 49152 - 3072); } while (0)
#define STAGE_W() do { const int q_ = k < 16 ? (k & 3) | ((k & 4) << 1) | ((k & 8) >> 1) : k, x_ = XP(q_), row_ = 448 * x_ + ((q_ >> 3) == 3 ? 96 : 16 * (q_ >> 3)); \
            A4((q_ & ~7) + x_, sl); D4(row_, sl); ++k; sl = sl == 4 * 32768 ? 0 : sl + 32768; } while (0)
#define LSYNC() do { WAIT_V(16); BAR(); } while (0)
        STAGE_W(); WAIT_V(12); BAR();
        STAGE_W(); LSYNC(); STAGE_W(); LSYNC();
        ASK(fv, rl);
        STAGE_W(); LSYNC(); STAGE_W(); LSYNC();
        NEED(fv, rl, 1);
        STAGE_W(); LSYNC(); STAGE_W(); LSYNC();
        LEVEL(2);
#pragma nounroll
        while (k < 13) { STAGE_W(); LSYNC(); }
        ASK(fv, rl);
        STAGE_W(); LSYNC(); STAGE_W(); LSYNC(); STAGE_W(); LSYNC();
        NEED(fv, rl, 2);
        const int nw = late ? 32 : 24;
#pragma nounroll
        while (k < nw) { STAGE_W(); LSYNC(); }
        const int np = late ? 16 : 16 - SHIFT;
#pragma nounroll
        for (int p = 0; p < np; ++p) {
            A4(k, sl); C4(sC, p, sl);
            ++k; sl = sl == 4 * 32768 ? 0 : sl + 32768;
            LSYNC();
        }
        if (SHIFT && late) {
            const __amdgpu_buffer_rsrc_t sR = SRD(KARG(const u16*, 9), DM * NP * 2);
#pragma nounroll
            for (int p = 16 - SHIFT; p < 16; ++p) {
                A4(72 + p, sl); C4(sR, p, sl);
                sl = sl == 4 * 32768 ? 0 : sl + 32768;
                LSYNC();
            }
        }
        const int tb = mb2 * 128 + (wr >> 1) * 64 + (wr & 1) * 8 + (ln >> 5) * 16, dcol = n0 + (ln & 31) * 4;
#define ROW(i) (tb + ((i) >> 3) * 32 + ((i) & 7))
        LT(4);
        FSTAMP(40);
        gu32* flag = (gu32*)(ws2 + WS_FLAG2 + tile * 128 + wr * 32);
        const __amdgpu_buffer_rsrc_t sP = SRD(KARG(float*, 14), SEQ * DM * 4);
#define POFF(i) ((ROW(i) * DM + dcol) * 4)
        const int wn1 = (ln >> 4) & 1, s0 = rs0 + wn1, s1 = rs0 + 2 + wn1;
        const int inp = wr * 8192 + ((ln >> 2) & 3) * 1024 + (ln & 3) * 256, sw = (ln & 15) ^ ((ln >> 5) * 8);
        LAS char* q0 = lds + (s0 >= 5 ? s0 - 5 : s0) * 32768 + inp;
        LAS char* q1 = lds + (s1 >= 5 ? s1 - 5 : s1) * 32768 + inp;
#define SUMS(i) (*(LAS f32x4*)(q0 + ((i) >> 3) * 4096 + ((sw ^ ((i) & 7)) * 16)) + *(LAS f32x4*)(q1 + ((i) >> 3) * 4096 + ((sw ^ ((i) & 7)) * 16)))
        f32x4 ex[16];
        if (late) {
            const __amdgpu_buffer_rsrc_t sO = SRD(KARG(float*, 14), SEQ * DM * 4);
            WAIT_V(8); BAR();
            FSTAMP(41);
            ASK(fv, flag);
            WAIT_V(0); BAR();
            FSTAMP(42);
            NEED(fv, flag, 1);
            FSTAMP(43);
#pragma unroll
            for (int i = 0; i < 16; ++i) ex[i] = __builtin_bit_cast(f32x4, __builtin_amdgcn_raw_buffer_load_b128(sP, POFF(i), 0, 16));
            FSTAMP(44);
            BAR(); asm volatile("" ::: "memory");
            FSTAMP(45);
            STAMP(1, 3);
#pragma unroll
            for (int i = 0; i < 16; ++i) __builtin_amdgcn_raw_buffer_store_b128(__builtin_bit_cast(u32x4, ex[i] + SUMS(i)), sO, (ROW(i) * DM + dcol) * 4, 0, OUTPOL);
        } else {
            const __amdgpu_buffer_rsrc_t sXk = SRD(KARG(const u16*, 0), SEQ * DM * 2), sDk = SRD(KARG(const u16*, 12), DM * 2), sGk = SRD(ws2 + WS_G, 8192);
            WAIT_V(8); BAR();
            FSTAMP(41);
            u32x2 dk = __builtin_amdgcn_raw_buffer_load_b64(sDk, dcol * 2, 0, 0), xv[16]; u32x4 gwd[4];
#pragma unroll
            for (int i = 0; i < 4; ++i) gwd[i] = __builtin_amdgcn_raw_buffer_load_b128(sGk, ROW(4 * i) * 4, 0, 16);
#pragma unroll
            for (int i = 0; i < 16; ++i) xv[i] = __builtin_amdgcn_raw_buffer_load_b64(sXk, (tb * DM + dcol) * 2 + (i & 1) * 2048, (((i) >> 3) * 32 + ((i) & 6)) * 2048, 0);
            WAIT_V(21); BAR();
            FSTAMP(42);
            asm volatile("" : "+v"(dk), "+v"(gwd[0]), "+v"(gwd[1]), "+v"(gwd[2]), "+v"(gwd[3]));
#pragma unroll
            for (int i = 0; i < 16; ++i) {
                const float g = bflo(gwd[i >> 2][i & 3]);
                ex[i] = (f32x4){g * rbf(bflo(xv[i][0]) * bflo(dk[0])), g * rbf(bfhi(xv[i][0]) * bfhi(dk[0])),
                                g * rbf(bflo(xv[i][1]) * bflo(dk[1])), g * rbf(bfhi(xv[i][1]) * bfhi(dk[1]))};
                asm volatile("" : "+v"(ex[i]));
            }
            FSTAMP(44);
            BAR(); asm volatile("" ::: "memory");
            FSTAMP(45);
            STAMP(1, 3);
#pragma unroll
            for (int i = 0; i < 16; ++i) __builtin_amdgcn_raw_buffer_store_b128(__builtin_bit_cast(u32x4, ex[i] + SUMS(i)), sP, POFF(i), 0, 16);
            FSTAMP(46);
            WAIT_V(0);
            if (ln == 0) __hip_atomic_store(flag, 1u, RLX_AGENT);
        }
        STAMP(1, 6);
        STAMP(1, 7);
        FSTAMP(47);
        FSTAMP(48);
        FDUMP();
        asm volatile(".Lk1_end_a:");
        return;
    }

    {
        unsigned long long gq[2][7];
#pragma unroll
        for (int c = 0; c < 2; ++c)
#pragma unroll
            for (int q = 0; q < 7; ++q) gq[c][q] = __hip_atomic_load(carry + (q + pm >= 7 ? q + pm - 7 : 0) * 1024 + CHAN(c, er), RLX_AGENT);
        f32x2 h[2];
        {
            f32x2 a16[2], run[2]; f32x4 E[2][6], F0[2]; f32x2 F1[2];
#pragma unroll
            for (int c = 0; c < 2; ++c) {
                a16[c] = *(LAS f32x2*)(ctab + c * (16 * 96) + 24);
#pragma unroll
                for (int i = 0; i < 6; ++i) E[c][i] = *(LAS f32x4*)(etab + c * (16 * 144) + i * 16);
                F0[c] = *(LAS f32x4*)(etab + c * (16 * 144) + wr * 32); F1[c] = *(LAS f32x2*)(etab + c * (16 * 144) + wr * 32 + 16);
                run[c] = (f32x2){0.f, 0.f};
            }
#pragma unroll
            for (int w = 0; w < 3; ++w)
                if (wr > w) {
#pragma unroll
                    for (int i = 0; i < 4; ++i)
#pragma unroll
                        for (int c = 0; c < 2; ++c) run[c] = cfma(a16[c], run[c], (f32x2){E[c][2 * w + (i >> 1)][(i & 1) * 2], E[c][2 * w + (i >> 1)][(i & 1) * 2 + 1]});
                }
#pragma unroll
            for (int c = 0; c < 2; ++c) {
                const f32x2 r1 = cfma(a16[c], run[c], (f32x2){F0[c][0], F0[c][1]}), r2 = cfma(a16[c], r1, (f32x2){F0[c][2], F0[c][3]}), r3 = cfma(a16[c], r2, F1[c]);
                h[c] = sel4(eq, run[c], r1, r2, r3);
            }
        }
        f32x4 ur[4], ui[4];
#pragma unroll
        for (int m = 0; m < 4; ++m) { ur[m] = park[m * 128]; ui[m] = park[m * 128 + 64]; }
        const f32x4 cs1 = *(LAS f32x4*)(ctab + 16 * 96);
        FSTAMP(30);
        f32x2 a256[2], aq[2];
#pragma unroll
        for (int c = 0; c < 2; ++c) {
            LAS char* ct = ctab + c * (16 * 96);
            a256[c] = *(LAS f32x2*)(ct + 80); aq[c] = cmul(*(LAS f32x2*)(ct + 16 + eq * 8), *(LAS f32x2*)(ct + 48 + wr * 8));
        }
        unsigned mx = 0;
#pragma unroll
        for (int q = 0; q < 7; ++q) mx = max(mx, max((unsigned)gq[0][q], (unsigned)gq[1][q]));
        if (pm > 0 && __any(mx == 0xFFFFFFFFu)) {
            unsigned spins = 0;
            do {
                mx = 0;
#pragma unroll
                for (int c = 0; c < 2; ++c)
#pragma unroll
                    for (int q = 0; q < 7; ++q) {
                        gq[c][q] = __hip_atomic_load(carry + (q + pm >= 7 ? q + pm - 7 : 0) * 1024 + CHAN(c, er), RLX_AGENT);
                        mx = max(mx, (unsigned)gq[c][q]);
                    }
            } while (__any(mx == 0xFFFFFFFFu) && ++spins < (1u << 18));
        }
        f32x2 cg[2] = {{0.f, 0.f}, {0.f, 0.f}};
#pragma unroll
        for (int q = 0; q < 7; ++q) {
            const unsigned keep = q + pm >= 7 ? 0xFFFFFFFFu : 0u;
#pragma unroll
            for (int c = 0; c < 2; ++c) cg[c] = cfma(a256[c], cg[c], (f32x2){__uint_as_float((unsigned)gq[c][q] & keep), __uint_as_float((unsigned)(gq[c][q] >> 32) & keep)});
        }
        h[0] = cfma(aq[0], cg[0], h[0]); h[1] = cfma(aq[1], cg[1], h[1]);
        STAMP(0, 5);
        FSTAMP(31);
        const unsigned ls = IMG_S(er, eq);
        const f32x2 b1 = {cs1[0], cs1[1]}, bf = {cs1[2], cs1[3]};
#pragma unroll
        for (int hh = 0; hh < 2; ++hh) {
            float vr[8], vi[8], wr8[8], wi8[8];
#pragma unroll
            for (int e = 0; e < 8; ++e) {
                const int m = 2 * hh + (e >> 2), q = e & 3;
                h[0] = cfma(a1, h[0], (f32x2){acc[m][7][q], acc[m][8][q]});
                h[1] = cfma(b1, h[1], (f32x2){ur[m][q], ui[m][q]});
                const float g2 = bflo(gt[m][q]);
                const f32x2 s0 = cmul(cf, h[0]), s1 = cmul(bf, h[1]);
                vr[e] = s0.x * g2; vi[e] = -s0.y * g2; wr8[e] = s1.x * g2; wi8[e] = -s1.y * g2;
            }
            ST16(ls + 40 * A2S + hh * 512, PK8(vr)); ST16(ls + 40 * A2S + hh * 512 + 128, PK8(wr8));
            ST16(ls + hh * 512, PK8(vi)); ST16(ls + hh * 512 + 128, PK8(wi8));
        }
    }
    STAMP(0, 6);
    int ty = threadIdx.x; asm volatile("" : "+v"(ty) :: "memory");
    const int ln = ty & 63, fr2 = ty & 15, fq2 = (ty >> 4) & 3;
    const int wn = wid & 1, kk = wid >> 1;
    LAS char* rH = lds + kk * 8192 + (ln >> 5) * 4096 + (ln & 31) * 8;
    LAS char* rC = lds + 16384 + (wn * 64 + fr2) * 128 + (((4 * kk + fq2) ^ (fr2 & 6)) * 16);
    LAS char* rWb = lds + 16384 + kk * 8192 + (ln >> 5) * 4096 + wn * 1024 + ((ln >> 2) & 7) * 128 + (ln & 3) * 8;
    const int rsw = (ln >> 3) & 3;
    f32x4 ac2[4][8];
    STAMP(0, 7);
    FSTAMP(32);
    FDUMP();
    s16x4 HX[8][2], HY[8][2], WX[4][2], WY[4][2];
    int sl = 0;

#define RD1(RD, i, H, W) do { \
        if ((i) < 8) { \
            if (RD == 1) W[(i) >> 1][(i) & 1] = tr4(rWb + sl + ((((i) >> 1) ^ rsw) * 32) + ((i) & 1) * 2048); \
            else if (((i) & 1) == 0) { const s16x8 v_ = *(LAS s16x8*)(rC + sl + ((i) >> 1) * 2048); \
                W[(i) >> 1][0] = __builtin_shufflevector(v_, v_, 0, 1, 2, 3); W[(i) >> 1][1] = __builtin_shufflevector(v_, v_, 4, 5, 6, 7); } \
        } else H[((i) - 8) >> 1][(i) & 1] = tr4(rH + sl + (((i) - 8) >> 1) * 256 + ((i) & 1) * 2048); \
    } while (0)
#define MM(i, HC, WC) mfma_acc(ac2[(i) & 3][(i) >> 2], cat8(WC[(i) & 3][0], WC[(i) & 3][1]), cat8(HC[(i) >> 2][0], HC[(i) >> 2][1]))
#define STEP(RD, HC, WC, HN, WN) do { \
        _Pragma("unroll") for (int i = 0; i < 32; ++i) { MM(i, HC, WC); if (i < 24) RD1(RD, i, HN, WN); } \
        sl = sl == 4 * 32768 ? 0 : sl + 32768; LGKM0(); \
    } while (0)

    LGKM0();
    BAR();
    STAMP_TAKE(st9);
#pragma unroll
    for (int i = 0; i < 24; ++i) RD1(1, i, HX, WX);
    sl = 32768; LGKM0();
    BAR();
#pragma unroll
    for (int i = 0; i < 32; ++i) {
        asm volatile("v_mfma_f32_16x16x32_bf16 %0, %1, %2, 0" : "=&v"(ac2[i & 3][i >> 2]) : "v"(cat8(WX[i & 3][0], WX[i & 3][1])), "v"(cat8(HX[i >> 2][0], HX[i >> 2][1])) : "memory");
        if (i < 24) RD1(1, i, HY, WY);
    }
    sl = 2 * 32768; LGKM0();
    BAR(); STEP(1, HY, WY, HX, WX);
    const int nf = late ? 15 : 11;
#pragma nounroll
    for (int i = 1; i < nf; ++i) {
        if (i == 3) WAIT_V(0);
        BAR(); STEP(1, HX, WX, HY, WY); BAR(); STEP(1, HY, WY, HX, WX);
    }
    BAR(); STEP(1, HX, WX, HY, WY); BAR(); STEP(0, HY, WY, HX, WX);
#pragma nounroll
    for (int i = late ? -SHIFT / 2 : SHIFT / 2; i < 7; ++i) { BAR(); STEP(0, HX, WX, HY, WY); BAR(); STEP(0, HY, WY, HX, WX); }
    BAR(); STEP(0, HX, WX, HY, WY);
    {
        const int sw = rs0 + wid;
        LAS char* dmp = lds + (sw >= 5 ? sw - 5 : sw) * 32768 + fq2 * 256;
#define DUMP(i) *(LAS f32x4*)(dmp + (i) * 1024 + ((fr2 ^ (4 * ((i) & 3) + fq2)) * 16)) = ac2[(i) & 3][(i) >> 2]
#pragma unroll
        for (int i = 0; i < 32; ++i) { MM(i, HY, WY); if (i >= 12) DUMP(i - 12); }
        asm volatile("s_nop 15" ::: "memory");
#pragma unroll
        for (int i = 20; i < 32; ++i) DUMP(i);
    }
    WAIT_L0();
    STAMP_PUT(1, 0, st0); STAMP_PUT(1, 1, st9);
    STAMP(1, 2);
    BAR();
    asm volatile(".Lk1_end_b:");
}

extern "C" void kernel_launch(void* const* d_in, const int* in_sizes, int n_in, void* d_out, int out_size, void* d_ws, size_t ws_size,
                              hipStream_t stream) {
    const u16* x = (const u16*)d_in[0];
    const u16* lre = (const u16*)d_in[1];
    const u16* lim = (const u16*)d_in[2];
    const u16* Bre = (const u16*)d_in[3];
    const u16* Bim = (const u16*)d_in[4];
    const u16* Cre = (const u16*)d_in[5];
    const u16* Cim = (const u16*)d_in[6];
    const u16* logdt = (const u16*)d_in[7];
    const u16* dskip = (const u16*)d_in[8];
    const u16* Wg = (const u16*)d_in[10];
    const u16* Wu = (const u16*)d_in[11];
    const u16* Wd = (const u16*)d_in[12];
    const u16* gw = (const u16*)d_in[13];
    char* ws = (char*)d_ws;
    (void)hipFuncSetAttribute((const void*)k1_fused, hipFuncAttributeMaxDynamicSharedMemorySize, K2_LDS);
    k1_fused<<<256, 512, K2_LDS, stream>>>(x, Wg, Wu, Bre, Bim, lre, lim, logdt, gw, Cre, Cim, Wd, dskip, ws, (float*)d_out);
}
```

```cpp
#include <hip/hip_runtime.h>
#include <hip/hip_bf16.h>


typedef unsigned short u16;
typedef __bf16 bf16x8 __attribute__((ext_vector_type(8)));
typedef __bf16 bf16x2 __attribute__((ext_vector_type(2)));
typedef short s16x4 __attribute__((ext_vector_type(4)));
typedef short s16x8 __attribute__((ext_vector_type(8)));
typedef float f32x4 __attribute__((ext_vector_type(4)));
typedef float f32x2 __attribute__((ext_vector_type(2)));
typedef unsigned u32x4 __attribute__((ext_vector_type(4)));
typedef unsigned u32x2 __attribute__((ext_vector_type(2)));
#define LAS __attribute__((address_space(3)))
#define GAS __attribute__((address_space(1)))
typedef GAS unsigned gu32;
typedef GAS unsigned long long gu64;
typedef __attribute__((address_space(4))) const unsigned long long* kptr;
#define RLX_AGENT __ATOMIC_RELAXED, __HIP_MEMORY_SCOPE_AGENT

constexpr int SEQ = 2048, DM = 1024, NP = 1024, FF = 3584;
constexpr int KSTEPS2 = (2 * NP + FF) / 64;

constexpr unsigned WS_CARRY = 0;
constexpr unsigned WS_G = 7 * 8192;
constexpr unsigned WS_RDY = 65536;
constexpr unsigned WS_FLAG2 = WS_RDY + 1024;
constexpr unsigned WS_HQ = WS_FLAG2 + 16384;
constexpr unsigned WS_HA = WS_HQ + 1024;
constexpr unsigned WS_HR = WS_HA + 1024;
constexpr unsigned WS_SYNC_BYTES = WS_HR + 2048;
constexpr size_t WS_A2 = 1 << 20;
constexpr unsigned A2S = 262144 + 128;
constexpr size_t WS_A2_BYTES = (size_t)KSTEPS2 * A2S;

__device__ __forceinline__ float bflo(unsigned v) { return __uint_as_float(v << 16); }
__device__ __forceinline__ float bfhi(unsigned v) { return __uint_as_float(v & 0xffff0000u); }
__device__ __forceinline__ float rbf(float f) { return (float)(__bf16)f; }
__device__ __forceinline__ unsigned pk2(float a, float b) {
    bf16x2 v; v.x = (__bf16)a; v.y = (__bf16)b; return __builtin_bit_cast(unsigned, v);
}
__device__ __forceinline__ float relu(float v) { return __int_as_float(max(__float_as_int(v), 0)); }
__device__ __forceinline__ float dot2(unsigned a, unsigned b, float c) {
    return __builtin_amdgcn_fdot2_f32_bf16(__builtin_bit_cast(bf16x2, a), __builtin_bit_cast(bf16x2, b), c, false);
}

#define WAIT_V(n) asm volatile("s_waitcnt vmcnt(" #n ")" ::: "memory")
#define WAIT_L0() asm volatile("s_waitcnt lgkmcnt(0)" ::: "memory")
#define LGKM0() __builtin_amdgcn_s_waitcnt(0xC07F)
#define BAR() do { asm volatile("" ::: "memory"); __builtin_amdgcn_s_barrier(); asm volatile("" ::: "memory"); } while (0)
#define SCHED() __builtin_amdgcn_sched_barrier(0)
#define SRD(p, bytes) __builtin_amdgcn_make_buffer_rsrc((void*)(p), (short)0, (int)(bytes), 0x00020000)

__device__ __forceinline__ void dma16(unsigned m0v, unsigned voff, __amdgpu_buffer_rsrc_t srd, unsigned soff) {
    asm volatile("s_mov_b32 m0, %0\n\ts_nop 0\n\tbuffer_load_dwordx4 %1, %2, %3 offen lds" ::"s"(m0v), "v"(voff), "s"(srd), "s"(soff) : "m0", "memory");
}
__device__ __forceinline__ bf16x8 lds_b128(LAS char* p) { return __builtin_bit_cast(bf16x8, *(LAS s16x8*)p); }
__device__ __forceinline__ bf16x8 lds_tr(LAS char* p0, LAS char* p1) {
    s16x4 a = __builtin_amdgcn_ds_read_tr16_b64_v4i16((LAS s16x4*)p0);
    s16x4 b = __builtin_amdgcn_ds_read_tr16_b64_v4i16((LAS s16x4*)p1);
    return __builtin_bit_cast(bf16x8, __builtin_shufflevector(a, b, 0, 1, 2, 3, 4, 5, 6, 7));
}
__device__ __forceinline__ f32x2 cmul(f32x2 a, f32x2 b) { return (f32x2){__builtin_fmaf(a.x, b.x, -(a.y * b.y)), __builtin_fmaf(a.x, b.y, a.y * b.x)}; }
__device__ __forceinline__ f32x2 cfma(f32x2 a, f32x2 b, f32x2 c) {
    return (f32x2){__builtin_fmaf(a.x, b.x, __builtin_fmaf(-a.y, b.y, c.x)), __builtin_fmaf(a.x, b.y, __builtin_fmaf(a.y, b.x, c.y))};
}
__device__ __forceinline__ f32x2 sel4(int i, f32x2 a, f32x2 b, f32x2 c, f32x2 d) {
    const bool o = i & 1, u = i & 2;
    const float lx = o ? b.x : a.x, ly = o ? b.y : a.y, hx = o ? d.x : c.x, hy = o ? d.y : c.y;
    return (f32x2){u ? hx : lx, u ? hy : ly};
}

constexpr int K2_LDS = 5 * 32768;
constexpr int PF_D0 = 4;

__device__ __forceinline__ void dma4c(unsigned m0v, unsigned voff, __amdgpu_buffer_rsrc_t srd, unsigned soff) {
    asm volatile("s_mov_b32 m0, %0\n\ts_nop 0\n\tbuffer_load_dwordx4 %1, %2, %3 offen sc1 lds\n\tbuffer_load_dwordx4 %1, %2, %3 offen offset:1024 sc1 lds\n\t"
                 "buffer_load_dwordx4 %1, %2, %3 offen offset:2048 sc1 lds\n\tbuffer_load_dwordx4 %1, %2, %3 offen offset:3072 sc1 lds" ::"s"(m0v), "v"(voff), "s"(srd), "s"(soff) : "m0", "memory");
}
__device__ __forceinline__ void dma4(unsigned m0v, unsigned voff, __amdgpu_buffer_rsrc_t srd, unsigned s0, unsigned s1, unsigned s2, unsigned s3) {
    asm volatile("s_mov_b32 m0, %0\n\ts_nop 0\n\tbuffer_load_dwordx4 %1, %2, %3 offen lds\n\tbuffer_load_dwordx4 %1, %2, %4 offen offset:1024 lds\n\t"
                 "buffer_load_dwordx4 %1, %2, %5 offen offset:2048 lds\n\tbuffer_load_dwordx4 %1, %2, %6 offen offset:3072 lds" ::"s"(m0v), "v"(voff), "s"(srd), "s"(s0), "s"(s1), "s"(s2), "s"(s3) : "m0", "memory");
}
__device__ __forceinline__ void mfma_acc(f32x4& c, bf16x8 a, bf16x8 b) {
    asm volatile("v_mfma_f32_16x16x32_bf16 %0, %1, %2, %0" : "+v"(c) : "v"(a), "v"(b) : "memory");
}
__device__ __forceinline__ void mfma_acc2(f32x4& c, bf16x8 a0, bf16x8 b0, bf16x8 a1, bf16x8 b1) {
    asm volatile("v_mfma_f32_16x16x32_bf16 %0, %1, %2, %0\n\tv_mfma_f32_16x16x32_bf16 %0, %3, %4, %0" : "+v"(c) : "v"(a0), "v"(b0), "v"(a1), "v"(b1) : "memory");
}
__device__ __forceinline__ s16x4 tr4(LAS char* p) { return __builtin_amdgcn_ds_read_tr16_b64_v4i16((LAS s16x4*)p); }
__device__ __forceinline__ bf16x8 cat8(s16x4 a, s16x4 b) { return __builtin_bit_cast(bf16x8, __builtin_shufflevector(a, b, 0, 1, 2, 3, 4, 5, 6, 7)); }

constexpr int K1_ASB = 40960;
constexpr int K1_W = 81920;
constexpr int K1_WBUF = 28704, K1_UOFF = 14352;
constexpr int K1_ETAB = K1_W + 2 * K1_WBUF;
constexpr int K1_CTAB = K1_ETAB + 32 * 144;
constexpr int K1_SINK = K1_CTAB + 32 * 96;
constexpr int K1_GTAB = K1_SINK + 1024;
constexpr int K1_HS = K1_GTAB + 1024;

__global__ __launch_bounds__(512, 2) void k1_fused(const u16* __restrict__ x, const u16* __restrict__ Wg, const u16* __restrict__ Wu,
                                                       const u16* __restrict__ Bre, const u16* __restrict__ Bim, const u16* __restrict__ lre,
                                                       const u16* __restrict__ lim, const u16* __restrict__ logdt, const u16* __restrict__ gw,
                                                       const u16* __restrict__ Cre, const u16* __restrict__ Cim, const u16* __restrict__ Wd,
                                                       const u16* __restrict__ dskip, char* __restrict__ ws, float* __restrict__ out) {
    extern __shared__ __attribute__((aligned(16))) char smem[];
    LAS char* lds = (LAS char*)smem;
    const unsigned ldsb = (unsigned)(size_t)lds;
    const int tid = threadIdx.x, lane = tid & 63;
    const int wid = __builtin_amdgcn_readfirstlane(tid >> 6);
    const int wr = wid & 3, wc = wid >> 2;
    const int bid = blockIdx.x, idx = bid >> 3, j = (bid & 7) * 4 + (idx & 3), pm = idx >> 2;
    const int fr = lane & 15, fq = lane >> 4;
#define ROLE(b_, i_) const int xc = (b_) & 7, late = (xc & 1), mb2 = (xc >> 2) * 8 + ((i_) >> 2), nblk = ((xc >> 1) & 1) * 4 + ((i_) & 3), tile = mb2 * 8 + nblk
    const char* code0; unsigned codeA, codeB, codeM;
    asm volatile("s_getpc_b64 %0\n.Lk1_ref:\n\ts_mov_b32 %1, .Lk1_end_a-.Lk1_ref\n\ts_mov_b32 %2, .Lk1_end_b-.Lk1_ref\n\ts_mov_b32 %3, .Lk1_mid-.Lk1_ref"
                 : "=s"(code0), "=s"(codeA), "=s"(codeB), "=s"(codeM));
    const __amdgpu_buffer_rsrc_t sI = SRD(code0, (codeA > codeB ? codeA : codeB) + 192);
    const unsigned mSink = ldsb + K1_SINK;
    const bool pf = wid == 7;
    if (pf) { dma16(mSink, (unsigned)(lane * 128), sI, 0u); dma16(mSink, (unsigned)(lane * 128), sI, 8192u); }

    const int ch = 32 * j + 8 * (fq & 1) + 4 * (fr >> 3) + 16 * ((fr >> 2) & 1) + (fr & 3);
    const int grow = pm * 256 + j * 8 + wid;
    u32x4 xa, xb, ga, gb; unsigned plr, pli, pld;
    {
        const u16* xr = x + (size_t)grow * DM;
        const unsigned l16 = lane * 16, c2 = ch * 2;
        asm volatile("s_nop 4\n\tglobal_load_dwordx4 %0, %7, %9\n\tglobal_load_dwordx4 %1, %7, %9 offset:1024\n\t"
                     "global_load_dwordx4 %2, %7, %10\n\tglobal_load_dwordx4 %3, %7, %10 offset:1024\n\t"
                     "global_load_ushort %4, %8, %11\n\tglobal_load_ushort %5, %8, %12\n\tglobal_load_ushort %6, %8, %13"
                     : "=&v"(xa), "=&v"(xb), "=&v"(ga), "=&v"(gb), "=&v"(plr), "=&v"(pli), "=&v"(pld)
                     : "v"(l16), "v"(c2), "s"(xr), "s"(gw), "s"(lre), "s"(lim), "s"(logdt) : "memory");
    }

    const int q4 = wid + 28, q5 = wid + 35;
    const __amdgpu_buffer_rsrc_t sX = SRD(x, SEQ * DM * 2), sG = SRD(Wg, DM * FF * 2), sU = SRD(Wu, DM * FF * 2);
    const __amdgpu_buffer_rsrc_t s4 = SRD(q4 < 32 ? x : (q4 & 2) ? Bim : Bre, q4 < 32 ? SEQ * DM * 2 : NP * DM * 2);
    const __amdgpu_buffer_rsrc_t s5 = SRD((q5 & 2) ? Bim : Bre, q5 < 40 ? NP * DM * 2 : 0);
    const int l3 = lane >> 3;
    const unsigned voffX = (unsigned)(((l3 >> 2) * 16 + (l3 & 3)) * 2048 + (((lane & 7) ^ (l3 & 6)) * 16));
    unsigned voffW;
    { const int rho = (tid * 4682) >> 16, c = tid - rho * 14, k = (rho & 0x13) | ((rho & 4) << 1) | ((rho & 8) >> 1);
      voffW = (unsigned)(k * (FF * 2) + c * 16); }
#define XROW(q) (pm * 256 + ((q) >> 3) * 64 + (((q) >> 2) & 1) * 8 + ((q) & 1) * 32 + (((q) >> 1) & 1) * 4)
#define BROW(q) (32 * j + 8 * (((q) >> 2) & 1) + 4 * ((q) & 1))
    constexpr unsigned WST = 64 * FF * 2, WSH = 32 * FF * 2;
    const unsigned so0 = (unsigned)(XROW(wid) * 2048), so1 = (unsigned)(XROW(wid + 7) * 2048), so2 = (unsigned)(XROW(wid + 14) * 2048), so3 = (unsigned)(XROW(wid + 21) * 2048),
                   so4 = (unsigned)((q4 < 32 ? XROW(q4) : BROW(q4)) * 2048), so5 = (unsigned)(BROW(q5) * 2048);
    const unsigned soW = (unsigned)(j * 224), mb0 = ldsb + wid * 1024;
    const unsigned m50 = q5 < 40 ? mb0 + 35 * 1024 : mSink, m51 = q5 < 40 ? mb0 + 35 * 1024 + K1_ASB : mSink;
#define ST_X(i, ko, b) dma16(mb0 + (b) * K1_ASB + (i) * 7 * 1024, voffX, sX, ((i) == 0 ? so0 : (i) == 1 ? so1 : (i) == 2 ? so2 : so3) + (ko))
#define ST_S4(ko, b) dma16(mb0 + (b) * K1_ASB + 28 * 1024, voffX, s4, so4 + (ko))
#define ST_S5(ko, b) dma16((b) ? m51 : m50, voffX, s5, so5 + (ko))
#define ST_G(h, t, b) dma16(mb0 + K1_W + (b) * K1_WBUF + (h) * 7168, voffW, sG, soW + (t) * WST + (h) * WSH)
#define ST_U(h, t, b) dma16(mb0 + K1_W + (b) * K1_WBUF + K1_UOFF + (h) * 7168, voffW, sU, soW + (t) * WST + (h) * WSH)
    if (!pf) { ST_X(0, 0, 0); ST_X(1, 0, 0); ST_X(2, 0, 0); ST_X(3, 0, 0); ST_S4(0, 0); ST_S5(0, 0); ST_G(0, 0, 0); ST_G(1, 0, 0); ST_U(0, 0, 0); ST_U(1, 0, 0); }

    const int ch0 = (fq ^ (fr & 6)) * 16;
    LAS char* rA0 = lds + wr * 8192 + fr * 128 + ch0;
    LAS char* rA1 = lds + wr * 8192 + fr * 128 + (ch0 ^ 64);
    LAS char* rS0 = lds + 32768 + wc * 4096 + fr * 128 + ch0;
    LAS char* rS1 = lds + 32768 + wc * 4096 + fr * 128 + (ch0 ^ 64);
    const int trow = (16 * (fq >> 1) + 4 * (fq & 1) + ((lane & 15) >> 2)) * 224;
    LAS char* rW = lds + K1_W + trow + wc * 96 + (lane & 3) * 8;
    LAS char* rM = lds + K1_W + trow + 192 + wc * 16 + (lane & 1) * 8 + ((lane >> 1) & 1) * K1_UOFF;

    f32x4 acc[4][9];
#pragma unroll
    for (int m = 0; m < 4; ++m)
#pragma unroll
        for (int n = 0; n < 9; ++n) { acc[m][n] = (f32x4){0.f, 0.f, 0.f, 0.f}; asm volatile("" : "+v"(acc[m][n])); }
    bf16x8 Af[4][2], Bf[3][2];

#define RD_A(m, b) do { Af[m][0] = lds_b128(rA0 + (b) * K1_ASB + (m) * 2048); Af[m][1] = lds_b128(rA1 + (b) * K1_ASB + (m) * 2048); } while (0)
#define RD_S(slot, ri, b) do { Bf[slot][0] = lds_b128(rS0 + (b) * K1_ASB + (ri) * 2048); Bf[slot][1] = lds_b128(rS1 + (b) * K1_ASB + (ri) * 2048); } while (0)
#define RD_W(slot, base, off) do { Bf[slot][0] = lds_tr((base) + (off), (base) + (off) + 1792); Bf[slot][1] = lds_tr((base) + (off) + 7168, (base) + (off) + 7168 + 1792); } while (0)
#define SEC1(b, d0, d1) do { RD_S(0, 0, b); RD_S(1, 1, b); d0; RD_W(2, rW, (b) * K1_WBUF); RD_A(0, b); d1; RD_A(1, b); RD_A(2, b); RD_A(3, b); } while (0)
#define SEC2(b, d0, d1, d2, d3) do { d0; RD_W(0, rW, (b) * K1_WBUF + 32); d1; RD_W(1, rW, (b) * K1_WBUF + 64); d2; RD_W(2, rM, (b) * K1_WBUF); d3; } while (0)
#define SEC3(b, d0, d1, d2, d3) do { d0; RD_W(0, rW, (b) * K1_WBUF + K1_UOFF); d1; RD_W(1, rW, (b) * K1_WBUF + K1_UOFF + 32); d2; RD_W(2, rW, (b) * K1_WBUF + K1_UOFF + 64); d3; } while (0)
#define GO(n0, n1, n2) do { asm volatile("" ::: "memory"); LGKM0(); BAR(); SCHED(); \
        _Pragma("unroll") for (int m = 0; m < 4; ++m) { \
            mfma_acc2(acc[m][n0], Af[m][0], Bf[0][0], Af[m][1], Bf[0][1]); mfma_acc2(acc[m][n1], Af[m][0], Bf[1][0], Af[m][1], Bf[1][1]); \
            mfma_acc2(acc[m][n2], Af[m][0], Bf[2][0], Af[m][1], Bf[2][1]); } \
        SCHED(); } while (0)

#define RELANE() int tz = threadIdx.x; asm volatile("" : "+v"(tz)); const int er = tz & 15, eq = (tz >> 4) & 3
    asm volatile("" : "+v"(rA0), "+v"(rA1), "+v"(rS0), "+v"(rS1), "+v"(rW), "+v"(rM));
    WAIT_V(10);
    if (pf) WAIT_V(0);
    asm volatile("" : "+v"(xa), "+v"(xb), "+v"(ga), "+v"(gb), "+v"(plr), "+v"(pli), "+v"(pld) :: "memory");
    {
        float s = 0.f, s2 = 0.f;
#pragma unroll
        for (int e = 0; e < 4; ++e) { s = dot2(xa[e], ga[e], s); s2 = dot2(xb[e], gb[e], s2); }
        s += s2;
#define DPP_ADD(ctrl, rmask) s += __int_as_float(__builtin_amdgcn_update_dpp(0, __float_as_int(s), ctrl, rmask, 0xf, true))
        DPP_ADD(0x111, 0xf); DPP_ADD(0x112, 0xf); DPP_ADD(0x114, 0xf); DPP_ADD(0x118, 0xf); DPP_ADD(0x142, 0xa); DPP_ADD(0x143, 0xc);
        const float g = rbf(__builtin_amdgcn_rcpf(1.f + __builtin_amdgcn_exp2f(-1.4426950408889634f * rbf(s))));
        if (lane == 63) __hip_atomic_store((gu32*)(ws + WS_G) + grow, pk2(g, 1.f - g), RLX_AGENT);
    }
    if (!pf) {
        BAR();
        ST_X(0, 128, 1); ST_X(1, 128, 1); ST_X(2, 128, 1); ST_X(3, 128, 1); ST_S4(128, 1); ST_S5(128, 1); ST_G(0, 1, 1); ST_G(1, 1, 1);
        WAIT_V(11);
        if (wc == 1) BAR();
#pragma nounroll
        for (int t = 0; t < 16; t += 2) {
            asm volatile("" : "+s"(t));
            const unsigned ko = t == 14 ? 0x80000000u : (unsigned)(t + 2) * 128u;
            BAR(); SEC1(0, ST_U(0, t + 1, 1), ST_U(1, t + 1, 1)); WAIT_V(10);
            GO(7, 8, 0);
            BAR(); SEC2(0, ST_X(0, ko, 0), ST_X(1, ko, 0), ST_X(2, ko, 0), ST_X(3, ko, 0));
            GO(1, 2, 6);
            BAR(); SEC3(0, ST_G(0, t + 2, 0), ST_G(1, t + 2, 0), ST_S4(ko, 0), ST_S5(ko, 0)); WAIT_V(10);
            GO(3, 4, 5);
            BAR(); SEC1(1, ST_U(0, t + 2, 0), ST_U(1, t + 2, 0)); WAIT_V(10);
            GO(7, 8, 0);
            BAR(); SEC2(1, ST_X(0, ko + 128, 1), ST_X(1, ko + 128, 1), ST_X(2, ko + 128, 1), ST_X(3, ko + 128, 1));
            GO(1, 2, 6);
            BAR(); SEC3(1, ST_G(0, t + 3, 1), ST_G(1, t + 3, 1), ST_S4(ko + 128, 1), ST_S5(ko + 128, 1)); WAIT_V(10);
            GO(3, 4, 5);
        }
    }
    int w7 = wid; asm volatile("" : "+s"(w7));
    if (w7 == 7) {
        {
            const float lr = bflo(plr), li = bflo(pli), dt = rbf(expf(bflo(pld)));
            const float e = expf(dt * lr); float sn, cs; sincosf(dt * li, &sn, &cs);
            const f32x2 a1 = {e * cs, e * sn};
            const float nr = a1.x - 1.f, den = lr * lr + li * li;
            f32x2 a16 = cmul(a1, a1); a16 = cmul(a16, a16); a16 = cmul(a16, a16); a16 = cmul(a16, a16);
            const f32x2 a32 = cmul(a16, a16), a48 = cmul(a32, a16), a64 = cmul(a32, a32), a128 = cmul(a64, a64), a192 = cmul(a128, a64), a256 = cmul(a128, a128);
            if (lane < 32) {
                LAS f32x4* ct = (LAS f32x4*)(lds + K1_CTAB + lane * 96);
                ct[0] = (f32x4){a1.x, a1.y, 2.f * ((nr * lr + a1.y * li) / den), 2.f * ((a1.y * lr - nr * li) / den)};
                ct[1] = (f32x4){1.f, 0.f, a16.x, a16.y}; ct[2] = (f32x4){a32.x, a32.y, a48.x, a48.y};
                ct[3] = (f32x4){1.f, 0.f, a64.x, a64.y}; ct[4] = (f32x4){a128.x, a128.y, a192.x, a192.y};
                ct[5] = (f32x4){a256.x, a256.y, 0.f, 0.f};
            }
        }
        const int pb = (bid >> 3) | ((bid & 7) << 5);
        const __amdgpu_buffer_rsrc_t sPW = SRD((pb & 1) ? Wg : Wu, DM * FF * 2), sPD = SRD((pb & 1) ? Wd : (pb & 2) ? Cim : Cre, (pb & 1) ? DM * FF * 2 : DM * NP * 2);
        const __amdgpu_buffer_rsrc_t sH = SRD(ws, WS_SYNC_BYTES);
        const unsigned l128 = lane * 128, OUT = 0x80000000u;
        const unsigned pds = (pb & 1) ? (1u << 20) : (1u << 19), pw0 = (unsigned)((pb >> 1) * 8192), pd0 = (unsigned)(((pb & 1) ? pb >> 1 : pb >> 2) * 8192);
#define PF_W(r) dma16(mSink, l128, sPW, pw0 + (unsigned)(r) * (1u << 20))
#define PF_D(r) dma16(mSink, l128, sPD, pd0 + (unsigned)(r) * pds)
#define RFL(v) ((unsigned)__builtin_amdgcn_readfirstlane(v))
#define HDMA(m, vo, srd, so) asm volatile("s_nop 4\n\ts_mov_b32 m0, %0\n\ts_nop 0\n\tbuffer_load_dwordx4 %1, %2, %3 offen sc1 lds" ::"s"((unsigned)(m)), "v"(vo), "s"(srd), "s"((unsigned)(so)) : "m0", "memory")
#define HLD(op, v, vo) asm volatile("s_nop 4\n\t" op " %0, %1, %2, 0 offen sc1\n\ts_waitcnt vmcnt(0)" : "=v"(v) : "v"(vo), "s"(sH) : "memory")
#define HST32(off, v) do { __builtin_amdgcn_raw_buffer_store_b32((unsigned)(v), sH, off, 0, 16); asm volatile("" ::: "memory"); } while (0)
#define HST64(off, lo_, hi2_) do { __builtin_amdgcn_raw_buffer_store_b64((u32x2){(unsigned)(lo_), (unsigned)(hi2_)}, sH, off, 0, 16); asm volatile("" ::: "memory"); } while (0)
#define REPLY(q_) do { _Pragma("unroll") for (int i = 0; i < 4; ++i) HST64(WS_HR + l16 * 2 + i * 8, (q_)[i], tag); } while (0)
#define LANE0(off) (l16 == 0 ? (unsigned)(off) : OUT)
#define ALLANS(a_) __all((a_)[0] == tag && (a_)[1] == tag && (a_)[2] == tag && (a_)[3] == tag)
#define ATX(cond, ...) do { int t_ = t; asm volatile("" : "+s"(t_)); if (cond) { unsigned l16 = l128; asm volatile("" : "+v"(l16)); l16 >>= 3; __VA_ARGS__ } } while (0)
#define AT(v, ...) ATX(t_ == (v), __VA_ARGS__)
#define AT2(v, ...) ATX((unsigned)(t_ - (v)) <= 2u, __VA_ARGS__)
        unsigned ask = 0, tag = 0, done = 0;
        const int boss = bid == 0 ? 0 : 64;
        BAR();
        HDMA(ldsb + K1_HS + 1024, l128 >> 3, sH, WS_HR + (bid >> 7) * 1024);
        HDMA(ldsb + K1_HS, l128 >> 3, sH, bid == 0 ? WS_HA : OUT);
        PF_W(1);
        BAR();
#pragma nounroll
        for (int t = 0; t < 16; t += 2) {
            asm volatile("" : "+s"(t));
            BAR(); SEC1(0, , );
            AT(10,
                HDMA(mSink, l128, sI, codeM); HDMA(mSink, l128, sI, codeM + 8192); HDMA(mSink, l128, sI, codeM + 16384););
            AT(12, HDMA(ldsb + K1_HS, l16, sH, WS_HA););
            GO(7, 8, 0);
            BAR(); SEC2(0, , , , );
            AT(0,
                ROLE(bid, idx);
                HST64((pm < 7 ? WS_CARRY + pm * 8192 + j * 256 : OUT) + (l16 < 32 * 16 ? l16 >> 1 : OUT >> 1), ~0u, ~0u);
                HST32(l16 == 4 * 16 ? WS_RDY + (pm * 32 + j) * 4 : (late ? OUT : WS_FLAG2 + tile * 128) + (l16 < 4 * 16 ? l16 * 2 : OUT >> 1), ~0u););
            AT(2,
                const __amdgpu_buffer_rsrc_t sA = SRD(ws + WS_A2, WS_A2_BYTES), sO = SRD(out, SEQ * DM * 4);
                const unsigned vo = (l16 >> 4) * A2S, vo2 = l16 << 13;
                HDMA(mSink, vo, sA, A2S - 128); HDMA(mSink, vo, sA, 65 * A2S - 128); HDMA(mSink, vo2, sO, 0u););
            AT2(4 + boss,
                WAIT_V(2);
                const u32x4 q = *(LAS u32x4*)(lds + K1_HS + l16), qp = *(LAS u32x4*)(lds + K1_GTAB + l16);
                if (t_ == 4 || __any(q[0] != qp[0] || q[1] != qp[1] || q[2] != qp[2] || q[3] != qp[3])) REPLY(q);
                *(LAS u32x4*)(lds + K1_GTAB + l16) = q;);
            AT2(6 + (int)done * 64, HDMA(ldsb + K1_HS + 1024, l16, sH, WS_HR + (bid >> 7) * 1024););
            GO(1, 2, 6);
            BAR(); SEC3(0, , PF_W(t + 2), PF_D(t - PF_D0), );
            GO(3, 4, 5);
            BAR(); SEC1(1, , );
            AT2(2 + boss, HDMA(ldsb + K1_HS, l16, sH, WS_HQ););
            AT(14, WAIT_V(0););
            GO(7, 8, 0);
            BAR(); SEC2(1, , , , );
            AT(0,
                WAIT_V(5);
                ask = RFL(*(LAS unsigned*)(lds + K1_HS + 1024 + (bid & 127) * 8)) + 1u;
                const u32x4 old = *(LAS u32x4*)(lds + K1_HS + l16);
                HST32(LANE0(WS_HQ + bid * 4), ask);
                tag = RFL(old[0]) + 1u;
                while (__any(old[0] == tag || old[1] == tag || old[2] == tag || old[3] == tag)) ++tag;);
            AT2(6 + (int)done * 64,
                WAIT_V(2);
                const u32x2 r = *(LAS u32x2*)(lds + K1_HS + 1024 + (bid & 127) * 8);
                if (RFL(r[0]) == ask) { tag = RFL(r[1]); done = 1u; HST32(LANE0(WS_HA + bid * 4), tag); });
            AT(12,
                WAIT_V(2);
                u32x4 ans = *(LAS u32x4*)(lds + K1_HS + l16);
                if (!(done && ALLANS(ans))) {
                    unsigned spins = 0;
                    _Pragma("nounroll") do {
                        if (bid == 0) { u32x4 q; HLD("buffer_load_dwordx4", q, WS_HQ + l16); REPLY(q); }
                        if (!done) {
                            u32x2 r; HLD("buffer_load_dwordx2", r, WS_HR + bid * 8);
                            if (RFL(r[0]) == ask) { tag = RFL(r[1]); done = 1u; HST32(LANE0(WS_HA + bid * 4), tag); }
                        }
                        HLD("buffer_load_dwordx4", ans, WS_HA + l16);
                    } while (!(done && ALLANS(ans)) && ++spins < (1u << 18));
                }
                HDMA(ldsb + K1_GTAB, l16, sH, WS_G + pm * 1024););
            GO(1, 2, 6);
            BAR(); SEC3(1, , PF_W(t + 3), PF_D(t + 1 - PF_D0), );
            GO(3, 4, 5);
        }
    }
    asm volatile(".Lk1_mid:\n\ts_nop 15" ::: "memory"); SCHED();
    if (wc == 0) BAR();
    RELANE();
    kptr ka = (kptr)__builtin_amdgcn_kernarg_segment_ptr(); asm volatile("" : "+s"(ka));
#define KARG(T, i) ((T)ka[i])
    char* const ws2 = KARG(char*, 13);

    LAS char* ctab = lds + K1_CTAB + (wc * 16 + er) * 96;
    LAS char* etab = lds + K1_ETAB + (wc * 16 + er) * 144;
    const f32x4 cst = *(LAS f32x4*)ctab;
    const f32x2 a1 = {cst[0], cst[1]}, cf = {cst[2], cst[3]};
    u32x4 gt[4];
#pragma unroll
    for (int m = 0; m < 4; ++m) gt[m] = *(LAS u32x4*)(lds + K1_GTAB + wr * 256 + eq * 64 + m * 16);
    const __amdgpu_buffer_rsrc_t sA2 = SRD(ws2 + WS_A2, WS_A2_BYTES);
#define ST16(voff, v) __builtin_amdgcn_raw_buffer_store_b128(v, sA2, voff, 0, 16)
#define PK8(v) (u32x4){pk2(v[0], v[1]), pk2(v[2], v[3]), pk2(v[4], v[5]), pk2(v[6], v[7])}
    const int mblk = pm * 2 + (wr >> 1);
#define IMG_Q(r, q) (unsigned)(((q) >> 1) * 256 + ((q) & 1) * 16 + ((r) & 3) * 32 + mblk * 16384 + (wr & 1) * 1024)
#define IMG_T(r, q) (IMG_Q(r, q) + (unsigned)((((r) >> 2) & 1) * 2048 + ((r) >> 3) * 128 + (j >> 2) * A2S + (j & 3) * 4096))
#define IMG_M(r, q) (IMG_Q(r, q) + (unsigned)((((r) >> 2) & 1) * 2048 + ((r) >> 3) * 512 + (24 + (j >> 2)) * A2S + (j & 3) * 4096))
#define IMG_S(r, q) (IMG_Q(r, q) + (unsigned)((((r) >> 2) & 1) * 4096 + ((r) >> 3) * 2048 + (32 + (j >> 1)) * A2S + (j & 1) * 8192))
    const unsigned lo = IMG_T(er, eq);
#define GLU_HALF(n, hh, o) do { float v[8]; \
        _Pragma("unroll") for (int e = 0; e < 8; ++e) { const int m = 2 * (hh) + (e >> 2), q = e & 3; v[e] = relu(acc[m][n][q]) * (acc[m][3 + (n)][q] * bfhi(gt[m][q])); } \
        o = PK8(v); } while (0)
#define GLU_TILE(n) do { u32x4 o_; GLU_HALF(n, 0, o_); ST16(lo + (wc * 48 + 8 * (n)) * A2S, o_); GLU_HALF(n, 1, o_); ST16(lo + (wc * 48 + 8 * (n)) * A2S + 512, o_); } while (0)
    GLU_TILE(0);
    GLU_TILE(1);
    asm volatile("" ::: "memory");
    {
        f32x2 h = {acc[0][7][0], acc[0][8][0]};
#pragma unroll
        for (int e = 1; e < 16; ++e) h = cfma(a1, h, (f32x2){acc[e >> 2][7][e & 3], acc[e >> 2][8][e & 3]});
        *(LAS f32x2*)(etab + (wr * 4 + eq) * 8) = h;
    }
    WAIT_V(0); WAIT_L0();
    BAR();
    gu32* rdy = (gu32*)(ws2 + WS_RDY);
    if (tz == 192) __hip_atomic_store(rdy + pm * 32 + j, 1u, RLX_AGENT);
    gu64* carry = (gu64*)(ws2 + WS_CARRY);
#define CHAN(c, r) (32 * j + 8 * (c) + 4 * ((r) >> 3) + 16 * (((r) >> 2) & 1) + ((r) & 3))
    if (wid < 2 && pm < 7) {
        const f32x2 a16 = *(LAS f32x2*)(lds + K1_CTAB + (wid * 16 + er) * 96 + 24);
        f32x4 E[8];
#pragma unroll
        for (int i = 0; i < 8; ++i) E[i] = *(LAS f32x4*)(lds + K1_ETAB + (wid * 16 + er) * 144 + i * 16);
        f32x2 run = {E[0][0], E[0][1]};
#pragma unroll
        for (int i = 1; i < 16; ++i) run = cfma(a16, run, (f32x2){E[i >> 1][(i & 1) * 2], E[i >> 1][(i & 1) * 2 + 1]});
        unsigned ux = __float_as_uint(run.x), uy = __float_as_uint(run.y);
        if (ux == 0xFFFFFFFFu) ux = 0x7FC00000u;
        if (eq == 0) __hip_atomic_store(carry + pm * 1024 + CHAN(wid, er), ((unsigned long long)uy << 32) | ux, RLX_AGENT);
    }
    GLU_TILE(2);
    {
        float v[8];
        const bool lowh = er < 8;
#pragma unroll
        for (int e = 0; e < 8; ++e) {
            const int m = e >> 2, q = e & 3;
            const float v0 = lowh ? relu(acc[m][6][q]) : acc[m][6][q] * bfhi(gt[m][q]);
            const float v1 = lowh ? relu(acc[2 + m][6][q]) : acc[2 + m][6][q] * bfhi(gt[2 + m][q]);
            const float rcv = __int_as_float(__builtin_amdgcn_mov_dpp(__float_as_int(lowh ? v1 : v0), 0x128, 0xf, 0xf, true));
            v[e] = (lowh ? v0 : v1) * rcv;
        }
        ST16(IMG_M(er, eq) + wc * 128, PK8(v));
    }
    LAS f32x4* park = (LAS f32x4*)(lds + 3 * 32768 + wr * 8192 + (tz & 63) * 16);
    if (wc == 1) {
#pragma unroll
        for (int m = 0; m < 4; ++m) { park[m * 128] = acc[m][7]; park[m * 128 + 64] = acc[m][8]; }
    }
    WAIT_L0();
    BAR();

    ROLE(bid, idx);
    const int n0 = nblk * 128;
    const int rs0 = late ? 48 % 5 : 40 % 5;

    if (wc == 1) {
        int ty = threadIdx.x; asm volatile("" : "+v"(ty) :: "memory");
        const int ln = ty & 63, e3 = ln >> 3;
#define LEVEL(lvl) do { if (ty == 256) __hip_atomic_store(rdy + pm * 32 + j, (unsigned)(lvl), RLX_AGENT); } while (0)
        const __amdgpu_buffer_rsrc_t sC = SRD(late ? KARG(const u16*, 10) : KARG(const u16*, 9), DM * NP * 2), sW = SRD(KARG(const u16*, 11), FF * DM * 2);
        const unsigned voffA = (unsigned)(ln * 16);
        const unsigned voffC = (unsigned)(e3 * 2048 + (((ln & 7) ^ (e3 & 6)) * 16));
        const unsigned voffD = (unsigned)(((e3 >> 2) * 8 + (e3 & 3)) * 2048 + ((((ln >> 1) & 3) ^ (e3 >> 1)) * 32) + (ln & 1) * 16);
        const unsigned soA = (unsigned)((late ? 0 : 48) * A2S + mb2 * 16384 + wr * 4096);
        const unsigned soC = (unsigned)((n0 + 32 * wr) * 2048);
        const unsigned soD = (unsigned)(((late ? 0 : 48) + 112 * wr) * 2048 + n0 * 2);
        const unsigned mB = ldsb + wr * 4096;
#define A4(img, sl) dma4c(mB + (sl), voffA, sA2, soA + (img) * A2S)
#define D4(row, sl) do { const unsigned b_ = soD + (row) * 2048; \
            asm volatile("s_mov_b32 m0, %0\n\ts_nop 0\n\tbuffer_load_dwordx4 %1, %2, %3 offen lds\n\tbuffer_load_dwordx4 %1, %2, %4 offen offset:2048 lds\n\t" \
                         "buffer_load_dwordx4 %1, %2, %5 offen offset:3072 lds\n\ts_mov_b32 m0, %6\n\ts_nop 0\n\tbuffer_load_dwordx4 %1, %2, %3 offen offset:128 lds" \
                         ::"s"(mB + (sl) + 16384), "v"(voffD), "s"(sW), "s"(b_), "s"(b_ + 8192 - 2048), "s"(b_ + 8192 + 128 - 3072), "s"(mB + (sl) + 16384 + 1024 - 128) : "m0", "memory"); } while (0)
#define C4(srd, p, sl) do { const unsigned b_ = soC + (p) * 128; dma4(mB + (sl) + 16384, voffC, srd, b_, b_ + 16384 - 1024, b_ + 32768 - 2048, b_ + 49152 - 3072); } while (0)
#define POLL(v, p) asm volatile("global_load_dword %0, %1, off sc1\n\ts_waitcnt vmcnt(0)" : "=v"(v) : "v"(p) : "memory")
        gu32* rl = rdy + (mb2 >> 1) * 32 + (ln & 31);
#define XP(k_) (((((k_) & 3) << 1) | (((k_) >> 2) & 1)) ^ (xc & 1))
        unsigned fv;
        D4(448 * XP(0), 0); D4(448 * XP(1), 32768); D4(448 * XP(2), 2 * 32768);
        POLL(fv, rl);
        {
            const int other = ((ln >> 2) ^ xc) & 1;
            unsigned spins = 0;
            while (!__all((int)fv >= 1 || other) && ++spins < (1u << 18)) { __builtin_amdgcn_s_sleep(4); POLL(fv, rl); }
        }
        A4(XP(0), 0); A4(XP(1), 32768); A4(XP(2), 2 * 32768);
        WAIT_V(8); BAR();
        int k = 3, sl = 3 * 32768;
#define NEXT_SLOT() sl = sl == 4 * 32768 ? 0 : sl + 32768
#define STAGE_W() do { const int q_ = k < 16 ? (k & 3) | ((k & 4) << 1) | ((k & 8) >> 1) : k, x_ = XP(q_), row_ = 448 * x_ + ((q_ >> 3) == 3 ? 96 : 16 * (q_ >> 3)); \
            A4((q_ & ~7) + x_, sl); D4(row_, sl); ++k; NEXT_SLOT(); } while (0)
#define LSYNC() do { WAIT_V(16); BAR(); } while (0)
        STAGE_W(); WAIT_V(12); BAR();
        const int nw = late ? 32 : 24;
#pragma nounroll
        while (k < nw) {
            asm volatile("s_bitcmp1_b32 0x2040, %2\n\ts_cbranch_scc0 1f\n\tglobal_load_dword %0, %1, off sc1\n1:" : "+v"(fv) : "v"(rl), "s"(k) : "scc", "memory");
            if ((0x10500u >> k) & 1) {
                if (k == 10) LEVEL(2);
                else if (!__all((int)fv >= (k >> 3))) {
                    unsigned spins = 0, w_;
                    do { __builtin_amdgcn_s_sleep(1); POLL(w_, rl); } while (!__all((int)w_ >= (k >> 3)) && ++spins < (1u << 18));
                }
            }
            STAGE_W(); LSYNC();
        }
#pragma nounroll
        for (int p = 0; p < 16; ++p) { A4(k, sl); C4(sC, p, sl); ++k; NEXT_SLOT(); LSYNC(); }
        const int tb = mb2 * 128 + (wr >> 1) * 64 + (wr & 1) * 8 + (ln >> 5) * 16, dcol = n0 + (ln & 31) * 4;
#define ROW(i) (tb + ((i) >> 3) * 32 + ((i) & 7))
        gu32* flag = (gu32*)(ws2 + WS_FLAG2 + tile * 128 + wr * 32);
        const __amdgpu_buffer_rsrc_t sP = SRD(KARG(float*, 14), SEQ * DM * 4);
#define POFF(i) ((ROW(i) * DM + dcol) * 4)
        const int wn1 = (ln >> 4) & 1, s0 = rs0 + wn1, s1 = rs0 + 2 + wn1;
        const int inp = wr * 8192 + ((ln >> 2) & 3) * 1024 + (ln & 3) * 256, sw = (ln & 15) ^ ((ln >> 5) * 8);
        LAS char* q0 = lds + (s0 >= 5 ? s0 - 5 : s0) * 32768 + inp;
        LAS char* q1 = lds + (s1 >= 5 ? s1 - 5 : s1) * 32768 + inp;
#define SUMS(i) (*(LAS f32x4*)(q0 + ((i) >> 3) * 4096 + ((sw ^ ((i) & 7)) * 16)) + *(LAS f32x4*)(q1 + ((i) >> 3) * 4096 + ((sw ^ ((i) & 7)) * 16)))
        f32x4 ex[16];
        if (late) {
            WAIT_V(8); BAR();
            POLL(fv, flag); BAR();
            for (unsigned spins = 0; !__all((int)fv >= 1) && spins < (1u << 18); ++spins) { __builtin_amdgcn_s_sleep(1); POLL(fv, flag); }
            u32x4 pq[8];
#pragma unroll
            for (int i = 0; i < 8; ++i) pq[i] = __builtin_amdgcn_raw_buffer_load_b128(sP, POFF(2 * i), 0, 16);
#pragma unroll
            for (int i = 0; i < 16; ++i) {
                ex[i] = (f32x4){bflo(pq[i >> 1][(i & 1) * 2]), bfhi(pq[i >> 1][(i & 1) * 2]), bflo(pq[i >> 1][(i & 1) * 2 + 1]), bfhi(pq[i >> 1][(i & 1) * 2 + 1])};
                asm volatile("" : "+v"(ex[i]));
            }
            BAR();
#pragma unroll
            for (int i = 0; i < 16; ++i) __builtin_amdgcn_raw_buffer_store_b128(__builtin_bit_cast(u32x4, ex[i] + SUMS(i)), sP, POFF(i), 0, 16);
        } else {
            const __amdgpu_buffer_rsrc_t sXk = SRD(KARG(const u16*, 0), SEQ * DM * 2), sDk = SRD(KARG(const u16*, 12), DM * 2), sGk = SRD(ws2 + WS_G, 8192);
            WAIT_V(8); BAR();
            WAIT_V(0); BAR();
            const u32x2 dk = __builtin_amdgcn_raw_buffer_load_b64(sDk, dcol * 2, 0, 0); u32x2 xv[16]; u32x4 gwd[4];
#pragma unroll
            for (int i = 0; i < 4; ++i) gwd[i] = __builtin_amdgcn_raw_buffer_load_b128(sGk, ROW(4 * i) * 4, 0, 16);
#pragma unroll
            for (int i = 0; i < 16; ++i) xv[i] = __builtin_amdgcn_raw_buffer_load_b64(sXk, (tb * DM + dcol) * 2 + (i & 1) * 2048, (((i) >> 3) * 32 + ((i) & 6)) * 2048, 0);
            const f32x4 dv = {bflo(dk[0]), bfhi(dk[0]), bflo(dk[1]), bfhi(dk[1])};
#pragma unroll
            for (int i = 0; i < 16; ++i) {
                ex[i] = (f32x4){bflo(xv[i][0]), bfhi(xv[i][0]), bflo(xv[i][1]), bfhi(xv[i][1])} * dv * bflo(gwd[i >> 2][i & 3]);
                asm volatile("" : "+v"(ex[i]));
            }
            BAR();
#pragma unroll
            for (int i = 0; i < 16; i += 2) {
                const f32x4 r0 = ex[i] + SUMS(i), r1 = ex[i + 1] + SUMS(i + 1);
                __builtin_amdgcn_raw_buffer_store_b128((u32x4){pk2(r0[0], r0[1]), pk2(r0[2], r0[3]), pk2(r1[0], r1[1]), pk2(r1[2], r1[3])}, sP, POFF(i), 0, 16);
            }
            WAIT_V(0);
            if (ln == 0) __hip_atomic_store(flag, 1u, RLX_AGENT);
        }
        asm volatile(".Lk1_end_a:");
        return;
    }

    {
        unsigned long long gq[2][7];
#pragma unroll
        for (int c = 0; c < 2; ++c)
#pragma unroll
            for (int q = 0; q < 7; ++q) gq[c][q] = __hip_atomic_load(carry + (q + pm >= 7 ? q + pm - 7 : 0) * 1024 + CHAN(c, er), RLX_AGENT);
        f32x2 h[2];
        {
            f32x2 a16[2], run[2]; f32x4 E[2][6], F0[2]; f32x2 F1[2];
#pragma unroll
            for (int c = 0; c < 2; ++c) {
                a16[c] = *(LAS f32x2*)(ctab + c * (16 * 96) + 24);
#pragma unroll
                for (int i = 0; i < 6; ++i) E[c][i] = *(LAS f32x4*)(etab + c * (16 * 144) + i * 16);
                F0[c] = *(LAS f32x4*)(etab + c * (16 * 144) + wr * 32); F1[c] = *(LAS f32x2*)(etab + c * (16 * 144) + wr * 32 + 16);
                run[c] = (f32x2){0.f, 0.f};
            }
#pragma unroll
            for (int w = 0; w < 3; ++w)
                if (wr > w) {
#pragma unroll
                    for (int i = 0; i < 4; ++i)
#pragma unroll
                        for (int c = 0; c < 2; ++c) run[c] = cfma(a16[c], run[c], (f32x2){E[c][2 * w + (i >> 1)][(i & 1) * 2], E[c][2 * w + (i >> 1)][(i & 1) * 2 + 1]});
                }
#pragma unroll
            for (int c = 0; c < 2; ++c) {
                const f32x2 r1 = cfma(a16[c], run[c], (f32x2){F0[c][0], F0[c][1]}), r2 = cfma(a16[c], r1, (f32x2){F0[c][2], F0[c][3]}), r3 = cfma(a16[c], r2, F1[c]);
                h[c] = sel4(eq, run[c], r1, r2, r3);
            }
        }
        f32x4 ur[4], ui[4];
#pragma unroll
        for (int m = 0; m < 4; ++m) { ur[m] = park[m * 128]; ui[m] = park[m * 128 + 64]; }
        const f32x4 cs1 = *(LAS f32x4*)(ctab + 16 * 96);

        f32x2 a256[2], aq[2];
#pragma unroll
        for (int c = 0; c < 2; ++c) {
            LAS char* ct = ctab + c * (16 * 96);
            a256[c] = *(LAS f32x2*)(ct + 80); aq[c] = cmul(*(LAS f32x2*)(ct + 16 + eq * 8), *(LAS f32x2*)(ct + 48 + wr * 8));
        }
        unsigned mx = 0;
#pragma unroll
        for (int q = 0; q < 7; ++q) mx = max(mx, max((unsigned)gq[0][q], (unsigned)gq[1][q]));
        if (pm > 0 && __any(mx == 0xFFFFFFFFu)) {
            unsigned spins = 0;
            do {
                mx = 0;
#pragma unroll
                for (int c = 0; c < 2; ++c)
#pragma unroll
                    for (int q = 0; q < 7; ++q) {
                        gq[c][q] = __hip_atomic_load(carry + (q + pm >= 7 ? q + pm - 7 : 0) * 1024 + CHAN(c, er), RLX_AGENT);
                        mx = max(mx, (unsigned)gq[c][q]);
                    }
            } while (__any(mx == 0xFFFFFFFFu) && ++spins < (1u << 18));
        }
        f32x2 cg[2] = {{0.f, 0.f}, {0.f, 0.f}};
#pragma unroll
        for (int q = 0; q < 7; ++q) {
            const unsigned keep = q + pm >= 7 ? 0xFFFFFFFFu : 0u;
#pragma unroll
            for (int c = 0; c < 2; ++c) cg[c] = cfma(a256[c], cg[c], (f32x2){__uint_as_float((unsigned)gq[c][q] & keep), __uint_as_float((unsigned)(gq[c][q] >> 32) & keep)});
        }
        h[0] = cfma(aq[0], cg[0], h[0]); h[1] = cfma(aq[1], cg[1], h[1]);
        const unsigned ls = IMG_S(er, eq);
        const f32x2 b1 = {cs1[0], cs1[1]}, bf = {cs1[2], cs1[3]};
#pragma unroll
        for (int hh = 0; hh < 2; ++hh) {
            float vr[8], vi[8], wr8[8], wi8[8];
#pragma unroll
            for (int e = 0; e < 8; ++e) {
                const int m = 2 * hh + (e >> 2), q = e & 3;
                h[0] = cfma(a1, h[0], (f32x2){acc[m][7][q], acc[m][8][q]});
                h[1] = cfma(b1, h[1], (f32x2){ur[m][q], ui[m][q]});
                const float g2 = bflo(gt[m][q]);
                const f32x2 s0 = cmul(cf, h[0]), s1 = cmul(bf, h[1]);
                vr[e] = s0.x * g2; vi[e] = -s0.y * g2; wr8[e] = s1.x * g2; wi8[e] = -s1.y * g2;
            }
            ST16(ls + 40 * A2S + hh * 512, PK8(vr)); ST16(ls + 40 * A2S + hh * 512 + 128, PK8(wr8));
            ST16(ls + hh * 512, PK8(vi)); ST16(ls + hh * 512 + 128, PK8(wi8));
        }
    }
    int ty = threadIdx.x; asm volatile("" : "+v"(ty) :: "memory");
    const int ln = ty & 63, fr2 = ty & 15, fq2 = (ty >> 4) & 3;
    const int wn = wid & 1, kk = wid >> 1;
    LAS char* rH = lds + kk * 8192 + (ln >> 5) * 4096 + (ln & 31) * 8;
    LAS char* rC = lds + 16384 + (wn * 64 + fr2) * 128 + (((4 * kk + fq2) ^ (fr2 & 6)) * 16);
    LAS char* rWb = lds + 16384 + kk * 8192 + (ln >> 5) * 4096 + wn * 1024 + ((ln >> 2) & 7) * 128 + (ln & 3) * 8;
    const int rsw = (ln >> 3) & 3;
    f32x4 ac2[4][8];
    s16x4 HX[8][2], HY[8][2], WX[4][2], WY[4][2];
    int sl = 0;

#define RD1(RD, i, H, W) do { \
        if ((i) < 8) { \
            if (RD == 1) W[(i) >> 1][(i) & 1] = tr4(rWb + sl + ((((i) >> 1) ^ rsw) * 32) + ((i) & 1) * 2048); \
            else if (((i) & 1) == 0) { const s16x8 v_ = *(LAS s16x8*)(rC + sl + ((i) >> 1) * 2048); \
                W[(i) >> 1][0] = __builtin_shufflevector(v_, v_, 0, 1, 2, 3); W[(i) >> 1][1] = __builtin_shufflevector(v_, v_, 4, 5, 6, 7); } \
        } else H[((i) - 8) >> 1][(i) & 1] = tr4(rH + sl + (((i) - 8) >> 1) * 256 + ((i) & 1) * 2048); \
    } while (0)
#define MM(i, HC, WC) mfma_acc(ac2[(i) & 3][(i) >> 2], cat8(WC[(i) & 3][0], WC[(i) & 3][1]), cat8(HC[(i) >> 2][0], HC[(i) >> 2][1]))
#define STEP(RD, HC, WC, HN, WN) do { \
        _Pragma("unroll") for (int i = 0; i < 32; ++i) { MM(i, HC, WC); if (i < 24) RD1(RD, i, HN, WN); } \
        sl = sl == 4 * 32768 ? 0 : sl + 32768; LGKM0(); \
    } while (0)

    LGKM0();
    BAR();
#pragma unroll
    for (int i = 0; i < 24; ++i) RD1(1, i, HX, WX);
    sl = 32768; LGKM0();
    BAR();
#pragma unroll
    for (int i = 0; i < 32; ++i) {
        asm volatile("v_mfma_f32_16x16x32_bf16 %0, %1, %2, 0" : "=&v"(ac2[i & 3][i >> 2]) : "v"(cat8(WX[i & 3][0], WX[i & 3][1])), "v"(cat8(HX[i >> 2][0], HX[i >> 2][1])) : "memory");
        if (i < 24) RD1(1, i, HY, WY);
    }
    sl = 2 * 32768; LGKM0();
    BAR(); STEP(1, HY, WY, HX, WX);
    const int nf = late ? 15 : 11;
#pragma nounroll
    for (int i = 1; i < nf; ++i) {
        if (i == 3) WAIT_V(0);
        BAR(); STEP(1, HX, WX, HY, WY); BAR(); STEP(1, HY, WY, HX, WX);
    }
    BAR(); STEP(1, HX, WX, HY, WY); BAR(); STEP(0, HY, WY, HX, WX);
#pragma nounroll
    for (int i = 0; i < 7; ++i) { BAR(); STEP(0, HX, WX, HY, WY); BAR(); STEP(0, HY, WY, HX, WX); }
    BAR(); STEP(0, HX, WX, HY, WY);
    {
        const int sw = rs0 + wid;
        LAS char* dmp = lds + (sw >= 5 ? sw - 5 : sw) * 32768 + fq2 * 256;
#define DUMP(i) *(LAS f32x4*)(dmp + (i) * 1024 + ((fr2 ^ (4 * ((i) & 3) + fq2)) * 16)) = ac2[(i) & 3][(i) >> 2]
#pragma unroll
        for (int i = 0; i < 32; ++i) { MM(i, HY, WY); if (i >= 12) DUMP(i - 12); }
        asm volatile("s_nop 15" ::: "memory");
#pragma unroll
        for (int i = 20; i < 32; ++i) DUMP(i);
    }
    WAIT_L0();
    BAR();
    asm volatile(".Lk1_end_b:");
}

extern "C" void kernel_launch(void* const* d_in, const int* in_sizes, int n_in, void* d_out, int out_size, void* d_ws, size_t ws_size,
                              hipStream_t stream) {
    const u16* x = (const u16*)d_in[0];
    const u16* lre = (const u16*)d_in[1];
    const u16* lim = (const u16*)d_in[2];
    const u16* Bre = (const u16*)d_in[3];
    const u16* Bim = (const u16*)d_in[4];
    const u16* Cre = (const u16*)d_in[5];
    const u16* Cim = (const u16*)d_in[6];
    const u16* logdt = (const u16*)d_in[7];
    const u16* dskip = (const u16*)d_in[8];
    const u16* Wg = (const u16*)d_in[10];
    const u16* Wu = (const u16*)d_in[11];
    const u16* Wd = (const u16*)d_in[12];
    const u16* gw = (const u16*)d_in[13];
    char* ws = (char*)d_ws;
    (void)hipFuncSetAttribute((const void*)k1_fused, hipFuncAttributeMaxDynamicSharedMemorySize, K2_LDS);
    k1_fused<<<256, 512, K2_LDS, stream>>>(x, Wg, Wu, Bre, Bim, lre, lim, logdt, gw, Cre, Cim, Wd, dskip, ws, (float*)d_out);
}
```
